```python
import torch

def _softmax(x, axis=-1):
    return torch.softmax(x, dim=axis)
def _gelu(x):
    return torch.nn.functional.gelu(x, approximate="tanh")
def _silu(x):
    return torch.nn.functional.silu(x)
def _sigmoid(x):
    return torch.sigmoid(x)
def _layernorm(x, axis=-1, eps=1e-5):
    m = x.mean(dim=axis, keepdim=True)
    v = x.var(dim=axis, keepdim=True, unbiased=False)
    return (x - m) / torch.sqrt(v + eps)
def _scatter_add(tgt, idx, src, axis=0):
    return tgt.clone().index_add_(axis, idx, src)
def _segment_sum(src, seg, n):
    out = torch.zeros((n,) + src.shape[1:], dtype=src.dtype)
    return out.index_add_(0, seg, src)
def _pad(x, pw):
    flat = tuple(v for pair in reversed(pw) for v in pair)
    return torch.nn.functional.pad(x, flat)

kernel_name = 'concat_heads__grouped_kv_mqa__ssm_conv_s5__prompt'

def setup_inputs():

    _w = (lambda a: (lambda t: t.to(torch.bfloat16) if t.is_floating_point() else t)(torch.as_tensor(a)))
    def _A_setup():
        B, S, D, H = 1, 2048, 512, 32
        Dh = D // H
        return {
            "x": _w((lambda *s: torch.randn(*s, dtype=torch.bfloat16) * 0.05)(B, S, D)),
            "wq": _w((lambda *s: torch.randn(*s, dtype=torch.bfloat16) * 0.8)(D, H * Dh)),
            "wk": _w((lambda *s: torch.randn(*s, dtype=torch.bfloat16) * 0.8)(D, Dh)),
            "wv": _w((lambda *s: torch.randn(*s, dtype=torch.bfloat16) * 0.05)(D, Dh)),
            "wo": _w((lambda *s: torch.randn(*s, dtype=torch.bfloat16) * 0.05)(H * Dh, D)),
        }
    _A_in = _A_setup()

    def _B_setup():
        B, S, D = 1, 2048, 512
        P = D
        Lambda_re = -0.5 * (lambda _x: torch.exp(torch.as_tensor(_x)))((lambda *s: torch.randn(*s, dtype=torch.bfloat16) * 0.2)(P))
        Lambda_im = (lambda *s: torch.randn(*s, dtype=torch.bfloat16) * 0.4)(P) * 1.0
        B_re = (lambda *s: torch.randn(*s, dtype=torch.bfloat16) * 0.2)(P, D) * (D ** -0.5)
        B_im = (lambda *s: torch.randn(*s, dtype=torch.bfloat16) * 0.2)(P, D) * (D ** -0.5)
        C_re = (lambda *s: torch.randn(*s, dtype=torch.bfloat16) * 0.2)(D, P) * (P ** -0.5)
        C_im = (lambda *s: torch.randn(*s, dtype=torch.bfloat16) * 0.2)(D, P) * (P ** -0.5)
        log_dt = (lambda *s: torch.randn(*s, dtype=torch.bfloat16) * 0.2)(P) * 0.3 - 2.0
        Dskip = (lambda *s: torch.randn(*s, dtype=torch.bfloat16) * 0.05)(D)
        return {
            "x": _w((lambda *s: torch.randn(*s, dtype=torch.bfloat16) * 0.05)(B, S, D)),
            "Lambda_re": _w(Lambda_re),
            "Lambda_im": _w(Lambda_im),
            "B_re": _w(B_re),
            "B_im": _w(B_im),
            "C_re": _w(C_re),
            "C_im": _w(C_im),
            "log_dt": _w(log_dt),
            "Dskip": _w(Dskip),
        }
    _B_in = _B_setup()

    return {
        "A_x": _A_in["x"],
        "A_wq": _A_in["wq"],
        "A_wk": _A_in["wk"],
        "A_wv": _A_in["wv"],
        "A_wo": _A_in["wo"],
        "B_x": _B_in["x"],
        "B_Lambda_re": _B_in["Lambda_re"],
        "B_Lambda_im": _B_in["Lambda_im"],
        "B_B_re": _B_in["B_re"],
        "B_B_im": _B_in["B_im"],
        "B_C_re": _B_in["C_re"],
        "B_C_im": _B_in["C_im"],
        "B_log_dt": _B_in["log_dt"],
        "B_Dskip": _B_in["Dskip"],
    }

def reference(A_x, A_wq, A_wk, A_wv, A_wo, B_x, B_Lambda_re, B_Lambda_im, B_B_re, B_B_im, B_C_re, B_C_im, B_log_dt, B_Dskip):
    def _A_ref(x, wq, wk, wv, wo):
        B, S, D = x.shape
        H = 32
        Dh = D // H
        q = (lambda x, *d: x.permute(*d))((x @ wq).reshape(B, S, H, Dh), 0, 2, 1, 3)
        k = x @ wk
        v = x @ wv
        scores = torch.einsum("bhqd,bkd->bhqk", q, k) * (Dh ** -0.5)
        p = _softmax(scores, axis=-1)
        o = torch.einsum("bhqk,bkd->bhqd", p, v)
        return (lambda x, *d: x.permute(*d))(o, 0, 2, 1, 3).reshape(B, S, H * Dh) @ wo

    def _B_ref(x, Lambda_re, Lambda_im, B_re, B_im, C_re, C_im, log_dt, Dskip):
        Bsz, L, H = x.shape
        Lam = Lambda_re + 1j * Lambda_im
        Bmat = B_re + 1j * B_im
        Cmat = C_re + 1j * C_im
        dt = torch.exp(log_dt)
        Lam_bar = torch.exp(dt * Lam)
        B_bar = ((Lam_bar - 1.0) / Lam)[:, None] * Bmat
        Bu = torch.einsum("bld,pd->blp", x + 0j, B_bar)
        h = torch.zeros_like(Bu[:, 0, :])
        hs = []
        for t in range(L):
            h = Lam_bar[None, :] * h + Bu[:, t, :]
            hs.append(h)
        hseq = torch.stack(hs, 1)
        y = 2.0 * torch.einsum("blp,dp->bld", hseq, Cmat).real + x * Dskip[None, None, :]
        return y

    a = _A_ref(A_x, A_wq, A_wk, A_wv, A_wo)
    b = _B_ref(B_x, B_Lambda_re, B_Lambda_im, B_B_re, B_B_im, B_C_re, B_C_im, B_log_dt, B_Dskip)
    return torch.concatenate([a, b], axis=-1)
```

```cpp
#include <hip/hip_runtime.h>
#include <hip/hip_bf16.h>
typedef unsigned short u16;
__device__ __forceinline__ float bf2f(u16 v){ return __uint_as_float(((unsigned)v)<<16); }
__device__ __forceinline__ float rbf(float f){ unsigned u=__float_as_uint(f); u=(u+0x7fff+((u>>16)&1))&0xffff0000u; return __uint_as_float(u);}
__global__ void gemm_nn(const u16* A,const u16* B,float* C,int M,int N,int K,int ldc,int rnd){
  int n=blockIdx.x*64+threadIdx.x; int m=blockIdx.y;
  if(n>=N) return; float s=0; for(int k=0;k<K;k++) s+=bf2f(A[m*K+k])*bf2f(B[k*N+n]);
  C[m*ldc+n]= rnd? rbf(s):s;
}
__global__ void gemm_nt(const u16* A,const u16* B,float* C,int M,int N,int K,int ldc){
  int n=blockIdx.x*64+threadIdx.x; int m=blockIdx.y;
  if(n>=N) return; float s=0; for(int k=0;k<K;k++) s+=bf2f(A[m*K+k])*bf2f(B[n*K+k]);
  C[m*ldc+n]=s;
}
__global__ void attn(const float* q,const float* k,const float* v,float* o){
  int row=blockIdx.x*64+threadIdx.x;
  float qq[16]; for(int d=0;d<16;d++) qq[d]=q[row*16+d]*0.25f;
  float m=-1e30f,l=0; float acc[16]; for(int d=0;d<16;d++) acc[d]=0;
  for(int j=0;j<2048;j++){ float s=0; for(int d=0;d<16;d++) s+=qq[d]*k[j*16+d];
    float mn=fmaxf(m,s); float a=__expf(m-mn); float p=__expf(s-mn); l=l*a+p; for(int d=0;d<16;d++) acc[d]=acc[d]*a+p*v[j*16+d]; m=mn; }
  for(int d=0;d<16;d++) o[row*16+d]=rbf(acc[d]/l);
}
__global__ void oproj(const float* o,const u16* wo,float* out){
  int n=blockIdx.x*64+threadIdx.x; int m=blockIdx.y; float s=0; for(int k=0;k<512;k++) s+=o[m*512+k]*bf2f(wo[k*512+n]);
  out[m*1024+n]=s;
}
__global__ void scan(const u16* lre,const u16* lim,const u16* ldt,float* ur,float* ui){
  int p=blockIdx.x*64+threadIdx.x;
  float dt=rbf(expf(bf2f(ldt[p]))); float lr=bf2f(lre[p]), li=bf2f(lim[p]);
  float e=expf(dt*lr); float ar=e*cosf(dt*li), ai=e*sinf(dt*li);
  float nr=ar-1.f, ni=ai; float den=lr*lr+li*li; float gr=(nr*lr+ni*li)/den, gi=(ni*lr-nr*li)/den;
  float hr=0,hi=0;
  for(int t=0;t<2048;t++){ float xr=ur[t*512+p], xi=ui[t*512+p]; float br=gr*xr-gi*xi, bi=gr*xi+gi*xr;
    float nhr=ar*hr-ai*hi+br, nhi=ar*hi+ai*hr+bi; hr=nhr; hi=nhi; ur[t*512+p]=hr; ui[t*512+p]=hi; }
}
__global__ void yproj(const float* hr,const float* hi,const u16* cr,const u16* ci,const u16* x,const u16* dsk,float* out){
  int n=blockIdx.x*64+threadIdx.x; int m=blockIdx.y; float s=0;
  for(int k=0;k<512;k++) s+=hr[m*512+k]*bf2f(cr[n*512+k])-hi[m*512+k]*bf2f(ci[n*512+k]);
  out[m*1024+512+n]=2.f*s+rbf(bf2f(x[m*512+n])*bf2f(dsk[n]));
}
extern "C" void kernel_launch(void* const* d_in, const int* in_sizes, int n_in, void* d_out, int out_size, void* d_ws, size_t ws_size, hipStream_t stream) {
  const u16* const* in=(const u16* const*)d_in;
  float* ws=(float*)d_ws; float* q=ws; float* k=q+2048*512; float* v=k+2048*16; float* o=v+2048*16; float* ur=o+2048*512; float* ui=ur+2048*512;
  float* out=(float*)d_out;
  gemm_nn<<<dim3(8,2048),64,0,stream>>>(in[0],in[1],q,2048,512,512,512,1);
  gemm_nn<<<dim3(1,2048),64,0,stream>>>(in[0],in[2],k,2048,16,512,16,1);
  gemm_nn<<<dim3(1,2048),64,0,stream>>>(in[0],in[3],v,2048,16,512,16,1);
  attn<<<1024,64,0,stream>>>(q,k,v,o);
  oproj<<<dim3(8,2048),64,0,stream>>>(o,in[4],out);
  gemm_nt<<<dim3(8,2048),64,0,stream>>>(in[5],in[8],ur,2048,512,512,512);
  gemm_nt<<<dim3(8,2048),64,0,stream>>>(in[5],in[9],ui,2048,512,512,512);
  scan<<<8,64,0,stream>>>(in[6],in[7],in[12],ur,ui);
  yproj<<<dim3(8,2048),64,0,stream>>>(ur,ui,in[10],in[11],in[5],in[13],out);
}
```

```cpp
#include <hip/hip_runtime.h>
typedef unsigned short u16;
typedef unsigned u32;
typedef unsigned long long u64;
typedef float f32x2 __attribute__((ext_vector_type(2)));
typedef float f32x4 __attribute__((ext_vector_type(4)));
typedef float f32x8 __attribute__((ext_vector_type(8)));
typedef float f32x16 __attribute__((ext_vector_type(16)));
typedef __bf16 bf16x2 __attribute__((ext_vector_type(2)));
typedef __bf16 bf16x8 __attribute__((ext_vector_type(8)));
typedef __bf16 bf16x16 __attribute__((ext_vector_type(16)));
typedef short s16x4 __attribute__((ext_vector_type(4)));
typedef u32 u32x2 __attribute__((ext_vector_type(2)));
typedef u32 u32x4 __attribute__((ext_vector_type(4)));
#define LAS __attribute__((address_space(3)))
#define DEVI __device__ __forceinline__

constexpr u32 WS_EPA = 0, WS_EPB = 256;
constexpr u32 WS_KVF = 4096;
constexpr u32 WS_EF = 8192;
constexpr u32 WS_HF = 16384;
constexpr u32 WS_OF = 32768;
constexpr u32 WS_KV = 65536;
constexpr u32 WS_E = 262144;
constexpr u32 WS_O = 524288;
constexpr u32 WS_H = WS_O + 2097152;
constexpr u32 WS_END = WS_H + 4194304;
constexpr u32 WS_DBG = WS_END;
constexpr u32 OOB = 0x80000000u;

struct Args {
  const u16 *xa, *wq, *wk, *wv, *wo, *xb, *lre, *lim, *bre, *bim, *cre, *cim, *ldt, *dsk;
  float* out;
  char* ws;
};

DEVI u32x4 mk_srd(const void* p, u32 bytes) {
  u64 a = (u64)p;
  u32x4 r = {(u32)a, (u32)(a >> 32) & 0xffffu, bytes, 0x20000u};
  return r;
}
DEVI void dma2(u32 m0v, u32 v0, u32 v1, u32x4 srd, u32 soff) {
  asm volatile("s_mov_b32 m0, %0\n\ts_nop 0\n\tbuffer_load_dwordx4 %1, %3, %4 offen lds\n\tbuffer_load_dwordx4 %2, %3, %4 offen offset:1024 lds"
               ::"s"(m0v), "v"(v0), "v"(v1), "s"(srd), "s"(soff) : "memory");
}
DEVI void dma2c(u32 m0v, u32 v0, u32x4 srd, u32 soff) {
  asm volatile("s_mov_b32 m0, %0\n\ts_nop 0\n\tbuffer_load_dwordx4 %1, %2, %3 offen sc1 lds\n\tbuffer_load_dwordx4 %1, %2, %3 offen offset:1024 sc1 lds"
               ::"s"(m0v), "v"(v0), "s"(srd), "s"(soff) : "memory");
}
DEVI void dma4(u32 m0v, u32 v0, u32x4 srd, u32 soff) {
  asm volatile("s_mov_b32 m0, %0\n\ts_nop 0\n\tbuffer_load_dwordx4 %1, %2, %3 offen lds\n\tbuffer_load_dwordx4 %1, %2, %3 offen offset:1024 lds\n\t"
               "buffer_load_dwordx4 %1, %2, %3 offen offset:2048 lds\n\tbuffer_load_dwordx4 %1, %2, %3 offen offset:3072 lds"
               ::"s"(m0v), "v"(v0), "s"(srd), "s"(soff) : "memory");
}
DEVI void dma4c(u32 m0v, u32 v0, u32x4 srd, u32 soff) {
  asm volatile("s_mov_b32 m0, %0\n\ts_nop 0\n\tbuffer_load_dwordx4 %1, %2, %3 offen sc1 lds\n\tbuffer_load_dwordx4 %1, %2, %3 offen offset:1024 sc1 lds\n\t"
               "buffer_load_dwordx4 %1, %2, %3 offen offset:2048 sc1 lds\n\tbuffer_load_dwordx4 %1, %2, %3 offen offset:3072 sc1 lds"
               ::"s"(m0v), "v"(v0), "s"(srd), "s"(soff) : "memory");
}
DEVI void st8c(u32x2 v, u32 voff, u32x4 srd) {
  asm volatile("buffer_store_dwordx2 %0, %1, %2, 0 offen sc1" ::"v"(v), "v"(voff), "s"(srd) : "memory");
}
DEVI void st16c(u32x4 v, u32 voff, u32x4 srd) {
  asm volatile("buffer_store_dwordx4 %0, %1, %2, 0 offen sc1\n\ts_nop 1" ::"v"(v), "v"(voff), "s"(srd) : "memory");
}
#define WAITV(n) asm volatile("s_waitcnt vmcnt(" #n ")" ::: "memory")
#define WAITVL(n) asm volatile("s_waitcnt vmcnt(" #n ") lgkmcnt(0)" ::: "memory")
#define BAR() asm volatile("s_barrier" ::: "memory")
template <int N> DEVI void waitvl_bar() { asm volatile("s_waitcnt vmcnt(%0) lgkmcnt(0)\n\ts_barrier" ::"i"(N) : "memory"); }

template <int SLEEP> DEVI void poll(u32 voff, bool need, u32x2 tag, u32x4 srd) {
  for (u32 it = 0; it < (1u << 18); ++it) {
    u32x2 f;
    asm volatile("buffer_load_dwordx2 %0, %1, %2, 0 offen sc1\n\ts_waitcnt vmcnt(0)" : "=&v"(f) : "v"(voff), "s"(srd) : "memory");
    bool bad = need && (f.x != tag.x || f.y != tag.y);
    if (__builtin_amdgcn_ballot_w64(bad) == 0) break;
    __builtin_amdgcn_s_sleep(SLEEP);
  }
}
DEVI u32 pk2(float a, float b) {
  f32x2 x = {a, b};
  return __builtin_bit_cast(u32, __builtin_convertvector(x, bf16x2));
}
DEVI float bf2f(u32 v) { return __builtin_bit_cast(float, v << 16); }
DEVI bf16x8 ldsr(u32 a) { return *(const LAS bf16x8*)(size_t)a; }
DEVI bf16x8 ldstr(u32 a0, u32 a1) {
  s16x4 x = __builtin_amdgcn_ds_read_tr16_b64_v4i16((LAS s16x4*)(size_t)a0);
  s16x4 y = __builtin_amdgcn_ds_read_tr16_b64_v4i16((LAS s16x4*)(size_t)a1);
  return __builtin_bit_cast(bf16x8, __builtin_shufflevector(x, y, 0, 1, 2, 3, 4, 5, 6, 7));
}
DEVI f32x16 mfma32(bf16x8 a, bf16x8 b, f32x16 c) { return __builtin_amdgcn_mfma_f32_32x32x16_bf16(a, b, c, 0, 0, 0); }
DEVI f32x4 mfma16(bf16x8 a, bf16x8 b, f32x4 c) { return __builtin_amdgcn_mfma_f32_16x16x32_bf16(a, b, c, 0, 0, 0); }

#define STDECL
#define STC(v)
#define ST(i)
#define STFLUSH

#define EX(r) "v_exp_f32 v" #r ", v" #r "\n\t"
#define SM(o, vv, p) "v_smfmac_f32_32x32x32_bf16 " o ", " vv ", " p ", v156\n\t"
#define OA "v[96:111]"
#define OB "v[112:127]"
#define QK(s, q) "v_mfma_f32_32x32x16_bf16 " s ", v[136:139], " q ", 0\n\t"
#define IT(X0, X1, Y0, Y1, KO, V0O, V1O, WT, E1, E2)                                        \
  EX(64) EX(65) SM(OB, Y1, "v[88:95]")                                                      \
  "ds_read_b128 " Y0 ", %[va] offset:" #V0O "\n\tds_read_b128 " Y1 ", %[va] offset:" #V1O "\n\t" \
  EX(66) EX(67) EX(68) QK("v[80:95]", "v[132:135]")                                         \
  "ds_read_b128 v[136:139], %[ka] offset:" #KO "\n\t"                                       \
  EX(69) EX(70) EX(71) EX(72) EX(73) SM(OA, X0, "v[64:71]")                                 \
  EX(74) EX(75) EX(76) EX(77) EX(78) EX(79) EX(80) EX(81) SM(OA, X1, "v[72:79]")            \
  EX(82) EX(83) EX(84) WT QK("v[64:79]", "v[128:131]") E1                                   \
  EX(85) EX(86) EX(87) EX(88) EX(89) SM(OB, X0, "v[80:87]") E2                              \
  EX(90) EX(91) EX(92) EX(93) EX(94) EX(95)
#define VA0 "v[140:143]"
#define VA1 "v[144:147]"
#define VB0 "v[148:151]"
#define VB1 "v[152:155]"
#define WL "s_waitcnt lgkmcnt(0)\n\t"

DEVI void role_a(const Args& a, u32 b, u32 w, u32 lane) {
  const u32 xcd = b & 7, idx = b >> 3, g = xcd * 4 + (idx >> 3), j = idx & 7;
  const u32 r32 = lane & 31, h = lane >> 5, th = w & 1, hp = w >> 1;
  const u32x4 sws = mk_srd(a.ws, WS_END), sxa = mk_srd(a.xa, 2048 * 1024), swq = mk_srd(a.wq, 512 * 1024);
  const u32x4 swkv = mk_srd((j & 1) ? a.wv : a.wk, 512 * 32);
  __builtin_amdgcn_s_setprio(3);
  STDECL;
  ST(0);
  u32x2 ep;
  asm volatile("buffer_load_dwordx2 %0, off, %1, 0 sc1" : "=v"(ep) : "s"(sws) : "memory");
  dma4(65536 + w * 4096, lane * 16, swkv, w * 4096);
  u32x4 xf0, xf1, xf2, xf3;
  {
    u32 vo = (lane & 15) * 1024 + (lane >> 4) * 16, so = (g * 64 + (j >> 1) * 16) * 1024 + w * 256;
    asm volatile("buffer_load_dwordx4 %0, %4, %5, %6 offen\n\tbuffer_load_dwordx4 %1, %4, %5, %6 offen offset:64\n\t"
                 "buffer_load_dwordx4 %2, %4, %5, %6 offen offset:128\n\tbuffer_load_dwordx4 %3, %4, %5, %6 offen offset:192"
                 : "=&v"(xf0), "=&v"(xf1), "=&v"(xf2), "=&v"(xf3) : "v"(vo), "s"(sxa), "s"(so) : "memory");
  }
  const u32 vr0 = (lane >> 3) * 1024 + (((lane & 7) ^ (lane >> 4)) << 4);
  const u32 vr1 = (lane >> 3) * 1024 + (((lane & 7) ^ (4 + (lane >> 4))) << 4) + 7168;
  const u32 vt0 = (lane >> 3) * 1024 + (((lane & 7) ^ (((lane >> 4) & 1) << 2)) << 4);
  const u32 vt1 = vt0 + 7168;
  const u32 rot = ((j + g) & 1) * 4;
  auto qdma = [&](u32 c) {
    u32 kc = (c + rot) & 7, sl = (c & 3) * 16384 + w * 2048;
    dma2(sl, vr0, vr1, sxa, (g * 64 + w * 16) * 1024 + kc * 128);
    dma2(sl + 8192, vt0, vt1, swq, (kc * 64 + w * 16) * 1024 + j * 128);
  };
  qdma(0); qdma(1); qdma(2); qdma(3);
  asm volatile("s_waitcnt vmcnt(16)" : "+v"(ep), "+v"(xf0), "+v"(xf1), "+v"(xf2), "+v"(xf3)::"memory");
  ST(1);
  u32x2 tag;
  {
    u64 e = (((u64)__builtin_amdgcn_readfirstlane(ep.y) << 32) | (u32)__builtin_amdgcn_readfirstlane(ep.x)) + 1;
    tag.x = (u32)e; tag.y = (u32)(e >> 32);
  }
  {
    const u32 base = 65536 + w * 4096 + ((lane >> 4) * 8 + ((lane & 15) >> 2)) * 32 + (lane & 3) * 8;
    bf16x8 wf[4], xf[4] = {__builtin_bit_cast(bf16x8, xf0), __builtin_bit_cast(bf16x8, xf1), __builtin_bit_cast(bf16x8, xf2), __builtin_bit_cast(bf16x8, xf3)};
#pragma unroll
    for (int i = 0; i < 4; ++i) wf[i] = ldstr(base + i * 1024, base + i * 1024 + 128);
    f32x4 acc = {0, 0, 0, 0};
    if (j & 1) {
#pragma unroll
      for (int i = 0; i < 4; ++i) acc = mfma16(xf[i], wf[i], acc);
    } else {
#pragma unroll
      for (int i = 0; i < 4; ++i) acc = mfma16(wf[i], xf[i], acc);
    }
    *(LAS f32x4*)(size_t)(65536 + w * 4096 + lane * 16) = acc;
    asm volatile("s_waitcnt lgkmcnt(0)\n\ts_barrier" ::: "memory");
    f32x4 s = *(LAS f32x4*)(size_t)(65536 + lane * 16);
#pragma unroll
    for (int i = 1; i < 4; ++i) s += *(LAS f32x4*)(size_t)(65536 + i * 4096 + lane * 16);
    u32x2 pv = {pk2(s[0], s[1]), pk2(s[2], s[3])};
    const u32 tq = j >> 1, g4 = lane >> 4, c16 = lane & 15;
    u32 off = WS_KV + (g * 2 + (tq >> 1)) * 2048;
    if (j & 1) off += 1024 + (tq & 1) * 512 + ((g4 >> 1) * 16 + c16) * 16 + (g4 & 1) * 8;
    else off += ((g4 & 1) * 32 + (tq & 1) * 16 + c16) * 16 + (g4 >> 1) * 8;
    st8c(pv, w == 0 ? off : OOB, sws);
  }
  ST(2);
  f32x16 qacc = {0, 0, 0, 0, 0, 0, 0, 0, 0, 0, 0, 0, 0, 0, 0, 0};
  const u32 rrow = (th * 32 + r32) * 128, rsw = (r32 >> 1) & 7;
  const u32 q4 = (lane & 15) >> 2, p4 = lane & 3, blk = (lane >> 4) & 1;
  const u32 trb = q4 * 128 + (((hp * 4 + blk * 2 + (p4 >> 1)) ^ ((q4 >> 1) << 2)) << 4) + (p4 & 1) * 8;
  {
    auto qcomp = [&](u32 c) {
      const u32 sl = (c & 3) * 16384;
#pragma unroll
      for (u32 s = 0; s < 4; ++s) {
        bf16x8 xb = ldsr(sl + rrow + (((2 * s + h) ^ rsw) << 4));
        u32 ta = sl + 8192 + trb + h * 1024 + s * 2048;
        qacc = mfma32(ldstr(ta, ta + 512), xb, qacc);
      }
    };
    waitvl_bar<13>(); qcomp(0);
    waitvl_bar<9>(); qdma(4);
    if (lane < 32) { u32x4 o = {0x3f803f80u, 0x3f803f80u, 0x3f803f80u, 0x3f803f80u};
      *(LAS u32x4*)(size_t)(65536 + (w * 2 + (lane >> 4)) * 2048 + ((lane >> 3) & 1) * 512 + (lane & 7) * 16) = o; }
    qcomp(1);
    waitvl_bar<9>(); qdma(5); qcomp(2);
    waitvl_bar<9>(); qdma(6); qcomp(3);
    WAITV(12);
    st8c(tag, (w == 0 && lane == 0) ? WS_KVF + (g * 8 + j) * 8 : OOB, sws);
    ST(3);
    waitvl_bar<9>(); qdma(7); qcomp(4);
    waitvl_bar<9>(); qcomp(5);
    waitvl_bar<5>(); qcomp(6);
    waitvl_bar<0>(); qcomp(7);
  }
  const float qs = 0.25f * 1.44269504088896340736f;
  u32x4 qa, qb;
#pragma unroll
  for (int i = 0; i < 4; ++i) { qa[i] = pk2(qacc[2 * i] * qs, qacc[2 * i + 1] * qs); qb[i] = pk2(qacc[8 + 2 * i] * qs, qacc[9 + 2 * i] * qs); }
  ST(4);
  poll<1>(WS_KVF + (w * 64 + lane) * 8, true, tag, sws);
  waitvl_bar<0>();
  ST(5);
  if (b == 0) st8c(tag, (w == 0 && lane == 0) ? WS_EPA : OOB, sws);
  f32x16 oa, ob;
  {
    const u32x4 skv = mk_srd(a.ws + WS_KV, 131072), swo = mk_srd(a.wo, 512 * 1024);
    u32 so = ((xcd + 1) & 7) * 16384;
    const u32 vl = lane * 16 + w * 4096;
#pragma unroll
    for (u32 c = 0; c < 4; ++c) { dma4c(c * 16384 + w * 4096, vl, skv, so); so = (so + 16384) & 0x1ffff; }
    u32 ka = lane * 16;
    u32 va = r32 < 16 ? (h * 16 + r32) * 16 : 64512 + ((r32 >= 20 && r32 < 28) ? 64 : 0);
    const u32 vinc = r32 < 16 ? 16384 : 0;
    u32 d0 = vl, d1 = vl, d2 = vl, d3 = vl;
    const u32 wv0 = vt0 + w * 32768 + j * 128;
    const u32 wso = (g & 3) * 131072;
    u32 m0v = w * 4096;
    const u32 m0lim = w * 4096 + 65536;
    asm volatile(
        "s_mov_b32 s40, %[k0]\n\ts_mov_b32 s41, %[k1]\n\ts_mov_b32 s42, %[k2]\n\ts_mov_b32 s43, %[k3]\n\t"
        "s_mov_b32 s44, 0x4000\n\ts_mov_b32 s45, 0x1ffff\n\ts_mov_b32 s46, 0x4000\n\ts_mov_b32 s47, 0\n\t"
        "s_mov_b32 m0, %[m0v]\n\t"
        "v_mov_b32 v156, 0xdddddddd\n\t"
        "v_mov_b32 v88, 0\n\tv_mov_b32 v89, 0\n\tv_mov_b32 v90, 0\n\tv_mov_b32 v91, 0\n\tv_mov_b32 v92, 0\n\tv_mov_b32 v93, 0\n\tv_mov_b32 v94, 0\n\tv_mov_b32 v95, 0\n\t"
        "v_mov_b32 v152, 0\n\tv_mov_b32 v153, 0\n\tv_mov_b32 v154, 0\n\tv_mov_b32 v155, 0\n\t"
        "v_mov_b32 v96, 0\n\tv_mov_b32 v97, 0\n\tv_mov_b32 v98, 0\n\tv_mov_b32 v99, 0\n\tv_mov_b32 v100, 0\n\tv_mov_b32 v101, 0\n\tv_mov_b32 v102, 0\n\tv_mov_b32 v103, 0\n\t"
        "v_mov_b32 v104, 0\n\tv_mov_b32 v105, 0\n\tv_mov_b32 v106, 0\n\tv_mov_b32 v107, 0\n\tv_mov_b32 v108, 0\n\tv_mov_b32 v109, 0\n\tv_mov_b32 v110, 0\n\tv_mov_b32 v111, 0\n\t"
        "v_mov_b32 v112, 0\n\tv_mov_b32 v113, 0\n\tv_mov_b32 v114, 0\n\tv_mov_b32 v115, 0\n\tv_mov_b32 v116, 0\n\tv_mov_b32 v117, 0\n\tv_mov_b32 v118, 0\n\tv_mov_b32 v119, 0\n\t"
        "v_mov_b32 v120, 0\n\tv_mov_b32 v121, 0\n\tv_mov_b32 v122, 0\n\tv_mov_b32 v123, 0\n\tv_mov_b32 v124, 0\n\tv_mov_b32 v125, 0\n\tv_mov_b32 v126, 0\n\tv_mov_b32 v127, 0\n\t"
        "s_waitcnt vmcnt(12)\n\ts_barrier\n\t"
        "ds_read_b128 v[136:139], %[ka]\n\tds_read_b128 " VA0 ", %[va] offset:1024\n\tds_read_b128 " VA1 ", %[va] offset:1536\n\t"
        "s_setprio 0\n\t"
        WL QK("v[64:79]", "v[128:131]") "s_nop 11\n\t"
        "1:\n\t"
        IT(VA0, VA1, VB0, VB1, 2048, 3072, 3584, WL, "", "")
        IT(VB0, VB1, VA0, VA1, 4096, 5120, 5632, WL, "", "")
        IT(VA0, VA1, VB0, VB1, 6144, 7168, 7680, WL, "", "")
        IT(VB0, VB1, VA0, VA1, 8192, 9216, 9728, WL, "", "")
        IT(VA0, VA1, VB0, VB1, 10240, 11264, 11776, WL, "", "")
        IT(VB0, VB1, VA0, VA1, 12288, 13312, 13824, WL, "", "")
        IT(VA0, VA1, VB0, VB1, 14336, 15360, 15872, "s_waitcnt vmcnt(8) lgkmcnt(0)\n\t",
           "s_barrier\n\t"
           "buffer_load_dwordx4 %[d0], s[40:43], %[so] offen sc1 lds\n\tbuffer_load_dwordx4 %[d1], s[40:43], %[so] offen offset:1024 sc1 lds\n\t"
           "v_add_u16 %[ka], s46, %[ka]\n\tv_add_u16 %[va], %[vinc], %[va]\n\t",
           "buffer_load_dwordx4 %[d2], s[40:43], %[so] offen offset:2048 sc1 lds\n\tbuffer_load_dwordx4 %[d3], s[40:43], %[so] offen offset:3072 sc1 lds\n\t")
        IT(VB0, VB1, VA0, VA1, 0, 1024, 1536, WL,
           "s_add_u32 %[so], %[so], s44\n\ts_and_b32 %[so], %[so], s45\n\t",
           "s_add_u32 m0, m0, s46\n\ts_cmp_lg_u32 m0, %[m0lim]\n\t")
        "s_cbranch_scc1 1b\n\t"
        "s_cmp_lg_u32 s47, 0\n\ts_cbranch_scc1 2f\n\t"
        "s_mov_b32 s47, 1\n\ts_mov_b32 s40, %[w0]\n\ts_mov_b32 s41, %[w1]\n\ts_mov_b32 s42, %[w2]\n\t"
        "s_mov_b32 s44, 0x20000\n\ts_mov_b32 s45, 0x7ffff\n\ts_mov_b32 %[so], %[wso]\n\ts_mov_b32 m0, %[m0v]\n\t"
        "v_mov_b32 %[d0], %[wv0]\n\tv_add_u32 %[d1], 7168, %[wv0]\n\tv_add_u32 %[d2], 14336, %[wv0]\n\tv_add_u32 %[d3], 21504, %[wv0]\n\t"
        "s_branch 1b\n\t"
        "2:\n\t"
        "s_setprio 3\n\ts_nop 0\n\t"
        SM(OB, VB1, "v[88:95]")
        "s_nop 15\n\t"
        : "=&{v[96:111]}"(oa), "=&{v[112:127]}"(ob), [ka] "+v"(ka), [va] "+v"(va), [so] "+s"(so),
          [d0] "+v"(d0), [d1] "+v"(d1), [d2] "+v"(d2), [d3] "+v"(d3)
        : "{v[128:131]}"(qa), "{v[132:135]}"(qb), [vinc] "v"(vinc), [wv0] "v"(wv0), [wso] "s"(wso), [m0v] "s"(m0v), [m0lim] "s"(m0lim),
          [k0] "s"(skv.x), [k1] "s"(skv.y), [k2] "s"(skv.z), [k3] "s"(skv.w), [w0] "s"(swo.x), [w1] "s"(swo.y), [w2] "s"(swo.z)
        : "memory", "scc", "s40", "s41", "s42", "s43", "s44", "s45", "s46", "s47",
          "v64", "v65", "v66", "v67", "v68", "v69", "v70", "v71", "v72", "v73", "v74", "v75", "v76", "v77", "v78", "v79",
          "v80", "v81", "v82", "v83", "v84", "v85", "v86", "v87", "v88", "v89", "v90", "v91", "v92", "v93", "v94", "v95",
          "v136", "v137", "v138", "v139", "v140", "v141", "v142", "v143", "v144", "v145", "v146", "v147",
          "v148", "v149", "v150", "v151", "v152", "v153", "v154", "v155", "v156");
  }
  ST(6);
  {
    float ma = 0.f;
#pragma unroll
    for (int i = 0; i < 8; ++i) ma += __builtin_fabsf(oa[i]) * 0.0625f + __builtin_fabsf(ob[i]) * 0.0625f;
    const bool ok = oa[8] >= 0x1p-64f && oa[8] <= 0x1p100f && ob[8] >= 0x1p-64f && ob[8] <= 0x1p100f && ma <= 3.0e38f;
    if (__builtin_amdgcn_ballot_w64(!ok) != 0) {
      const __amdgpu_buffer_rsrc_t rk = __builtin_amdgcn_make_buffer_rsrc(a.ws + WS_KV, 0, 131072, 0x20000);
      const u32x4 ones = {0x3f803f80u, 0x3f803f80u, 0x3f803f80u, 0x3f803f80u};
#pragma unroll 1
      for (int t = 0; t < 2; ++t) {
        const bf16x8 q = __builtin_bit_cast(bf16x8, t ? qb : qa);
        f32x16 z = {0, 0, 0, 0, 0, 0, 0, 0, 0, 0, 0, 0, 0, 0, 0, 0};
        float m = -__builtin_inff();
#pragma unroll 1
        for (u32 kt = 0; kt < 64; ++kt) {
          u32x4 kf = __builtin_amdgcn_raw_buffer_load_b128(rk, lane * 16, kt * 2048, 16);
          f32x16 s = mfma32(__builtin_bit_cast(bf16x8, kf), q, z);
#pragma unroll
          for (int i = 0; i < 16; ++i) m = __builtin_fmaxf(m, s[i]);
        }
        m = __builtin_fmaxf(m, __shfl_xor(m, 32));
        f32x16 o = z;
#pragma unroll 1
        for (u32 kt = 0; kt < 64; ++kt) {
          u32x4 kf = __builtin_amdgcn_raw_buffer_load_b128(rk, lane * 16, kt * 2048, 16);
          u32x4 v0 = __builtin_amdgcn_raw_buffer_load_b128(rk, (h * 16 + (r32 & 15)) * 16 + 1024, kt * 2048, 16);
          u32x4 v1 = __builtin_amdgcn_raw_buffer_load_b128(rk, (h * 16 + (r32 & 15)) * 16 + 1536, kt * 2048, 16);
          if (r32 >= 16) { v0 = ones; v1 = ones; }
          f32x16 s = mfma32(__builtin_bit_cast(bf16x8, kf), q, z);
          f32x8 p0, p1;
#pragma unroll
          for (int i = 0; i < 8; ++i) { p0[i] = __builtin_amdgcn_exp2f(s[i] - m); p1[i] = __builtin_amdgcn_exp2f(s[8 + i] - m); }
          o = __builtin_amdgcn_smfmac_f32_32x32x32_bf16(__builtin_bit_cast(bf16x8, v0), __builtin_bit_cast(bf16x16, p0), o, (int)0xdddddddd, 0, 0);
          o = __builtin_amdgcn_smfmac_f32_32x32x32_bf16(__builtin_bit_cast(bf16x8, v1), __builtin_bit_cast(bf16x16, p1), o, (int)0xdddddddd, 0, 0);
        }
        if (t) ob = o; else oa = o;
      }
    }
  }
  {
    const float ia = __builtin_amdgcn_rcpf(oa[8]), ib = __builtin_amdgcn_rcpf(ob[8]);
    u32x4 pa, pb;
#pragma unroll
    for (int i = 0; i < 4; ++i) { pa[i] = pk2(oa[2 * i] * ia, oa[2 * i + 1] * ia); pb[i] = pk2(ob[2 * i] * ib, ob[2 * i + 1] * ib); }
    const u32 off = WS_O + g * 65536 + ((j * 4 + hp * 2) * 2 + th) * 1024 + lane * 16;
    st16c(pa, off, sws);
    st16c(pb, off + 2048, sws);
  }
  waitvl_bar<2>();
  bf16x8 wf[32];
#pragma unroll
  for (u32 i = 0; i < 32; ++i) { u32 ta = trb + h * 512 + i * 2048; wf[i] = ldstr(ta, ta + 1024); }
  waitvl_bar<0>();
  st8c(tag, (w == 0 && lane == 0) ? WS_OF + g * 256 + j * 8 : OOB, sws);
  ST(7);
  poll<1>(WS_OF + g * 256 + (lane & 7) * 8, true, tag, sws);
  ST(8);
#pragma unroll
  for (u32 c = 0; c < 4; ++c) dma4c(c * 16384 + w * 4096, lane * 16, sws, WS_O + g * 65536 + (((g & 3) + c) & 3) * 16384 + w * 4096);
  f32x16 acc = {0, 0, 0, 0, 0, 0, 0, 0, 0, 0, 0, 0, 0, 0, 0, 0};
  auto ocomp = [&](u32 c) {
#pragma unroll
    for (u32 i = 0; i < 8; ++i) acc = mfma32(ldsr(c * 16384 + i * 2048 + th * 1024 + lane * 16), wf[c * 8 + i], acc);
  };
  waitvl_bar<12>(); ocomp(0);
  waitvl_bar<8>(); ocomp(1);
  waitvl_bar<4>(); ocomp(2);
  waitvl_bar<0>(); ocomp(3);
  ST(9);
  float* op = a.out + (size_t)(g * 64 + th * 32 + h * 4) * 1024 + j * 64 + hp * 32 + r32;
#pragma unroll
  for (int i = 0; i < 16; ++i) op[((i & 3) + 8 * (i >> 2)) * 1024] = acc[i];
  ST(10);
  STFLUSH;
}

DEVI void cmul(float& r, float& i, float ar, float ai, float br, float bi) { r = ar * br - ai * bi; i = ar * bi + ai * br; }
DEVI void cstep(float& r, float& i, float ar, float ai, float ur, float ui) {
  float nr = __builtin_fmaf(r, ar, __builtin_fmaf(-i, ai, ur)), ni = __builtin_fmaf(i, ar, __builtin_fmaf(r, ai, ui));
  r = nr; i = ni;
}

DEVI void role_b(const Args& a, u32 b, u32 w, u32 lane) {
  const u32 xcd = b & 7, idx = b >> 3, g = xcd * 4 + (idx >> 3), j = idx & 7;
  const u32 r32 = lane & 31, h = lane >> 5, th = w & 1, ph = w >> 1;
  const u32x4 sws = mk_srd(a.ws, WS_END), sxb = mk_srd(a.xb, 2048 * 1024);
  __builtin_amdgcn_s_setprio(2);
  STDECL;
  ST(0);
  u32x2 ep;
  u32 plr, pli, pdt;
  {
    const u32x4 s0 = mk_srd(a.lre, 1024), s1 = mk_srd(a.lim, 1024), s2 = mk_srd(a.ldt, 1024);
    const u32 vo = (j * 64 + ph * 32 + r32) * 2;
    asm volatile("buffer_load_dwordx2 %0, off, %4, 0 offset:256 sc1\n\tbuffer_load_ushort %1, %8, %5, 0 offen\n\t"
                 "buffer_load_ushort %2, %8, %6, 0 offen\n\tbuffer_load_ushort %3, %8, %7, 0 offen"
                 : "=&v"(ep), "=&v"(plr), "=&v"(pli), "=&v"(pdt) : "s"(sws), "s"(s0), "s"(s1), "s"(s2), "v"(vo) : "memory");
  }
  const u32 vr0 = (lane >> 3) * 1024 + (((lane & 7) ^ (lane >> 4)) << 4);
  const u32 vr1 = (lane >> 3) * 1024 + (((lane & 7) ^ (4 + (lane >> 4))) << 4) + 7168;
  const u32 rsw = (r32 >> 1) & 7;
  f32x16 ur = {0, 0, 0, 0, 0, 0, 0, 0, 0, 0, 0, 0, 0, 0, 0, 0}, ui = ur;
  float ar, ai, gr, gi;
  u32x2 tag;
  {
    const u32x4 sbr = mk_srd(a.bre, 512 * 1024), sbi = mk_srd(a.bim, 512 * 1024);
    const u32 xrow = (16 * h + ((lane >> 3) & 3)) * 1024;
    const u32 vx0 = xrow + (((lane & 7) ^ (lane >> 4)) << 4);
    const u32 vx1 = xrow + (((lane & 7) ^ (4 + (lane >> 4))) << 4) + 3072;
    const u32 rot = (j + (g & 3) * 2) & 7;
    auto bdma = [&](u32 c) {
      u32 kc = (c + rot) & 7, sl = (c % 3) * 24576 + w * 2048;
      dma2(sl, vx0, vx1, sxb, (g * 64 + ph * 32 + th * 8) * 1024 + kc * 128);
      dma2(sl + 8192, vr0, vr1, sbr, (j * 64 + w * 16) * 1024 + kc * 128);
      dma2(sl + 16384, vr0, vr1, sbi, (j * 64 + w * 16) * 1024 + kc * 128);
    };
    bdma(0); bdma(1); bdma(2);
    asm volatile("s_waitcnt vmcnt(18)" : "+v"(ep), "+v"(plr), "+v"(pli), "+v"(pdt)::"memory");
    {
      u64 e = (((u64)__builtin_amdgcn_readfirstlane(ep.y) << 32) | (u32)__builtin_amdgcn_readfirstlane(ep.x)) + 1;
      tag.x = (u32)e; tag.y = (u32)(e >> 32);
    }
    {
      const float lr = bf2f(plr), li = bf2f(pli);
      const float dt = bf2f(pk2(expf(bf2f(pdt)), 0.f) & 0xffffu);
      const float e = expf(dt * lr);
      float sn, cs;
      sincosf(dt * li, &sn, &cs);
      ar = e * cs; ai = e * sn;
      const float nr = ar - 1.f, ni = ai;
      const bool sw = __builtin_fabsf(lr) >= __builtin_fabsf(li);
      const float big = sw ? lr : li, sm = sw ? li : lr, rt = sm / big, den = big + sm * rt;
      const float x = sw ? nr : ni, y = sw ? ni : nr;
      gr = 2.f * (x + y * rt) / den;
      gi = 2.f * (y - x * rt) / den;
      gi = sw ? gi : -gi;
      ai = -ai;
    }
    auto bcomp = [&](u32 c) {
      const u32 sl = (c % 3) * 24576;
#pragma unroll
      for (u32 s = 0; s < 4; ++s) {
        const u32 sw = ((2 * s + h) ^ rsw) << 4;
        bf16x8 xf = ldsr(sl + (th * 32 + r32) * 128 + sw);
        ur = mfma32(xf, ldsr(sl + 8192 + (ph * 32 + r32) * 128 + sw), ur);
        ui = mfma32(xf, ldsr(sl + 16384 + (ph * 32 + r32) * 128 + sw), ui);
      }
    };
    waitvl_bar<12>(); bcomp(0);
    waitvl_bar<6>(); bdma(3); bcomp(1);
    waitvl_bar<6>(); bdma(4); bcomp(2);
    waitvl_bar<6>(); bdma(5); bcomp(3);
    waitvl_bar<6>(); bdma(6); bcomp(4);
    waitvl_bar<6>(); bdma(7); bcomp(5);
    waitvl_bar<6>(); bcomp(6);
    waitvl_bar<0>(); bcomp(7);
  }
  ST(1);
  float er = 0.f, ei = 0.f;
#pragma unroll
  for (int i = 0; i < 16; ++i) {
    float xr = gr * ur[i] - gi * ui[i], xi = -(gr * ui[i] + gi * ur[i]);
    ur[i] = xr; ui[i] = xi;
    cstep(er, ei, ar, ai, xr, xi);
  }
  float a16r = ar, a16i = ai;
#pragma unroll
  for (int i = 0; i < 4; ++i) cmul(a16r, a16i, a16r, a16i, a16r, a16i);
  const u32 eb = 73728 + (ph * 32 + r32) * 8;
  { f32x2 e2 = {er, ei}; *(LAS f32x2*)(size_t)(eb + (th * 2 + h) * 512) = e2; }
  waitvl_bar<0>();
  f32x2 es[4];
#pragma unroll
  for (int s = 0; s < 4; ++s) es[s] = *(LAS f32x2*)(size_t)(eb + s * 512);
  if (th == 0) {
    float tr = es[0].x, ti = es[0].y;
#pragma unroll
    for (int s = 1; s < 4; ++s) cstep(tr, ti, a16r, a16i, es[s].x, es[s].y);
    u32x2 ev = {__builtin_bit_cast(u32, tr), __builtin_bit_cast(u32, ti)};
    st8c(ev, h == 0 ? WS_E + (j * 32 + g) * 512 + (ph * 32 + r32) * 8 : OOB, sws);
    WAITV(0);
    st8c(tag, lane == 0 ? WS_EF + ((j * 2 + ph) * 32 + g) * 8 : OOB, sws);
  }
  ST(2);
  float a64r = a16r, a64i = a16i;
  cmul(a64r, a64i, a64r, a64i, a64r, a64i);
  cmul(a64r, a64i, a64r, a64i, a64r, a64i);
  float akr = a64r, aki = a64i;
#pragma unroll
  for (int i = 0; i < 4; ++i) cmul(akr, aki, akr, aki, akr, aki);
  poll<4>(WS_EF + ((j * 2 + ph) * 32 + r32) * 8, r32 < g, tag, sws);
  ST(3);
  float cr = 0.f, ci = 0.f;
  {
    const __amdgpu_buffer_rsrc_t re = __builtin_amdgcn_make_buffer_rsrc(a.ws + WS_E + j * 16384, 0, 16384, 0x20000);
    u32x2 ev[16];
#pragma unroll
    for (int m = 0; m < 16; ++m) {
      int gp = (int)g - 1 - 16 * (int)h - m;
      ev[m] = __builtin_amdgcn_raw_buffer_load_b64(re, gp >= 0 ? (u32)gp * 512 + (ph * 32 + r32) * 8 : OOB, 0, 16);
    }
#pragma unroll
    for (int m = 15; m >= 0; --m) { const f32x2 e2 = __builtin_bit_cast(f32x2, ev[m]); cstep(cr, ci, a64r, a64i, e2.x, e2.y); }
    float orr = __shfl_xor(cr, 32), oi = __shfl_xor(ci, 32);
    float lr_ = h ? orr : cr, li_ = h ? oi : ci, hr_ = h ? cr : orr, hi_ = h ? ci : oi;
    if (g > 16) { cstep(hr_, hi_, akr, aki, lr_, li_); cr = hr_; ci = hi_; } else { cr = lr_; ci = li_; }
#pragma unroll
    for (u32 s = 0; s < 3; ++s) {
      float tr = cr, ti = ci;
      cstep(tr, ti, a16r, a16i, es[s].x, es[s].y);
      const bool use = s < th * 2 + h;
      cr = use ? tr : cr; ci = use ? ti : ci;
    }
  }
#pragma unroll
  for (int i = 0; i < 16; ++i) { cstep(cr, ci, ar, ai, ur[i], ui[i]); ur[i] = cr; ui[i] = ci; }
  {
    const u32 off = WS_H + (g * 8 + j) * 16384 + (ph * 2 + th) * 2048 + h * 512;
#pragma unroll
    for (u32 i = 0; i < 2; ++i) {
      u32x4 pr, pi;
#pragma unroll
      for (int e = 0; e < 4; ++e) { pr[e] = pk2(ur[8 * i + 2 * e], ur[8 * i + 2 * e + 1]); pi[e] = pk2(ui[8 * i + 2 * e], ui[8 * i + 2 * e + 1]); }
      const u32 o = off + i * 1024 + ((r32 ^ ((2 * i + h) << 2)) << 4);
      st16c(pr, o, sws);
      st16c(pi, o + 8192, sws);
    }
  }
  waitvl_bar<0>();
  st8c(tag, (w == 0 && lane == 0) ? WS_HF + g * 256 + j * 8 : OOB, sws);
  ST(4);
  const u32 col = j * 64 + ph * 32 + r32;
  f32x16 acc;
  {
    const float dk = bf2f(a.dsk[col]);
    const u16* xp = a.xb + (size_t)(g * 64 + th * 32 + h * 4) * 512 + col;
#pragma unroll
    for (int i = 0; i < 16; ++i) acc[i] = bf2f(xp[((i & 3) + 8 * (i >> 2)) * 512]) * dk;
    asm volatile("" : "+v"(acc));
  }
  poll<4>(WS_HF + g * 256 + (lane & 7) * 8, true, tag, sws);
  ST(5);
  if (g == 31 && j == 0) st8c(tag, (w == 0 && lane == 0) ? WS_EPB : OOB, sws);
  {
    const u32x4 scr = mk_srd(a.cre, 512 * 1024), sci = mk_srd(a.cim, 512 * 1024);
    auto cdma = [&](u32 c) {
      u32 jj = ((c >> 1) + j) & 7, sl = (c % 5) * 16384 + w * 2048;
      dma2c(sl, lane * 16, sws, WS_H + (g * 8 + jj) * 16384 + (c & 1) * 8192 + w * 2048);
      dma2(sl + 8192, vr0, vr1, (c & 1) ? sci : scr, (j * 64 + w * 16) * 1024 + jj * 128);
    };
    const u32 q4 = (lane & 15) >> 2, p4 = lane & 3, blk = (lane >> 4) & 1;
    const u32 tb0 = (th * 2 + (p4 >> 1)) * 1024 + blk * 512 + (((8 * h + q4) ^ ((2 * (p4 >> 1) + blk) << 2)) << 4) + (p4 & 1) * 8;
    const u32 tb1 = tb0 ^ 64;
    auto ccomp = [&](u32 c) {
      const u32 sl = (c % 5) * 16384;
#pragma unroll
      for (u32 s = 0; s < 4; ++s) {
        const u32 o = sl + (s >> 1) * 4096 + (s & 1) * 256;
        acc = mfma32(ldstr(tb0 + o, tb1 + o), ldsr(sl + 8192 + (ph * 32 + r32) * 128 + (((2 * s + h) ^ rsw) << 4)), acc);
      }
    };
    cdma(0); cdma(1); cdma(2); cdma(3); cdma(4);
    waitvl_bar<16>(); ccomp(0);
#pragma unroll
    for (u32 c = 1; c < 12; ++c) { waitvl_bar<12>(); cdma(c + 4); ccomp(c); }
    waitvl_bar<12>(); ccomp(12);
    waitvl_bar<8>(); ccomp(13);
    waitvl_bar<4>(); ccomp(14);
    waitvl_bar<0>(); ccomp(15);
  }
  ST(6);
  float* op = a.out + (size_t)(g * 64 + th * 32 + h * 4) * 1024 + 512 + col;
#pragma unroll
  for (int i = 0; i < 16; ++i) op[((i & 3) + 8 * (i >> 2)) * 1024] = acc[i];
  ST(7);
  STFLUSH;
}

__global__ void __launch_bounds__(256, 2) fused_kernel(Args a) {
  __shared__ __attribute__((aligned(16))) char lds[81920];
  asm volatile("" ::"v"((u32)(size_t)(LAS char*)lds));
  const u32 lane = threadIdx.x & 63, w = __builtin_amdgcn_readfirstlane(threadIdx.x >> 6), b = blockIdx.x;
  if (b < 256) role_a(a, b, w, lane); else {
    role_b(a, b - 256, w, lane);
  }
}

extern "C" void kernel_launch(void* const* d_in, const int* in_sizes, int n_in, void* d_out, int out_size, void* d_ws,
                              size_t ws_size, hipStream_t stream) {
  Args a;
  a.xa = (const u16*)d_in[0]; a.wq = (const u16*)d_in[1]; a.wk = (const u16*)d_in[2]; a.wv = (const u16*)d_in[3];
  a.wo = (const u16*)d_in[4]; a.xb = (const u16*)d_in[5]; a.lre = (const u16*)d_in[6]; a.lim = (const u16*)d_in[7];
  a.bre = (const u16*)d_in[8]; a.bim = (const u16*)d_in[9]; a.cre = (const u16*)d_in[10]; a.cim = (const u16*)d_in[11];
  a.ldt = (const u16*)d_in[12]; a.dsk = (const u16*)d_in[13];
  a.out = (float*)d_out; a.ws = (char*)d_ws;
  fused_kernel<<<512, 256, 0, stream>>>(a);
}
```

```cpp
#define STAMP
#define DIAG 2
#define DIAGTHR 31.5f
#include <hip/hip_runtime.h>
typedef unsigned short u16;
typedef unsigned u32;
typedef unsigned long long u64;
typedef float f32x2 __attribute__((ext_vector_type(2)));
typedef float f32x4 __attribute__((ext_vector_type(4)));
typedef float f32x8 __attribute__((ext_vector_type(8)));
typedef float f32x16 __attribute__((ext_vector_type(16)));
typedef __bf16 bf16x2 __attribute__((ext_vector_type(2)));
typedef __bf16 bf16x8 __attribute__((ext_vector_type(8)));
typedef __bf16 bf16x16 __attribute__((ext_vector_type(16)));
typedef short s16x4 __attribute__((ext_vector_type(4)));
typedef u32 u32x2 __attribute__((ext_vector_type(2)));
typedef u32 u32x4 __attribute__((ext_vector_type(4)));
#define LAS __attribute__((address_space(3)))
#define DEVI __device__ __forceinline__

constexpr u32 WS_EPA = 0, WS_EPB = 256;
constexpr u32 WS_KVF = 4096;
constexpr u32 WS_EF = 8192;
constexpr u32 WS_HF = 16384;
constexpr u32 WS_OF = 32768;
constexpr u32 WS_KV = 65536;
constexpr u32 WS_E = 262144;
constexpr u32 WS_O = 524288;
constexpr u32 WS_H = WS_O + 2097152;
constexpr u32 WS_END = WS_H + 4194304;
constexpr u32 WS_DBG = WS_END;
constexpr u32 OOB = 0x80000000u;

struct Args {
  const u16 *xa, *wq, *wk, *wv, *wo, *xb, *lre, *lim, *bre, *bim, *cre, *cim, *ldt, *dsk;
  float* out;
  char* ws;
};

DEVI u32x4 mk_srd(const void* p, u32 bytes) {
  u64 a = (u64)p;
  u32x4 r = {(u32)a, (u32)(a >> 32) & 0xffffu, bytes, 0x20000u};
  return r;
}
DEVI void dma2(u32 m0v, u32 v0, u32 v1, u32x4 srd, u32 soff) {
  asm volatile("s_mov_b32 m0, %0\n\ts_nop 0\n\tbuffer_load_dwordx4 %1, %3, %4 offen lds\n\tbuffer_load_dwordx4 %2, %3, %4 offen offset:1024 lds"
               ::"s"(m0v), "v"(v0), "v"(v1), "s"(srd), "s"(soff) : "memory");
}
DEVI void dma2c(u32 m0v, u32 v0, u32x4 srd, u32 soff) {
  asm volatile("s_mov_b32 m0, %0\n\ts_nop 0\n\tbuffer_load_dwordx4 %1, %2, %3 offen sc1 lds\n\tbuffer_load_dwordx4 %1, %2, %3 offen offset:1024 sc1 lds"
               ::"s"(m0v), "v"(v0), "s"(srd), "s"(soff) : "memory");
}
DEVI void dma4(u32 m0v, u32 v0, u32x4 srd, u32 soff) {
  asm volatile("s_mov_b32 m0, %0\n\ts_nop 0\n\tbuffer_load_dwordx4 %1, %2, %3 offen lds\n\tbuffer_load_dwordx4 %1, %2, %3 offen offset:1024 lds\n\t"
               "buffer_load_dwordx4 %1, %2, %3 offen offset:2048 lds\n\tbuffer_load_dwordx4 %1, %2, %3 offen offset:3072 lds"
               ::"s"(m0v), "v"(v0), "s"(srd), "s"(soff) : "memory");
}
DEVI void dma4c(u32 m0v, u32 v0, u32x4 srd, u32 soff) {
  asm volatile("s_mov_b32 m0, %0\n\ts_nop 0\n\tbuffer_load_dwordx4 %1, %2, %3 offen sc1 lds\n\tbuffer_load_dwordx4 %1, %2, %3 offen offset:1024 sc1 lds\n\t"
               "buffer_load_dwordx4 %1, %2, %3 offen offset:2048 sc1 lds\n\tbuffer_load_dwordx4 %1, %2, %3 offen offset:3072 sc1 lds"
               ::"s"(m0v), "v"(v0), "s"(srd), "s"(soff) : "memory");
}
DEVI void st8c(u32x2 v, u32 voff, u32x4 srd) {
  asm volatile("buffer_store_dwordx2 %0, %1, %2, 0 offen sc1" ::"v"(v), "v"(voff), "s"(srd) : "memory");
}
DEVI void st16c(u32x4 v, u32 voff, u32x4 srd) {
  asm volatile("buffer_store_dwordx4 %0, %1, %2, 0 offen sc1\n\ts_nop 1" ::"v"(v), "v"(voff), "s"(srd) : "memory");
}
#define WAITV(n) asm volatile("s_waitcnt vmcnt(" #n ")" ::: "memory")
#define WAITVL(n) asm volatile("s_waitcnt vmcnt(" #n ") lgkmcnt(0)" ::: "memory")
#define BAR() asm volatile("s_barrier" ::: "memory")
template <int N> DEVI void waitvl_bar() { asm volatile("s_waitcnt vmcnt(%0) lgkmcnt(0)\n\ts_barrier" ::"i"(N) : "memory"); }

template <int SLEEP> DEVI void poll(u32 voff, bool need, u32x2 tag, u32x4 srd) {
  for (u32 it = 0; it < (1u << 18); ++it) {
    u32x2 f;
    asm volatile("buffer_load_dwordx2 %0, %1, %2, 0 offen sc1\n\ts_waitcnt vmcnt(0)" : "=&v"(f) : "v"(voff), "s"(srd) : "memory");
    bool bad = need && (f.x != tag.x || f.y != tag.y);
    if (__builtin_amdgcn_ballot_w64(bad) == 0) break;
    __builtin_amdgcn_s_sleep(SLEEP);
  }
}
DEVI u32 pk2(float a, float b) {
  f32x2 x = {a, b};
  return __builtin_bit_cast(u32, __builtin_convertvector(x, bf16x2));
}
DEVI float bf2f(u32 v) { return __builtin_bit_cast(float, v << 16); }
DEVI bf16x8 ldsr(u32 a) { return *(const LAS bf16x8*)(size_t)a; }
DEVI bf16x8 ldstr(u32 a0, u32 a1) {
  s16x4 x = __builtin_amdgcn_ds_read_tr16_b64_v4i16((LAS s16x4*)(size_t)a0);
  s16x4 y = __builtin_amdgcn_ds_read_tr16_b64_v4i16((LAS s16x4*)(size_t)a1);
  return __builtin_bit_cast(bf16x8, __builtin_shufflevector(x, y, 0, 1, 2, 3, 4, 5, 6, 7));
}
DEVI f32x16 mfma32(bf16x8 a, bf16x8 b, f32x16 c) { return __builtin_amdgcn_mfma_f32_32x32x16_bf16(a, b, c, 0, 0, 0); }
DEVI f32x4 mfma16(bf16x8 a, bf16x8 b, f32x4 c) { return __builtin_amdgcn_mfma_f32_16x16x32_bf16(a, b, c, 0, 0, 0); }

#define STDECL u32 stv[12] = {0,0,0,0,0,0,0,0,0,0,0,0}, stc[12] = {0,0,0,0,0,0,0,0,0,0,0,0}
#define ST(i) do { u64 x_, y_; asm volatile("s_memrealtime %0\n\ts_memtime %1\n\ts_waitcnt lgkmcnt(0)" : "=s"(x_), "=s"(y_) :: "memory"); stv[i] = (u32)x_ | 1; stc[i] = (u32)y_; } while (0)
#define STC(v) asm volatile("s_memtime %0\n\ts_waitcnt lgkmcnt(0)" : "=s"(v) :: "memory")
#define STFLUSH do { if (lane == 0 && w == 0) for (int i_ = 0; i_ < 12; ++i_) ((u64*)(a.ws + WS_DBG))[blockIdx.x * 16 + i_] = stv[i_] | ((u64)stc[i_] << 32); } while (0)

#define EX(r) "v_exp_f32 v" #r ", v" #r "\n\t"
#define SM(o, vv, p) "v_smfmac_f32_32x32x32_bf16 " o ", " vv ", " p ", v156\n\t"
#define OA "v[96:111]"
#define OB "v[112:127]"
#define QK(s, q) "v_mfma_f32_32x32x16_bf16 " s ", v[136:139], " q ", 0\n\t"
#define IT(X0, X1, Y0, Y1, KO, V0O, V1O, WT, E1, E2)                                        \
  EX(64) EX(65) SM(OB, Y1, "v[88:95]")                                                      \
  "ds_read_b128 " Y0 ", %[va] offset:" #V0O "\n\tds_read_b128 " Y1 ", %[va] offset:" #V1O "\n\t" \
  EX(66) EX(67) EX(68) QK("v[80:95]", "v[132:135]")                                         \
  "ds_read_b128 v[136:139], %[ka] offset:" #KO "\n\t"                                       \
  EX(69) EX(70) EX(71) EX(72) EX(73) SM(OA, X0, "v[64:71]")                                 \
  EX(74) EX(75) EX(76) EX(77) EX(78) EX(79) EX(80) EX(81) SM(OA, X1, "v[72:79]")            \
  EX(82) EX(83) EX(84) WT QK("v[64:79]", "v[128:131]") E1                                   \
  EX(85) EX(86) EX(87) EX(88) EX(89) SM(OB, X0, "v[80:87]") E2                              \
  EX(90) EX(91) EX(92) EX(93) EX(94) EX(95)
#define VA0 "v[140:143]"
#define VA1 "v[144:147]"
#define VB0 "v[148:151]"
#define VB1 "v[152:155]"
#define WL "s_waitcnt lgkmcnt(0)\n\t"

DEVI void role_a(const Args& a, u32 b, u32 w, u32 lane) {
  const u32 xcd = b & 7, idx = b >> 3, g = xcd * 4 + (idx >> 3), j = idx & 7;
  const u32 r32 = lane & 31, h = lane >> 5, th = w & 1, hp = w >> 1;
  const u32x4 sws = mk_srd(a.ws, WS_END), sxa = mk_srd(a.xa, 2048 * 1024), swq = mk_srd(a.wq, 512 * 1024);
  const u32x4 swkv = mk_srd((j & 1) ? a.wv : a.wk, 512 * 32);
  __builtin_amdgcn_s_setprio(3);
  STDECL;
  ST(0);
  u32x2 ep;
  asm volatile("buffer_load_dwordx2 %0, off, %1, 0 sc1" : "=v"(ep) : "s"(sws) : "memory");
  dma4(65536 + w * 4096, lane * 16, swkv, w * 4096);
  u32x4 xf0, xf1, xf2, xf3;
  {
    u32 vo = (lane & 15) * 1024 + (lane >> 4) * 16, so = (g * 64 + (j >> 1) * 16) * 1024 + w * 256;
    asm volatile("buffer_load_dwordx4 %0, %4, %5, %6 offen\n\tbuffer_load_dwordx4 %1, %4, %5, %6 offen offset:64\n\t"
                 "buffer_load_dwordx4 %2, %4, %5, %6 offen offset:128\n\tbuffer_load_dwordx4 %3, %4, %5, %6 offen offset:192"
                 : "=&v"(xf0), "=&v"(xf1), "=&v"(xf2), "=&v"(xf3) : "v"(vo), "s"(sxa), "s"(so) : "memory");
  }
  const u32 vr0 = (lane >> 3) * 1024 + (((lane & 7) ^ (lane >> 4)) << 4);
  const u32 vr1 = (lane >> 3) * 1024 + (((lane & 7) ^ (4 + (lane >> 4))) << 4) + 7168;
  const u32 vt0 = (lane >> 3) * 1024 + (((lane & 7) ^ (((lane >> 4) & 1) << 2)) << 4);
  const u32 vt1 = vt0 + 7168;
  const u32 rot = ((j + g) & 1) * 4;
  auto qdma = [&](u32 c) {
    u32 kc = (c + rot) & 7, sl = (c & 3) * 16384 + w * 2048;
    dma2(sl, vr0, vr1, sxa, (g * 64 + w * 16) * 1024 + kc * 128);
    dma2(sl + 8192, vt0, vt1, swq, (kc * 64 + w * 16) * 1024 + j * 128);
  };
  qdma(0); qdma(1); qdma(2); qdma(3);
  asm volatile("s_waitcnt vmcnt(16)" : "+v"(ep), "+v"(xf0), "+v"(xf1), "+v"(xf2), "+v"(xf3)::"memory");
  ST(1);
  u32x2 tag;
  {
    u64 e = (((u64)__builtin_amdgcn_readfirstlane(ep.y) << 32) | (u32)__builtin_amdgcn_readfirstlane(ep.x)) + 1;
    tag.x = (u32)e; tag.y = (u32)(e >> 32);
  }
  {
    const u32 base = 65536 + w * 4096 + ((lane >> 4) * 8 + ((lane & 15) >> 2)) * 32 + (lane & 3) * 8;
    bf16x8 wf[4], xf[4] = {__builtin_bit_cast(bf16x8, xf0), __builtin_bit_cast(bf16x8, xf1), __builtin_bit_cast(bf16x8, xf2), __builtin_bit_cast(bf16x8, xf3)};
#pragma unroll
    for (int i = 0; i < 4; ++i) wf[i] = ldstr(base + i * 1024, base + i * 1024 + 128);
    f32x4 acc = {0, 0, 0, 0};
    if (j & 1) {
#pragma unroll
      for (int i = 0; i < 4; ++i) acc = mfma16(xf[i], wf[i], acc);
    } else {
#pragma unroll
      for (int i = 0; i < 4; ++i) acc = mfma16(wf[i], xf[i], acc);
    }
    *(LAS f32x4*)(size_t)(65536 + w * 4096 + lane * 16) = acc;
    asm volatile("s_waitcnt lgkmcnt(0)\n\ts_barrier" ::: "memory");
    f32x4 s = *(LAS f32x4*)(size_t)(65536 + lane * 16);
#pragma unroll
    for (int i = 1; i < 4; ++i) s += *(LAS f32x4*)(size_t)(65536 + i * 4096 + lane * 16);
    u32x2 pv = {pk2(s[0], s[1]), pk2(s[2], s[3])};
    const u32 tq = j >> 1, g4 = lane >> 4, c16 = lane & 15;
    u32 off = WS_KV + (g * 2 + (tq >> 1)) * 2048;
    if (j & 1) off += 1024 + (tq & 1) * 512 + ((g4 >> 1) * 16 + c16) * 16 + (g4 & 1) * 8;
    else off += ((g4 & 1) * 32 + (tq & 1) * 16 + c16) * 16 + (g4 >> 1) * 8;
    st8c(pv, w == 0 ? off : OOB, sws);
  }
  ST(2);
  f32x16 qacc = {0, 0, 0, 0, 0, 0, 0, 0, 0, 0, 0, 0, 0, 0, 0, 0};
  const u32 rrow = (th * 32 + r32) * 128, rsw = (r32 >> 1) & 7;
  const u32 q4 = (lane & 15) >> 2, p4 = lane & 3, blk = (lane >> 4) & 1;
  const u32 trb = q4 * 128 + (((hp * 4 + blk * 2 + (p4 >> 1)) ^ ((q4 >> 1) << 2)) << 4) + (p4 & 1) * 8;
  {
    auto qcomp = [&](u32 c) {
      const u32 sl = (c & 3) * 16384;
#pragma unroll
      for (u32 s = 0; s < 4; ++s) {
        bf16x8 xb = ldsr(sl + rrow + (((2 * s + h) ^ rsw) << 4));
        u32 ta = sl + 8192 + trb + h * 1024 + s * 2048;
        qacc = mfma32(ldstr(ta, ta + 512), xb, qacc);
      }
    };
    waitvl_bar<13>(); qcomp(0);
    waitvl_bar<9>(); qdma(4);
    if (lane < 32) { u32x4 o = {0x3f803f80u, 0x3f803f80u, 0x3f803f80u, 0x3f803f80u};
      *(LAS u32x4*)(size_t)(65536 + (w * 2 + (lane >> 4)) * 2048 + ((lane >> 3) & 1) * 512 + (lane & 7) * 16) = o; }
    qcomp(1);
    waitvl_bar<9>(); qdma(5); qcomp(2);
    waitvl_bar<9>(); qdma(6); qcomp(3);
    WAITV(12);
    st8c(tag, (w == 0 && lane == 0) ? WS_KVF + (g * 8 + j) * 8 : OOB, sws);
    ST(3);
    waitvl_bar<9>(); qdma(7); qcomp(4);
    waitvl_bar<9>(); qcomp(5);
    waitvl_bar<5>(); qcomp(6);
    waitvl_bar<0>(); qcomp(7);
  }
  const float qs = 0.25f * 1.44269504088896340736f;
  u32x4 qa, qb;
#pragma unroll
  for (int i = 0; i < 4; ++i) { qa[i] = pk2(qacc[2 * i] * qs, qacc[2 * i + 1] * qs); qb[i] = pk2(qacc[8 + 2 * i] * qs, qacc[9 + 2 * i] * qs); }
  ST(4);
  poll<1>(WS_KVF + (w * 64 + lane) * 8, true, tag, sws);
  waitvl_bar<0>();
  ST(5);
  if (b == 0) st8c(tag, (w == 0 && lane == 0) ? WS_EPA : OOB, sws);
  f32x16 oa, ob;
  {
    const u32x4 skv = mk_srd(a.ws + WS_KV, 131072), swo = mk_srd(a.wo, 512 * 1024);
    u32 so = ((xcd + 1) & 7) * 16384;
    const u32 vl = lane * 16 + w * 4096;
#pragma unroll
    for (u32 c = 0; c < 4; ++c) { dma4c(c * 16384 + w * 4096, vl, skv, so); so = (so + 16384) & 0x1ffff; }
    u32 ka = lane * 16;
    u32 va = r32 < 16 ? (h * 16 + r32) * 16 : 64512 + ((r32 >= 20 && r32 < 28) ? 64 : 0);
    const u32 vinc = r32 < 16 ? 16384 : 0;
    u32 d0 = vl, d1 = vl, d2 = vl, d3 = vl;
    const u32 wv0 = vt0 + w * 32768 + j * 128;
    const u32 wso = (g & 3) * 131072;
    u32 m0v = w * 4096;
    const u32 m0lim = w * 4096 + 65536;
    asm volatile(
        "s_mov_b32 s40, %[k0]\n\ts_mov_b32 s41, %[k1]\n\ts_mov_b32 s42, %[k2]\n\ts_mov_b32 s43, %[k3]\n\t"
        "s_mov_b32 s44, 0x4000\n\ts_mov_b32 s45, 0x1ffff\n\ts_mov_b32 s46, 0x4000\n\ts_mov_b32 s47, 0\n\t"
        "s_mov_b32 m0, %[m0v]\n\t"
        "v_mov_b32 v156, 0xdddddddd\n\t"
        "v_mov_b32 v88, 0\n\tv_mov_b32 v89, 0\n\tv_mov_b32 v90, 0\n\tv_mov_b32 v91, 0\n\tv_mov_b32 v92, 0\n\tv_mov_b32 v93, 0\n\tv_mov_b32 v94, 0\n\tv_mov_b32 v95, 0\n\t"
        "v_mov_b32 v152, 0\n\tv_mov_b32 v153, 0\n\tv_mov_b32 v154, 0\n\tv_mov_b32 v155, 0\n\t"
        "v_mov_b32 v96, 0\n\tv_mov_b32 v97, 0\n\tv_mov_b32 v98, 0\n\tv_mov_b32 v99, 0\n\tv_mov_b32 v100, 0\n\tv_mov_b32 v101, 0\n\tv_mov_b32 v102, 0\n\tv_mov_b32 v103, 0\n\t"
        "v_mov_b32 v104, 0\n\tv_mov_b32 v105, 0\n\tv_mov_b32 v106, 0\n\tv_mov_b32 v107, 0\n\tv_mov_b32 v108, 0\n\tv_mov_b32 v109, 0\n\tv_mov_b32 v110, 0\n\tv_mov_b32 v111, 0\n\t"
        "v_mov_b32 v112, 0\n\tv_mov_b32 v113, 0\n\tv_mov_b32 v114, 0\n\tv_mov_b32 v115, 0\n\tv_mov_b32 v116, 0\n\tv_mov_b32 v117, 0\n\tv_mov_b32 v118, 0\n\tv_mov_b32 v119, 0\n\t"
        "v_mov_b32 v120, 0\n\tv_mov_b32 v121, 0\n\tv_mov_b32 v122, 0\n\tv_mov_b32 v123, 0\n\tv_mov_b32 v124, 0\n\tv_mov_b32 v125, 0\n\tv_mov_b32 v126, 0\n\tv_mov_b32 v127, 0\n\t"
        "s_waitcnt vmcnt(12)\n\ts_barrier\n\t"
        "ds_read_b128 v[136:139], %[ka]\n\tds_read_b128 " VA0 ", %[va] offset:1024\n\tds_read_b128 " VA1 ", %[va] offset:1536\n\t"
        "s_setprio 0\n\t"
        WL QK("v[64:79]", "v[128:131]") "s_nop 11\n\t"
        "1:\n\t"
        IT(VA0, VA1, VB0, VB1, 2048, 3072, 3584, WL, "", "")
        IT(VB0, VB1, VA0, VA1, 4096, 5120, 5632, WL, "", "")
        IT(VA0, VA1, VB0, VB1, 6144, 7168, 7680, WL, "", "")
        IT(VB0, VB1, VA0, VA1, 8192, 9216, 9728, WL, "", "")
        IT(VA0, VA1, VB0, VB1, 10240, 11264, 11776, WL, "", "")
        IT(VB0, VB1, VA0, VA1, 12288, 13312, 13824, WL, "", "")
        IT(VA0, VA1, VB0, VB1, 14336, 15360, 15872, "s_waitcnt vmcnt(8) lgkmcnt(0)\n\t",
           "s_barrier\n\t"
           "buffer_load_dwordx4 %[d0], s[40:43], %[so] offen sc1 lds\n\tbuffer_load_dwordx4 %[d1], s[40:43], %[so] offen offset:1024 sc1 lds\n\t"
           "v_add_u16 %[ka], s46, %[ka]\n\tv_add_u16 %[va], %[vinc], %[va]\n\t",
           "buffer_load_dwordx4 %[d2], s[40:43], %[so] offen offset:2048 sc1 lds\n\tbuffer_load_dwordx4 %[d3], s[40:43], %[so] offen offset:3072 sc1 lds\n\t")
        IT(VB0, VB1, VA0, VA1, 0, 1024, 1536, WL,
           "s_add_u32 %[so], %[so], s44\n\ts_and_b32 %[so], %[so], s45\n\t",
           "s_add_u32 m0, m0, s46\n\ts_cmp_lg_u32 m0, %[m0lim]\n\t")
        "s_cbranch_scc1 1b\n\t"
        "s_cmp_lg_u32 s47, 0\n\ts_cbranch_scc1 2f\n\t"
        "s_mov_b32 s47, 1\n\ts_mov_b32 s40, %[w0]\n\ts_mov_b32 s41, %[w1]\n\ts_mov_b32 s42, %[w2]\n\t"
        "s_mov_b32 s44, 0x20000\n\ts_mov_b32 s45, 0x7ffff\n\ts_mov_b32 %[so], %[wso]\n\ts_mov_b32 m0, %[m0v]\n\t"
        "v_mov_b32 %[d0], %[wv0]\n\tv_add_u32 %[d1], 7168, %[wv0]\n\tv_add_u32 %[d2], 14336, %[wv0]\n\tv_add_u32 %[d3], 21504, %[wv0]\n\t"
        "s_branch 1b\n\t"
        "2:\n\t"
        "s_setprio 3\n\ts_nop 0\n\t"
        SM(OB, VB1, "v[88:95]")
        "s_nop 15\n\t"
        : "=&{v[96:111]}"(oa), "=&{v[112:127]}"(ob), [ka] "+v"(ka), [va] "+v"(va), [so] "+s"(so),
          [d0] "+v"(d0), [d1] "+v"(d1), [d2] "+v"(d2), [d3] "+v"(d3)
        : "{v[128:131]}"(qa), "{v[132:135]}"(qb), [vinc] "v"(vinc), [wv0] "v"(wv0), [wso] "s"(wso), [m0v] "s"(m0v), [m0lim] "s"(m0lim),
          [k0] "s"(skv.x), [k1] "s"(skv.y), [k2] "s"(skv.z), [k3] "s"(skv.w), [w0] "s"(swo.x), [w1] "s"(swo.y), [w2] "s"(swo.z)
        : "memory", "scc", "s40", "s41", "s42", "s43", "s44", "s45", "s46", "s47",
          "v64", "v65", "v66", "v67", "v68", "v69", "v70", "v71", "v72", "v73", "v74", "v75", "v76", "v77", "v78", "v79",
          "v80", "v81", "v82", "v83", "v84", "v85", "v86", "v87", "v88", "v89", "v90", "v91", "v92", "v93", "v94", "v95",
          "v136", "v137", "v138", "v139", "v140", "v141", "v142", "v143", "v144", "v145", "v146", "v147",
          "v148", "v149", "v150", "v151", "v152", "v153", "v154", "v155", "v156");
  }
  ST(6);
  {
    float ma = 0.f;
#pragma unroll
    for (int i = 0; i < 8; ++i) ma += __builtin_fabsf(oa[i]) * 0.0625f + __builtin_fabsf(ob[i]) * 0.0625f;
    const bool ok = oa[8] >= 0x1p-64f && oa[8] <= 0x1p100f && ob[8] >= 0x1p-64f && ob[8] <= 0x1p100f && ma <= 3.0e38f;
    if (__builtin_amdgcn_ballot_w64(!ok) != 0) {
      const __amdgpu_buffer_rsrc_t rk = __builtin_amdgcn_make_buffer_rsrc(a.ws + WS_KV, 0, 131072, 0x20000);
      const u32x4 ones = {0x3f803f80u, 0x3f803f80u, 0x3f803f80u, 0x3f803f80u};
#pragma unroll 1
      for (int t = 0; t < 2; ++t) {
        const bf16x8 q = __builtin_bit_cast(bf16x8, t ? qb : qa);
        f32x16 z = {0, 0, 0, 0, 0, 0, 0, 0, 0, 0, 0, 0, 0, 0, 0, 0};
        float m = -__builtin_inff();
#pragma unroll 1
        for (u32 kt = 0; kt < 64; ++kt) {
          u32x4 kf = __builtin_amdgcn_raw_buffer_load_b128(rk, lane * 16, kt * 2048, 16);
          f32x16 s = mfma32(__builtin_bit_cast(bf16x8, kf), q, z);
#pragma unroll
          for (int i = 0; i < 16; ++i) m = __builtin_fmaxf(m, s[i]);
        }
        m = __builtin_fmaxf(m, __shfl_xor(m, 32));
        f32x16 o = z;
#pragma unroll 1
        for (u32 kt = 0; kt < 64; ++kt) {
          u32x4 kf = __builtin_amdgcn_raw_buffer_load_b128(rk, lane * 16, kt * 2048, 16);
          u32x4 v0 = __builtin_amdgcn_raw_buffer_load_b128(rk, (h * 16 + (r32 & 15)) * 16 + 1024, kt * 2048, 16);
          u32x4 v1 = __builtin_amdgcn_raw_buffer_load_b128(rk, (h * 16 + (r32 & 15)) * 16 + 1536, kt * 2048, 16);
          if (r32 >= 16) { v0 = ones; v1 = ones; }
          f32x16 s = mfma32(__builtin_bit_cast(bf16x8, kf), q, z);
          f32x8 p0, p1;
#pragma unroll
          for (int i = 0; i < 8; ++i) { p0[i] = __builtin_amdgcn_exp2f(s[i] - m); p1[i] = __builtin_amdgcn_exp2f(s[8 + i] - m); }
          o = __builtin_amdgcn_smfmac_f32_32x32x32_bf16(__builtin_bit_cast(bf16x8, v0), __builtin_bit_cast(bf16x16, p0), o, (int)0xdddddddd, 0, 0);
          o = __builtin_amdgcn_smfmac_f32_32x32x32_bf16(__builtin_bit_cast(bf16x8, v1), __builtin_bit_cast(bf16x16, p1), o, (int)0xdddddddd, 0, 0);
        }
        if (t) ob = o; else oa = o;
      }
    }
  }
  {
    const float ia = __builtin_amdgcn_rcpf(oa[8]), ib = __builtin_amdgcn_rcpf(ob[8]);
    u32x4 pa, pb;
#pragma unroll
    for (int i = 0; i < 4; ++i) { pa[i] = pk2(oa[2 * i] * ia, oa[2 * i + 1] * ia); pb[i] = pk2(ob[2 * i] * ib, ob[2 * i + 1] * ib); }
    const u32 off = WS_O + g * 65536 + ((j * 4 + hp * 2) * 2 + th) * 1024 + lane * 16;
    st16c(pa, off, sws);
    st16c(pb, off + 2048, sws);
  }
  waitvl_bar<2>();
  bf16x8 wf[32];
#pragma unroll
  for (u32 i = 0; i < 32; ++i) { u32 ta = trb + h * 512 + i * 2048; wf[i] = ldstr(ta, ta + 1024); }
  waitvl_bar<0>();
  st8c(tag, (w == 0 && lane == 0) ? WS_OF + g * 256 + j * 8 : OOB, sws);
  ST(7);
  poll<1>(WS_OF + g * 256 + (lane & 7) * 8, true, tag, sws);
  ST(8);
#pragma unroll
  for (u32 c = 0; c < 4; ++c) dma4c(c * 16384 + w * 4096, lane * 16, sws, WS_O + g * 65536 + (((g & 3) + c) & 3) * 16384 + w * 4096);
  f32x16 acc = {0, 0, 0, 0, 0, 0, 0, 0, 0, 0, 0, 0, 0, 0, 0, 0};
  auto ocomp = [&](u32 c) {
#pragma unroll
    for (u32 i = 0; i < 8; ++i) acc = mfma32(ldsr(c * 16384 + i * 2048 + th * 1024 + lane * 16), wf[c * 8 + i], acc);
  };
  waitvl_bar<12>(); ocomp(0);
  waitvl_bar<8>(); ocomp(1);
  waitvl_bar<4>(); ocomp(2);
  waitvl_bar<0>(); ocomp(3);
  ST(9);
  float* op = a.out + (size_t)(g * 64 + th * 32 + h * 4) * 1024 + j * 64 + hp * 32 + r32;
#pragma unroll
  for (int i = 0; i < 16; ++i) op[((i & 3) + 8 * (i >> 2)) * 1024] = acc[i];
  ST(10);
  {
    auto T = [&](int i, int k) { return (float)(stv[i] - stv[k]) * 0.01f; };
    float d0 = 0.f, d1 = 0.f;
    if (DIAG == 1) { d0 = T(1, 0); d1 = T(5, 0); }
    if (DIAG == 2) { d0 = T(6, 5); d1 = (float)(stc[6] - stc[5]) * 0.001f; }
    if (DIAG == 3) { d0 = T(10, 6); d1 = T(10, 0); }
    if (DIAG == 4) { u32 la = __builtin_amdgcn_s_getreg(6 | (31 << 11)); d0 = (la & 0xfff) ? 11.f : 1.f; d1 = 32.f - T(5, 0); }
    if (DIAG == 5) { d0 = T(3, 0); d1 = T(4, 0); }
    if (DIAG == 6) { d0 = 32.f - T(6, 5); d1 = 8.f - T(1, 0); }
    asm volatile("s_waitcnt vmcnt(0)" ::: "memory");
    if (w == 0 && lane == 0 && T(10, 0) > DIAGTHR) { op[0] = d0; op[512] = d1; }
  }
}

DEVI void cmul(float& r, float& i, float ar, float ai, float br, float bi) { r = ar * br - ai * bi; i = ar * bi + ai * br; }
DEVI void cstep(float& r, float& i, float ar, float ai, float ur, float ui) {
  float nr = __builtin_fmaf(r, ar, __builtin_fmaf(-i, ai, ur)), ni = __builtin_fmaf(i, ar, __builtin_fmaf(r, ai, ui));
  r = nr; i = ni;
}

DEVI void role_b(const Args& a, u32 b, u32 w, u32 lane) {
  const u32 xcd = b & 7, idx = b >> 3, g = xcd * 4 + (idx >> 3), j = idx & 7;
  const u32 r32 = lane & 31, h = lane >> 5, th = w & 1, ph = w >> 1;
  const u32x4 sws = mk_srd(a.ws, WS_END), sxb = mk_srd(a.xb, 2048 * 1024);
  __builtin_amdgcn_s_setprio(2);
  STDECL;
  ST(0);
  u32x2 ep;
  u32 plr, pli, pdt;
  {
    const u32x4 s0 = mk_srd(a.lre, 1024), s1 = mk_srd(a.lim, 1024), s2 = mk_srd(a.ldt, 1024);
    const u32 vo = (j * 64 + ph * 32 + r32) * 2;
    asm volatile("buffer_load_dwordx2 %0, off, %4, 0 offset:256 sc1\n\tbuffer_load_ushort %1, %8, %5, 0 offen\n\t"
                 "buffer_load_ushort %2, %8, %6, 0 offen\n\tbuffer_load_ushort %3, %8, %7, 0 offen"
                 : "=&v"(ep), "=&v"(plr), "=&v"(pli), "=&v"(pdt) : "s"(sws), "s"(s0), "s"(s1), "s"(s2), "v"(vo) : "memory");
  }
  const u32 vr0 = (lane >> 3) * 1024 + (((lane & 7) ^ (lane >> 4)) << 4);
  const u32 vr1 = (lane >> 3) * 1024 + (((lane & 7) ^ (4 + (lane >> 4))) << 4) + 7168;
  const u32 rsw = (r32 >> 1) & 7;
  f32x16 ur = {0, 0, 0, 0, 0, 0, 0, 0, 0, 0, 0, 0, 0, 0, 0, 0}, ui = ur;
  float ar, ai, gr, gi;
  u32x2 tag;
  {
    const u32x4 sbr = mk_srd(a.bre, 512 * 1024), sbi = mk_srd(a.bim, 512 * 1024);
    const u32 xrow = (16 * h + ((lane >> 3) & 3)) * 1024;
    const u32 vx0 = xrow + (((lane & 7) ^ (lane >> 4)) << 4);
    const u32 vx1 = xrow + (((lane & 7) ^ (4 + (lane >> 4))) << 4) + 3072;
    const u32 rot = (j + (g & 3) * 2) & 7;
    auto bdma = [&](u32 c) {
      u32 kc = (c + rot) & 7, sl = (c % 3) * 24576 + w * 2048;
      dma2(sl, vx0, vx1, sxb, (g * 64 + ph * 32 + th * 8) * 1024 + kc * 128);
      dma2(sl + 8192, vr0, vr1, sbr, (j * 64 + w * 16) * 1024 + kc * 128);
      dma2(sl + 16384, vr0, vr1, sbi, (j * 64 + w * 16) * 1024 + kc * 128);
    };
    bdma(0); bdma(1); bdma(2);
    asm volatile("s_waitcnt vmcnt(18)" : "+v"(ep), "+v"(plr), "+v"(pli), "+v"(pdt)::"memory");
    {
      u64 e = (((u64)__builtin_amdgcn_readfirstlane(ep.y) << 32) | (u32)__builtin_amdgcn_readfirstlane(ep.x)) + 1;
      tag.x = (u32)e; tag.y = (u32)(e >> 32);
    }
    {
      const float lr = bf2f(plr), li = bf2f(pli);
      const float dt = bf2f(pk2(expf(bf2f(pdt)), 0.f) & 0xffffu);
      const float e = expf(dt * lr);
      float sn, cs;
      sincosf(dt * li, &sn, &cs);
      ar = e * cs; ai = e * sn;
      const float nr = ar - 1.f, ni = ai;
      const bool sw = __builtin_fabsf(lr) >= __builtin_fabsf(li);
      const float big = sw ? lr : li, sm = sw ? li : lr, rt = sm / big, den = big + sm * rt;
      const float x = sw ? nr : ni, y = sw ? ni : nr;
      gr = 2.f * (x + y * rt) / den;
      gi = 2.f * (y - x * rt) / den;
      gi = sw ? gi : -gi;
      ai = -ai;
    }
    auto bcomp = [&](u32 c) {
      const u32 sl = (c % 3) * 24576;
#pragma unroll
      for (u32 s = 0; s < 4; ++s) {
        const u32 sw = ((2 * s + h) ^ rsw) << 4;
        bf16x8 xf = ldsr(sl + (th * 32 + r32) * 128 + sw);
        ur = mfma32(xf, ldsr(sl + 8192 + (ph * 32 + r32) * 128 + sw), ur);
        ui = mfma32(xf, ldsr(sl + 16384 + (ph * 32 + r32) * 128 + sw), ui);
      }
    };
    waitvl_bar<12>(); bcomp(0);
    waitvl_bar<6>(); bdma(3); bcomp(1);
    waitvl_bar<6>(); bdma(4); bcomp(2);
    waitvl_bar<6>(); bdma(5); bcomp(3);
    waitvl_bar<6>(); bdma(6); bcomp(4);
    waitvl_bar<6>(); bdma(7); bcomp(5);
    waitvl_bar<6>(); bcomp(6);
    waitvl_bar<0>(); bcomp(7);
  }
  ST(1);
  float er = 0.f, ei = 0.f;
#pragma unroll
  for (int i = 0; i < 16; ++i) {
    float xr = gr * ur[i] - gi * ui[i], xi = -(gr * ui[i] + gi * ur[i]);
    ur[i] = xr; ui[i] = xi;
    cstep(er, ei, ar, ai, xr, xi);
  }
  float a16r = ar, a16i = ai;
#pragma unroll
  for (int i = 0; i < 4; ++i) cmul(a16r, a16i, a16r, a16i, a16r, a16i);
  const u32 eb = 73728 + (ph * 32 + r32) * 8;
  { f32x2 e2 = {er, ei}; *(LAS f32x2*)(size_t)(eb + (th * 2 + h) * 512) = e2; }
  waitvl_bar<0>();
  f32x2 es[4];
#pragma unroll
  for (int s = 0; s < 4; ++s) es[s] = *(LAS f32x2*)(size_t)(eb + s * 512);
  if (th == 0) {
    float tr = es[0].x, ti = es[0].y;
#pragma unroll
    for (int s = 1; s < 4; ++s) cstep(tr, ti, a16r, a16i, es[s].x, es[s].y);
    u32x2 ev = {__builtin_bit_cast(u32, tr), __builtin_bit_cast(u32, ti)};
    st8c(ev, h == 0 ? WS_E + (j * 32 + g) * 512 + (ph * 32 + r32) * 8 : OOB, sws);
    WAITV(0);
    st8c(tag, lane == 0 ? WS_EF + ((j * 2 + ph) * 32 + g) * 8 : OOB, sws);
  }
  ST(2);
  float a64r = a16r, a64i = a16i;
  cmul(a64r, a64i, a64r, a64i, a64r, a64i);
  cmul(a64r, a64i, a64r, a64i, a64r, a64i);
  float akr = a64r, aki = a64i;
#pragma unroll
  for (int i = 0; i < 4; ++i) cmul(akr, aki, akr, aki, akr, aki);
  poll<4>(WS_EF + ((j * 2 + ph) * 32 + r32) * 8, r32 < g, tag, sws);
  ST(3);
  float cr = 0.f, ci = 0.f;
  {
    const __amdgpu_buffer_rsrc_t re = __builtin_amdgcn_make_buffer_rsrc(a.ws + WS_E + j * 16384, 0, 16384, 0x20000);
    u32x2 ev[16];
#pragma unroll
    for (int m = 0; m < 16; ++m) {
      int gp = (int)g - 1 - 16 * (int)h - m;
      ev[m] = __builtin_amdgcn_raw_buffer_load_b64(re, gp >= 0 ? (u32)gp * 512 + (ph * 32 + r32) * 8 : OOB, 0, 16);
    }
#pragma unroll
    for (int m = 15; m >= 0; --m) { const f32x2 e2 = __builtin_bit_cast(f32x2, ev[m]); cstep(cr, ci, a64r, a64i, e2.x, e2.y); }
    float orr = __shfl_xor(cr, 32), oi = __shfl_xor(ci, 32);
    float lr_ = h ? orr : cr, li_ = h ? oi : ci, hr_ = h ? cr : orr, hi_ = h ? ci : oi;
    if (g > 16) { cstep(hr_, hi_, akr, aki, lr_, li_); cr = hr_; ci = hi_; } else { cr = lr_; ci = li_; }
#pragma unroll
    for (u32 s = 0; s < 3; ++s) {
      float tr = cr, ti = ci;
      cstep(tr, ti, a16r, a16i, es[s].x, es[s].y);
      const bool use = s < th * 2 + h;
      cr = use ? tr : cr; ci = use ? ti : ci;
    }
  }
#pragma unroll
  for (int i = 0; i < 16; ++i) { cstep(cr, ci, ar, ai, ur[i], ui[i]); ur[i] = cr; ui[i] = ci; }
  {
    const u32 off = WS_H + (g * 8 + j) * 16384 + (ph * 2 + th) * 2048 + h * 512;
#pragma unroll
    for (u32 i = 0; i < 2; ++i) {
      u32x4 pr, pi;
#pragma unroll
      for (int e = 0; e < 4; ++e) { pr[e] = pk2(ur[8 * i + 2 * e], ur[8 * i + 2 * e + 1]); pi[e] = pk2(ui[8 * i + 2 * e], ui[8 * i + 2 * e + 1]); }
      const u32 o = off + i * 1024 + ((r32 ^ ((2 * i + h) << 2)) << 4);
      st16c(pr, o, sws);
      st16c(pi, o + 8192, sws);
    }
  }
  waitvl_bar<0>();
  st8c(tag, (w == 0 && lane == 0) ? WS_HF + g * 256 + j * 8 : OOB, sws);
  ST(4);
  const u32 col = j * 64 + ph * 32 + r32;
  f32x16 acc;
  {
    const float dk = bf2f(a.dsk[col]);
    const u16* xp = a.xb + (size_t)(g * 64 + th * 32 + h * 4) * 512 + col;
#pragma unroll
    for (int i = 0; i < 16; ++i) acc[i] = bf2f(xp[((i & 3) + 8 * (i >> 2)) * 512]) * dk;
    asm volatile("" : "+v"(acc));
  }
  poll<4>(WS_HF + g * 256 + (lane & 7) * 8, true, tag, sws);
  ST(5);
  if (g == 31 && j == 0) st8c(tag, (w == 0 && lane == 0) ? WS_EPB : OOB, sws);
  {
    const u32x4 scr = mk_srd(a.cre, 512 * 1024), sci = mk_srd(a.cim, 512 * 1024);
    auto cdma = [&](u32 c) {
      u32 jj = ((c >> 1) + j) & 7, sl = (c % 5) * 16384 + w * 2048;
      dma2c(sl, lane * 16, sws, WS_H + (g * 8 + jj) * 16384 + (c & 1) * 8192 + w * 2048);
      dma2(sl + 8192, vr0, vr1, (c & 1) ? sci : scr, (j * 64 + w * 16) * 1024 + jj * 128);
    };
    const u32 q4 = (lane & 15) >> 2, p4 = lane & 3, blk = (lane >> 4) & 1;
    const u32 tb0 = (th * 2 + (p4 >> 1)) * 1024 + blk * 512 + (((8 * h + q4) ^ ((2 * (p4 >> 1) + blk) << 2)) << 4) + (p4 & 1) * 8;
    const u32 tb1 = tb0 ^ 64;
    auto ccomp = [&](u32 c) {
      const u32 sl = (c % 5) * 16384;
#pragma unroll
      for (u32 s = 0; s < 4; ++s) {
        const u32 o = sl + (s >> 1) * 4096 + (s & 1) * 256;
        acc = mfma32(ldstr(tb0 + o, tb1 + o), ldsr(sl + 8192 + (ph * 32 + r32) * 128 + (((2 * s + h) ^ rsw) << 4)), acc);
      }
    };
    cdma(0); cdma(1); cdma(2); cdma(3); cdma(4);
    waitvl_bar<16>(); ccomp(0);
#pragma unroll
    for (u32 c = 1; c < 12; ++c) { waitvl_bar<12>(); cdma(c + 4); ccomp(c); }
    waitvl_bar<12>(); ccomp(12);
    waitvl_bar<8>(); ccomp(13);
    waitvl_bar<4>(); ccomp(14);
    waitvl_bar<0>(); ccomp(15);
  }
  ST(6);
  float* op = a.out + (size_t)(g * 64 + th * 32 + h * 4) * 1024 + 512 + col;
#pragma unroll
  for (int i = 0; i < 16; ++i) op[((i & 3) + 8 * (i >> 2)) * 1024] = acc[i];
  ST(7);
  STFLUSH;
}

__global__ void __launch_bounds__(256, 2) fused_kernel(Args a) {
  __shared__ __attribute__((aligned(16))) char lds[81920];
  asm volatile("" ::"v"((u32)(size_t)(LAS char*)lds));
  const u32 lane = threadIdx.x & 63, w = __builtin_amdgcn_readfirstlane(threadIdx.x >> 6), b = blockIdx.x;
  if (b < 256) role_a(a, b, w, lane); else {
    role_b(a, b - 256, w, lane);
  }
}

extern "C" void kernel_launch(void* const* d_in, const int* in_sizes, int n_in, void* d_out, int out_size, void* d_ws,
                              size_t ws_size, hipStream_t stream) {
  Args a;
  a.xa = (const u16*)d_in[0]; a.wq = (const u16*)d_in[1]; a.wk = (const u16*)d_in[2]; a.wv = (const u16*)d_in[3];
  a.wo = (const u16*)d_in[4]; a.xb = (const u16*)d_in[5]; a.lre = (const u16*)d_in[6]; a.lim = (const u16*)d_in[7];
  a.bre = (const u16*)d_in[8]; a.bim = (const u16*)d_in[9]; a.cre = (const u16*)d_in[10]; a.cim = (const u16*)d_in[11];
  a.ldt = (const u16*)d_in[12]; a.dsk = (const u16*)d_in[13];
  a.out = (float*)d_out; a.ws = (char*)d_ws;
  fused_kernel<<<512, 256, 0, stream>>>(a);
}
```

```cpp
#define STAMP
#define DIAG 7
#define DIAGTHR 2.5f
#include <hip/hip_runtime.h>
typedef unsigned short u16;
typedef unsigned u32;
typedef unsigned long long u64;
typedef float f32x2 __attribute__((ext_vector_type(2)));
typedef float f32x4 __attribute__((ext_vector_type(4)));
typedef float f32x8 __attribute__((ext_vector_type(8)));
typedef float f32x16 __attribute__((ext_vector_type(16)));
typedef __bf16 bf16x2 __attribute__((ext_vector_type(2)));
typedef __bf16 bf16x8 __attribute__((ext_vector_type(8)));
typedef __bf16 bf16x16 __attribute__((ext_vector_type(16)));
typedef short s16x4 __attribute__((ext_vector_type(4)));
typedef u32 u32x2 __attribute__((ext_vector_type(2)));
typedef u32 u32x4 __attribute__((ext_vector_type(4)));
#define LAS __attribute__((address_space(3)))
#define DEVI __device__ __forceinline__

constexpr u32 WS_EPA = 0, WS_EPB = 256;
constexpr u32 WS_KVF = 4096;
constexpr u32 WS_EF = 8192;
constexpr u32 WS_HF = 16384;
constexpr u32 WS_OF = 32768;
constexpr u32 WS_KV = 65536;
constexpr u32 WS_E = 262144;
constexpr u32 WS_O = 524288;
constexpr u32 WS_H = WS_O + 2097152;
constexpr u32 WS_END = WS_H + 4194304;
constexpr u32 WS_DBG = WS_END;
constexpr u32 OOB = 0x80000000u;

struct Args {
  const u16 *xa, *wq, *wk, *wv, *wo, *xb, *lre, *lim, *bre, *bim, *cre, *cim, *ldt, *dsk;
  float* out;
  char* ws;
};

DEVI u32x4 mk_srd(const void* p, u32 bytes) {
  u64 a = (u64)p;
  u32x4 r = {(u32)a, (u32)(a >> 32) & 0xffffu, bytes, 0x20000u};
  return r;
}
DEVI void dma2(u32 m0v, u32 v0, u32 v1, u32x4 srd, u32 soff) {
  asm volatile("s_mov_b32 m0, %0\n\ts_nop 0\n\tbuffer_load_dwordx4 %1, %3, %4 offen lds\n\tbuffer_load_dwordx4 %2, %3, %4 offen offset:1024 lds"
               ::"s"(m0v), "v"(v0), "v"(v1), "s"(srd), "s"(soff) : "memory");
}
DEVI void dma2c(u32 m0v, u32 v0, u32x4 srd, u32 soff) {
  asm volatile("s_mov_b32 m0, %0\n\ts_nop 0\n\tbuffer_load_dwordx4 %1, %2, %3 offen sc1 lds\n\tbuffer_load_dwordx4 %1, %2, %3 offen offset:1024 sc1 lds"
               ::"s"(m0v), "v"(v0), "s"(srd), "s"(soff) : "memory");
}
DEVI void dma4(u32 m0v, u32 v0, u32x4 srd, u32 soff) {
  asm volatile("s_mov_b32 m0, %0\n\ts_nop 0\n\tbuffer_load_dwordx4 %1, %2, %3 offen lds\n\tbuffer_load_dwordx4 %1, %2, %3 offen offset:1024 lds\n\t"
               "buffer_load_dwordx4 %1, %2, %3 offen offset:2048 lds\n\tbuffer_load_dwordx4 %1, %2, %3 offen offset:3072 lds"
               ::"s"(m0v), "v"(v0), "s"(srd), "s"(soff) : "memory");
}
DEVI void dma4c(u32 m0v, u32 v0, u32x4 srd, u32 soff) {
  asm volatile("s_mov_b32 m0, %0\n\ts_nop 0\n\tbuffer_load_dwordx4 %1, %2, %3 offen sc1 lds\n\tbuffer_load_dwordx4 %1, %2, %3 offen offset:1024 sc1 lds\n\t"
               "buffer_load_dwordx4 %1, %2, %3 offen offset:2048 sc1 lds\n\tbuffer_load_dwordx4 %1, %2, %3 offen offset:3072 sc1 lds"
               ::"s"(m0v), "v"(v0), "s"(srd), "s"(soff) : "memory");
}
DEVI void st8c(u32x2 v, u32 voff, u32x4 srd) {
  asm volatile("buffer_store_dwordx2 %0, %1, %2, 0 offen sc1" ::"v"(v), "v"(voff), "s"(srd) : "memory");
}
DEVI void st16c(u32x4 v, u32 voff, u32x4 srd) {
  asm volatile("buffer_store_dwordx4 %0, %1, %2, 0 offen sc1\n\ts_nop 1" ::"v"(v), "v"(voff), "s"(srd) : "memory");
}
#define WAITV(n) asm volatile("s_waitcnt vmcnt(" #n ")" ::: "memory")
#define WAITVL(n) asm volatile("s_waitcnt vmcnt(" #n ") lgkmcnt(0)" ::: "memory")
#define BAR() asm volatile("s_barrier" ::: "memory")
template <int N> DEVI void waitvl_bar() { asm volatile("s_waitcnt vmcnt(%0) lgkmcnt(0)\n\ts_barrier" ::"i"(N) : "memory"); }

template <int SLEEP> DEVI void poll(u32 voff, bool need, u32x2 tag, u32x4 srd) {
  for (u32 it = 0; it < (1u << 18); ++it) {
    u32x2 f;
    asm volatile("buffer_load_dwordx2 %0, %1, %2, 0 offen sc1\n\ts_waitcnt vmcnt(0)" : "=&v"(f) : "v"(voff), "s"(srd) : "memory");
    bool bad = need && (f.x != tag.x || f.y != tag.y);
    if (__builtin_amdgcn_ballot_w64(bad) == 0) break;
    __builtin_amdgcn_s_sleep(SLEEP);
  }
}
DEVI u32 pk2(float a, float b) {
  f32x2 x = {a, b};
  return __builtin_bit_cast(u32, __builtin_convertvector(x, bf16x2));
}
DEVI float bf2f(u32 v) { return __builtin_bit_cast(float, v << 16); }
DEVI bf16x8 ldsr(u32 a) { return *(const LAS bf16x8*)(size_t)a; }
DEVI bf16x8 ldstr(u32 a0, u32 a1) {
  s16x4 x = __builtin_amdgcn_ds_read_tr16_b64_v4i16((LAS s16x4*)(size_t)a0);
  s16x4 y = __builtin_amdgcn_ds_read_tr16_b64_v4i16((LAS s16x4*)(size_t)a1);
  return __builtin_bit_cast(bf16x8, __builtin_shufflevector(x, y, 0, 1, 2, 3, 4, 5, 6, 7));
}
DEVI f32x16 mfma32(bf16x8 a, bf16x8 b, f32x16 c) { return __builtin_amdgcn_mfma_f32_32x32x16_bf16(a, b, c, 0, 0, 0); }
DEVI f32x4 mfma16(bf16x8 a, bf16x8 b, f32x4 c) { return __builtin_amdgcn_mfma_f32_16x16x32_bf16(a, b, c, 0, 0, 0); }

#define STDECL u32 stv[12] = {0,0,0,0,0,0,0,0,0,0,0,0}, stc[12] = {0,0,0,0,0,0,0,0,0,0,0,0}
#define ST(i) do { u64 x_, y_; asm volatile("s_memrealtime %0\n\ts_memtime %1\n\ts_waitcnt lgkmcnt(0)" : "=s"(x_), "=s"(y_) :: "memory"); stv[i] = (u32)x_ | 1; stc[i] = (u32)y_; } while (0)
#define STC(v) asm volatile("s_memtime %0\n\ts_waitcnt lgkmcnt(0)" : "=s"(v) :: "memory")
#define STFLUSH do { if (lane == 0 && w == 0) for (int i_ = 0; i_ < 12; ++i_) ((u64*)(a.ws + WS_DBG))[blockIdx.x * 16 + i_] = stv[i_] | ((u64)stc[i_] << 32); } while (0)

#define EX(r) "v_exp_f32 v" #r ", v" #r "\n\t"
#define SM(o, vv, p) "v_smfmac_f32_32x32x32_bf16 " o ", " vv ", " p ", v156\n\t"
#define OA "v[96:111]"
#define OB "v[112:127]"
#define QK(s, q) "v_mfma_f32_32x32x16_bf16 " s ", v[136:139], " q ", 0\n\t"
#define IT(X0, X1, Y0, Y1, KO, V0O, V1O, WT, E1, E2)                                        \
  EX(64) EX(65) SM(OB, Y1, "v[88:95]")                                                      \
  "ds_read_b128 " Y0 ", %[va] offset:" #V0O "\n\tds_read_b128 " Y1 ", %[va] offset:" #V1O "\n\t" \
  EX(66) EX(67) EX(68) QK("v[80:95]", "v[132:135]")                                         \
  "ds_read_b128 v[136:139], %[ka] offset:" #KO "\n\t"                                       \
  EX(69) EX(70) EX(71) EX(72) EX(73) SM(OA, X0, "v[64:71]")                                 \
  EX(74) EX(75) EX(76) EX(77) EX(78) EX(79) EX(80) EX(81) SM(OA, X1, "v[72:79]")            \
  EX(82) EX(83) EX(84) WT QK("v[64:79]", "v[128:131]") E1                                   \
  EX(85) EX(86) EX(87) EX(88) EX(89) SM(OB, X0, "v[80:87]") E2                              \
  EX(90) EX(91) EX(92) EX(93) EX(94) EX(95)
#define VA0 "v[140:143]"
#define VA1 "v[144:147]"
#define VB0 "v[148:151]"
#define VB1 "v[152:155]"
#define WL "s_waitcnt lgkmcnt(0)\n\t"

DEVI void role_a(const Args& a, u32 b, u32 w, u32 lane, u64 tf) {
  const u32 xcd = b & 7, idx = b >> 3, g = xcd * 4 + (idx >> 3), j = idx & 7;
  const u32 r32 = lane & 31, h = lane >> 5, th = w & 1, hp = w >> 1;
  const u32x4 sws = mk_srd(a.ws, WS_END), sxa = mk_srd(a.xa, 2048 * 1024), swq = mk_srd(a.wq, 512 * 1024);
  const u32x4 swkv = mk_srd((j & 1) ? a.wv : a.wk, 512 * 32);
  __builtin_amdgcn_s_setprio(3);
  STDECL;
  ST(0);
  u32x2 ep;
  asm volatile("buffer_load_dwordx2 %0, off, %1, 0 sc1" : "=v"(ep) : "s"(sws) : "memory");
  dma4(65536 + w * 4096, lane * 16, swkv, w * 4096);
  u32x4 xf0, xf1, xf2, xf3;
  {
    u32 vo = (lane & 15) * 1024 + (lane >> 4) * 16, so = (g * 64 + (j >> 1) * 16) * 1024 + w * 256;
    asm volatile("buffer_load_dwordx4 %0, %4, %5, %6 offen\n\tbuffer_load_dwordx4 %1, %4, %5, %6 offen offset:64\n\t"
                 "buffer_load_dwordx4 %2, %4, %5, %6 offen offset:128\n\tbuffer_load_dwordx4 %3, %4, %5, %6 offen offset:192"
                 : "=&v"(xf0), "=&v"(xf1), "=&v"(xf2), "=&v"(xf3) : "v"(vo), "s"(sxa), "s"(so) : "memory");
  }
  const u32 vr0 = (lane >> 3) * 1024 + (((lane & 7) ^ (lane >> 4)) << 4);
  const u32 vr1 = (lane >> 3) * 1024 + (((lane & 7) ^ (4 + (lane >> 4))) << 4) + 7168;
  const u32 vt0 = (lane >> 3) * 1024 + (((lane & 7) ^ (((lane >> 4) & 1) << 2)) << 4);
  const u32 vt1 = vt0 + 7168;
  const u32 rot = ((j + g) & 1) * 4;
  auto qdma = [&](u32 c) {
    u32 kc = (c + rot) & 7, sl = (c & 3) * 16384 + w * 2048;
    dma2(sl, vr0, vr1, sxa, (g * 64 + w * 16) * 1024 + kc * 128);
    dma2(sl + 8192, vt0, vt1, swq, (kc * 64 + w * 16) * 1024 + j * 128);
  };
  qdma(0); qdma(1); qdma(2); qdma(3);
  asm volatile("s_waitcnt vmcnt(16)" : "+v"(ep), "+v"(xf0), "+v"(xf1), "+v"(xf2), "+v"(xf3)::"memory");
  ST(1);
  u32x2 tag;
  {
    u64 e = (((u64)__builtin_amdgcn_readfirstlane(ep.y) << 32) | (u32)__builtin_amdgcn_readfirstlane(ep.x)) + 1;
    tag.x = (u32)e; tag.y = (u32)(e >> 32);
  }
  {
    const u32 base = 65536 + w * 4096 + ((lane >> 4) * 8 + ((lane & 15) >> 2)) * 32 + (lane & 3) * 8;
    bf16x8 wf[4], xf[4] = {__builtin_bit_cast(bf16x8, xf0), __builtin_bit_cast(bf16x8, xf1), __builtin_bit_cast(bf16x8, xf2), __builtin_bit_cast(bf16x8, xf3)};
#pragma unroll
    for (int i = 0; i < 4; ++i) wf[i] = ldstr(base + i * 1024, base + i * 1024 + 128);
    f32x4 acc = {0, 0, 0, 0};
    if (j & 1) {
#pragma unroll
      for (int i = 0; i < 4; ++i) acc = mfma16(xf[i], wf[i], acc);
    } else {
#pragma unroll
      for (int i = 0; i < 4; ++i) acc = mfma16(wf[i], xf[i], acc);
    }
    *(LAS f32x4*)(size_t)(65536 + w * 4096 + lane * 16) = acc;
    asm volatile("s_waitcnt lgkmcnt(0)\n\ts_barrier" ::: "memory");
    f32x4 s = *(LAS f32x4*)(size_t)(65536 + lane * 16);
#pragma unroll
    for (int i = 1; i < 4; ++i) s += *(LAS f32x4*)(size_t)(65536 + i * 4096 + lane * 16);
    u32x2 pv = {pk2(s[0], s[1]), pk2(s[2], s[3])};
    const u32 tq = j >> 1, g4 = lane >> 4, c16 = lane & 15;
    u32 off = WS_KV + (g * 2 + (tq >> 1)) * 2048;
    if (j & 1) off += 1024 + (tq & 1) * 512 + ((g4 >> 1) * 16 + c16) * 16 + (g4 & 1) * 8;
    else off += ((g4 & 1) * 32 + (tq & 1) * 16 + c16) * 16 + (g4 >> 1) * 8;
    st8c(pv, w == 0 ? off : OOB, sws);
  }
  ST(2);
  f32x16 qacc = {0, 0, 0, 0, 0, 0, 0, 0, 0, 0, 0, 0, 0, 0, 0, 0};
  const u32 rrow = (th * 32 + r32) * 128, rsw = (r32 >> 1) & 7;
  const u32 q4 = (lane & 15) >> 2, p4 = lane & 3, blk = (lane >> 4) & 1;
  const u32 trb = q4 * 128 + (((hp * 4 + blk * 2 + (p4 >> 1)) ^ ((q4 >> 1) << 2)) << 4) + (p4 & 1) * 8;
  {
    auto qcomp = [&](u32 c) {
      const u32 sl = (c & 3) * 16384;
#pragma unroll
      for (u32 s = 0; s < 4; ++s) {
        bf16x8 xb = ldsr(sl + rrow + (((2 * s + h) ^ rsw) << 4));
        u32 ta = sl + 8192 + trb + h * 1024 + s * 2048;
        qacc = mfma32(ldstr(ta, ta + 512), xb, qacc);
      }
    };
    waitvl_bar<13>(); qcomp(0);
    waitvl_bar<9>(); qdma(4);
    if (lane < 32) { u32x4 o = {0x3f803f80u, 0x3f803f80u, 0x3f803f80u, 0x3f803f80u};
      *(LAS u32x4*)(size_t)(65536 + (w * 2 + (lane >> 4)) * 2048 + ((lane >> 3) & 1) * 512 + (lane & 7) * 16) = o; }
    qcomp(1);
    waitvl_bar<9>(); qdma(5); qcomp(2);
    waitvl_bar<9>(); qdma(6); qcomp(3);
    WAITV(12);
    st8c(tag, (w == 0 && lane == 0) ? WS_KVF + (g * 8 + j) * 8 : OOB, sws);
    ST(3);
    waitvl_bar<9>(); qdma(7); qcomp(4);
    waitvl_bar<9>(); qcomp(5);
    waitvl_bar<5>(); qcomp(6);
    waitvl_bar<0>(); qcomp(7);
  }
  const float qs = 0.25f * 1.44269504088896340736f;
  u32x4 qa, qb;
#pragma unroll
  for (int i = 0; i < 4; ++i) { qa[i] = pk2(qacc[2 * i] * qs, qacc[2 * i + 1] * qs); qb[i] = pk2(qacc[8 + 2 * i] * qs, qacc[9 + 2 * i] * qs); }
  ST(4);
  poll<1>(WS_KVF + (w * 64 + lane) * 8, true, tag, sws);
  waitvl_bar<0>();
  ST(5);
  if (b == 0) st8c(tag, (w == 0 && lane == 0) ? WS_EPA : OOB, sws);
  f32x16 oa, ob;
  {
    const u32x4 skv = mk_srd(a.ws + WS_KV, 131072), swo = mk_srd(a.wo, 512 * 1024);
    u32 so = ((xcd + 1) & 7) * 16384;
    const u32 vl = lane * 16 + w * 4096;
#pragma unroll
    for (u32 c = 0; c < 4; ++c) { dma4c(c * 16384 + w * 4096, vl, skv, so); so = (so + 16384) & 0x1ffff; }
    u32 ka = lane * 16;
    u32 va = r32 < 16 ? (h * 16 + r32) * 16 : 64512 + ((r32 >= 20 && r32 < 28) ? 64 : 0);
    const u32 vinc = r32 < 16 ? 16384 : 0;
    u32 d0 = vl, d1 = vl, d2 = vl, d3 = vl;
    const u32 wv0 = vt0 + w * 32768 + j * 128;
    const u32 wso = (g & 3) * 131072;
    u32 m0v = w * 4096;
    const u32 m0lim = w * 4096 + 65536;
    asm volatile(
        "s_mov_b32 s40, %[k0]\n\ts_mov_b32 s41, %[k1]\n\ts_mov_b32 s42, %[k2]\n\ts_mov_b32 s43, %[k3]\n\t"
        "s_mov_b32 s44, 0x4000\n\ts_mov_b32 s45, 0x1ffff\n\ts_mov_b32 s46, 0x4000\n\ts_mov_b32 s47, 0\n\t"
        "s_mov_b32 m0, %[m0v]\n\t"
        "v_mov_b32 v156, 0xdddddddd\n\t"
        "v_mov_b32 v88, 0\n\tv_mov_b32 v89, 0\n\tv_mov_b32 v90, 0\n\tv_mov_b32 v91, 0\n\tv_mov_b32 v92, 0\n\tv_mov_b32 v93, 0\n\tv_mov_b32 v94, 0\n\tv_mov_b32 v95, 0\n\t"
        "v_mov_b32 v152, 0\n\tv_mov_b32 v153, 0\n\tv_mov_b32 v154, 0\n\tv_mov_b32 v155, 0\n\t"
        "v_mov_b32 v96, 0\n\tv_mov_b32 v97, 0\n\tv_mov_b32 v98, 0\n\tv_mov_b32 v99, 0\n\tv_mov_b32 v100, 0\n\tv_mov_b32 v101, 0\n\tv_mov_b32 v102, 0\n\tv_mov_b32 v103, 0\n\t"
        "v_mov_b32 v104, 0\n\tv_mov_b32 v105, 0\n\tv_mov_b32 v106, 0\n\tv_mov_b32 v107, 0\n\tv_mov_b32 v108, 0\n\tv_mov_b32 v109, 0\n\tv_mov_b32 v110, 0\n\tv_mov_b32 v111, 0\n\t"
        "v_mov_b32 v112, 0\n\tv_mov_b32 v113, 0\n\tv_mov_b32 v114, 0\n\tv_mov_b32 v115, 0\n\tv_mov_b32 v116, 0\n\tv_mov_b32 v117, 0\n\tv_mov_b32 v118, 0\n\tv_mov_b32 v119, 0\n\t"
        "v_mov_b32 v120, 0\n\tv_mov_b32 v121, 0\n\tv_mov_b32 v122, 0\n\tv_mov_b32 v123, 0\n\tv_mov_b32 v124, 0\n\tv_mov_b32 v125, 0\n\tv_mov_b32 v126, 0\n\tv_mov_b32 v127, 0\n\t"
        "s_waitcnt vmcnt(12)\n\ts_barrier\n\t"
        "ds_read_b128 v[136:139], %[ka]\n\tds_read_b128 " VA0 ", %[va] offset:1024\n\tds_read_b128 " VA1 ", %[va] offset:1536\n\t"
        "s_setprio 0\n\t"
        WL QK("v[64:79]", "v[128:131]") "s_nop 11\n\t"
        "1:\n\t"
        IT(VA0, VA1, VB0, VB1, 2048, 3072, 3584, WL, "", "")
        IT(VB0, VB1, VA0, VA1, 4096, 5120, 5632, WL, "", "")
        IT(VA0, VA1, VB0, VB1, 6144, 7168, 7680, WL, "", "")
        IT(VB0, VB1, VA0, VA1, 8192, 9216, 9728, WL, "", "")
        IT(VA0, VA1, VB0, VB1, 10240, 11264, 11776, WL, "", "")
        IT(VB0, VB1, VA0, VA1, 12288, 13312, 13824, WL, "", "")
        IT(VA0, VA1, VB0, VB1, 14336, 15360, 15872, "s_waitcnt vmcnt(8) lgkmcnt(0)\n\t",
           "s_barrier\n\t"
           "buffer_load_dwordx4 %[d0], s[40:43], %[so] offen sc1 lds\n\tbuffer_load_dwordx4 %[d1], s[40:43], %[so] offen offset:1024 sc1 lds\n\t"
           "v_add_u16 %[ka], s46, %[ka]\n\tv_add_u16 %[va], %[vinc], %[va]\n\t",
           "buffer_load_dwordx4 %[d2], s[40:43], %[so] offen offset:2048 sc1 lds\n\tbuffer_load_dwordx4 %[d3], s[40:43], %[so] offen offset:3072 sc1 lds\n\t")
        IT(VB0, VB1, VA0, VA1, 0, 1024, 1536, WL,
           "s_add_u32 %[so], %[so], s44\n\ts_and_b32 %[so], %[so], s45\n\t",
           "s_add_u32 m0, m0, s46\n\ts_cmp_lg_u32 m0, %[m0lim]\n\t")
        "s_cbranch_scc1 1b\n\t"
        "s_cmp_lg_u32 s47, 0\n\ts_cbranch_scc1 2f\n\t"
        "s_mov_b32 s47, 1\n\ts_mov_b32 s40, %[w0]\n\ts_mov_b32 s41, %[w1]\n\ts_mov_b32 s42, %[w2]\n\t"
        "s_mov_b32 s44, 0x20000\n\ts_mov_b32 s45, 0x7ffff\n\ts_mov_b32 %[so], %[wso]\n\ts_mov_b32 m0, %[m0v]\n\t"
        "v_mov_b32 %[d0], %[wv0]\n\tv_add_u32 %[d1], 7168, %[wv0]\n\tv_add_u32 %[d2], 14336, %[wv0]\n\tv_add_u32 %[d3], 21504, %[wv0]\n\t"
        "s_branch 1b\n\t"
        "2:\n\t"
        "s_setprio 3\n\ts_nop 0\n\t"
        SM(OB, VB1, "v[88:95]")
        "s_nop 15\n\t"
        : "=&{v[96:111]}"(oa), "=&{v[112:127]}"(ob), [ka] "+v"(ka), [va] "+v"(va), [so] "+s"(so),
          [d0] "+v"(d0), [d1] "+v"(d1), [d2] "+v"(d2), [d3] "+v"(d3)
        : "{v[128:131]}"(qa), "{v[132:135]}"(qb), [vinc] "v"(vinc), [wv0] "v"(wv0), [wso] "s"(wso), [m0v] "s"(m0v), [m0lim] "s"(m0lim),
          [k0] "s"(skv.x), [k1] "s"(skv.y), [k2] "s"(skv.z), [k3] "s"(skv.w), [w0] "s"(swo.x), [w1] "s"(swo.y), [w2] "s"(swo.z)
        : "memory", "scc", "s40", "s41", "s42", "s43", "s44", "s45", "s46", "s47",
          "v64", "v65", "v66", "v67", "v68", "v69", "v70", "v71", "v72", "v73", "v74", "v75", "v76", "v77", "v78", "v79",
          "v80", "v81", "v82", "v83", "v84", "v85", "v86", "v87", "v88", "v89", "v90", "v91", "v92", "v93", "v94", "v95",
          "v136", "v137", "v138", "v139", "v140", "v141", "v142", "v143", "v144", "v145", "v146", "v147",
          "v148", "v149", "v150", "v151", "v152", "v153", "v154", "v155", "v156");
  }
  ST(6);
  {
    float ma = 0.f;
#pragma unroll
    for (int i = 0; i < 8; ++i) ma += __builtin_fabsf(oa[i]) * 0.0625f + __builtin_fabsf(ob[i]) * 0.0625f;
    const bool ok = oa[8] >= 0x1p-64f && oa[8] <= 0x1p100f && ob[8] >= 0x1p-64f && ob[8] <= 0x1p100f && ma <= 3.0e38f;
    if (__builtin_amdgcn_ballot_w64(!ok) != 0) {
      const __amdgpu_buffer_rsrc_t rk = __builtin_amdgcn_make_buffer_rsrc(a.ws + WS_KV, 0, 131072, 0x20000);
      const u32x4 ones = {0x3f803f80u, 0x3f803f80u, 0x3f803f80u, 0x3f803f80u};
#pragma unroll 1
      for (int t = 0; t < 2; ++t) {
        const bf16x8 q = __builtin_bit_cast(bf16x8, t ? qb : qa);
        f32x16 z = {0, 0, 0, 0, 0, 0, 0, 0, 0, 0, 0, 0, 0, 0, 0, 0};
        float m = -__builtin_inff();
#pragma unroll 1
        for (u32 kt = 0; kt < 64; ++kt) {
          u32x4 kf = __builtin_amdgcn_raw_buffer_load_b128(rk, lane * 16, kt * 2048, 16);
          f32x16 s = mfma32(__builtin_bit_cast(bf16x8, kf), q, z);
#pragma unroll
          for (int i = 0; i < 16; ++i) m = __builtin_fmaxf(m, s[i]);
        }
        m = __builtin_fmaxf(m, __shfl_xor(m, 32));
        f32x16 o = z;
#pragma unroll 1
        for (u32 kt = 0; kt < 64; ++kt) {
          u32x4 kf = __builtin_amdgcn_raw_buffer_load_b128(rk, lane * 16, kt * 2048, 16);
          u32x4 v0 = __builtin_amdgcn_raw_buffer_load_b128(rk, (h * 16 + (r32 & 15)) * 16 + 1024, kt * 2048, 16);
          u32x4 v1 = __builtin_amdgcn_raw_buffer_load_b128(rk, (h * 16 + (r32 & 15)) * 16 + 1536, kt * 2048, 16);
          if (r32 >= 16) { v0 = ones; v1 = ones; }
          f32x16 s = mfma32(__builtin_bit_cast(bf16x8, kf), q, z);
          f32x8 p0, p1;
#pragma unroll
          for (int i = 0; i < 8; ++i) { p0[i] = __builtin_amdgcn_exp2f(s[i] - m); p1[i] = __builtin_amdgcn_exp2f(s[8 + i] - m); }
          o = __builtin_amdgcn_smfmac_f32_32x32x32_bf16(__builtin_bit_cast(bf16x8, v0), __builtin_bit_cast(bf16x16, p0), o, (int)0xdddddddd, 0, 0);
          o = __builtin_amdgcn_smfmac_f32_32x32x32_bf16(__builtin_bit_cast(bf16x8, v1), __builtin_bit_cast(bf16x16, p1), o, (int)0xdddddddd, 0, 0);
        }
        if (t) ob = o; else oa = o;
      }
    }
  }
  {
    const float ia = __builtin_amdgcn_rcpf(oa[8]), ib = __builtin_amdgcn_rcpf(ob[8]);
    u32x4 pa, pb;
#pragma unroll
    for (int i = 0; i < 4; ++i) { pa[i] = pk2(oa[2 * i] * ia, oa[2 * i + 1] * ia); pb[i] = pk2(ob[2 * i] * ib, ob[2 * i + 1] * ib); }
    const u32 off = WS_O + g * 65536 + ((j * 4 + hp * 2) * 2 + th) * 1024 + lane * 16;
    st16c(pa, off, sws);
    st16c(pb, off + 2048, sws);
  }
  waitvl_bar<2>();
  bf16x8 wf[32];
#pragma unroll
  for (u32 i = 0; i < 32; ++i) { u32 ta = trb + h * 512 + i * 2048; wf[i] = ldstr(ta, ta + 1024); }
  waitvl_bar<0>();
  st8c(tag, (w == 0 && lane == 0) ? WS_OF + g * 256 + j * 8 : OOB, sws);
  ST(7);
  poll<1>(WS_OF + g * 256 + (lane & 7) * 8, true, tag, sws);
  ST(8);
#pragma unroll
  for (u32 c = 0; c < 4; ++c) dma4c(c * 16384 + w * 4096, lane * 16, sws, WS_O + g * 65536 + (((g & 3) + c) & 3) * 16384 + w * 4096);
  f32x16 acc = {0, 0, 0, 0, 0, 0, 0, 0, 0, 0, 0, 0, 0, 0, 0, 0};
  auto ocomp = [&](u32 c) {
#pragma unroll
    for (u32 i = 0; i < 8; ++i) acc = mfma32(ldsr(c * 16384 + i * 2048 + th * 1024 + lane * 16), wf[c * 8 + i], acc);
  };
  waitvl_bar<12>(); ocomp(0);
  waitvl_bar<8>(); ocomp(1);
  waitvl_bar<4>(); ocomp(2);
  waitvl_bar<0>(); ocomp(3);
  ST(9);
  float* op = a.out + (size_t)(g * 64 + th * 32 + h * 4) * 1024 + j * 64 + hp * 32 + r32;
#pragma unroll
  for (int i = 0; i < 16; ++i) op[((i & 3) + 8 * (i >> 2)) * 1024] = acc[i];
  ST(10);
  {
    auto T = [&](int i, int k) { return (float)(stv[i] - stv[k]) * 0.01f; };
    float d0 = 0.f, d1 = 0.f;
    if (DIAG == 1) { d0 = T(1, 0); d1 = T(5, 0); }
    if (DIAG == 2) { d0 = T(6, 5); d1 = (float)(stc[6] - stc[5]) * 0.001f; }
    if (DIAG == 3) { d0 = T(10, 6); d1 = T(10, 0); }
    if (DIAG == 4) { u32 la = __builtin_amdgcn_s_getreg(6 | (31 << 11)); d0 = (la & 0xfff) ? 11.f : 1.f; d1 = 32.f - T(5, 0); }
    if (DIAG == 5) { d0 = T(3, 0); d1 = T(4, 0); }
    if (DIAG == 6) { d0 = 32.f - T(6, 5); d1 = 8.f - T(1, 0); }
    asm volatile("s_waitcnt vmcnt(0)" ::: "memory");
    ST(11);
    float cond = T(10, 0);
    if (DIAG == 7) { d0 = (float)(stv[0] - (u32)tf) * 0.01f; d1 = T(10, 0); cond = d0; }
    if (DIAG == 8) { d0 = T(11, 10); d1 = T(10, 0); cond = d0; }
    if (DIAG == 9) { d0 = T(5, 0); d1 = T(1, 0); cond = d0; }
    if (DIAG == 10) { cond = 0.f; while ((u32)__builtin_amdgcn_s_memrealtime() - stv[10] < 400) __builtin_amdgcn_s_sleep(2); }
    if (w == 0 && lane == 0 && cond > DIAGTHR) { op[0] = d0; op[512] = d1; }
  }
}

DEVI void cmul(float& r, float& i, float ar, float ai, float br, float bi) { r = ar * br - ai * bi; i = ar * bi + ai * br; }
DEVI void cstep(float& r, float& i, float ar, float ai, float ur, float ui) {
  float nr = __builtin_fmaf(r, ar, __builtin_fmaf(-i, ai, ur)), ni = __builtin_fmaf(i, ar, __builtin_fmaf(r, ai, ui));
  r = nr; i = ni;
}

DEVI void role_b(const Args& a, u32 b, u32 w, u32 lane) {
  const u32 xcd = b & 7, idx = b >> 3, g = xcd * 4 + (idx >> 3), j = idx & 7;
  const u32 r32 = lane & 31, h = lane >> 5, th = w & 1, ph = w >> 1;
  const u32x4 sws = mk_srd(a.ws, WS_END), sxb = mk_srd(a.xb, 2048 * 1024);
  __builtin_amdgcn_s_setprio(2);
  STDECL;
  ST(0);
  u32x2 ep;
  u32 plr, pli, pdt;
  {
    const u32x4 s0 = mk_srd(a.lre, 1024), s1 = mk_srd(a.lim, 1024), s2 = mk_srd(a.ldt, 1024);
    const u32 vo = (j * 64 + ph * 32 + r32) * 2;
    asm volatile("buffer_load_dwordx2 %0, off, %4, 0 offset:256 sc1\n\tbuffer_load_ushort %1, %8, %5, 0 offen\n\t"
                 "buffer_load_ushort %2, %8, %6, 0 offen\n\tbuffer_load_ushort %3, %8, %7, 0 offen"
                 : "=&v"(ep), "=&v"(plr), "=&v"(pli), "=&v"(pdt) : "s"(sws), "s"(s0), "s"(s1), "s"(s2), "v"(vo) : "memory");
  }
  const u32 vr0 = (lane >> 3) * 1024 + (((lane & 7) ^ (lane >> 4)) << 4);
  const u32 vr1 = (lane >> 3) * 1024 + (((lane & 7) ^ (4 + (lane >> 4))) << 4) + 7168;
  const u32 rsw = (r32 >> 1) & 7;
  f32x16 ur = {0, 0, 0, 0, 0, 0, 0, 0, 0, 0, 0, 0, 0, 0, 0, 0}, ui = ur;
  float ar, ai, gr, gi;
  u32x2 tag;
  {
    const u32x4 sbr = mk_srd(a.bre, 512 * 1024), sbi = mk_srd(a.bim, 512 * 1024);
    const u32 xrow = (16 * h + ((lane >> 3) & 3)) * 1024;
    const u32 vx0 = xrow + (((lane & 7) ^ (lane >> 4)) << 4);
    const u32 vx1 = xrow + (((lane & 7) ^ (4 + (lane >> 4))) << 4) + 3072;
    const u32 rot = (j + (g & 3) * 2) & 7;
    auto bdma = [&](u32 c) {
      u32 kc = (c + rot) & 7, sl = (c % 3) * 24576 + w * 2048;
      dma2(sl, vx0, vx1, sxb, (g * 64 + ph * 32 + th * 8) * 1024 + kc * 128);
      dma2(sl + 8192, vr0, vr1, sbr, (j * 64 + w * 16) * 1024 + kc * 128);
      dma2(sl + 16384, vr0, vr1, sbi, (j * 64 + w * 16) * 1024 + kc * 128);
    };
    bdma(0); bdma(1); bdma(2);
    asm volatile("s_waitcnt vmcnt(18)" : "+v"(ep), "+v"(plr), "+v"(pli), "+v"(pdt)::"memory");
    {
      u64 e = (((u64)__builtin_amdgcn_readfirstlane(ep.y) << 32) | (u32)__builtin_amdgcn_readfirstlane(ep.x)) + 1;
      tag.x = (u32)e; tag.y = (u32)(e >> 32);
    }
    {
      const float lr = bf2f(plr), li = bf2f(pli);
      const float dt = bf2f(pk2(expf(bf2f(pdt)), 0.f) & 0xffffu);
      const float e = expf(dt * lr);
      float sn, cs;
      sincosf(dt * li, &sn, &cs);
      ar = e * cs; ai = e * sn;
      const float nr = ar - 1.f, ni = ai;
      const bool sw = __builtin_fabsf(lr) >= __builtin_fabsf(li);
      const float big = sw ? lr : li, sm = sw ? li : lr, rt = sm / big, den = big + sm * rt;
      const float x = sw ? nr : ni, y = sw ? ni : nr;
      gr = 2.f * (x + y * rt) / den;
      gi = 2.f * (y - x * rt) / den;
      gi = sw ? gi : -gi;
      ai = -ai;
    }
    auto bcomp = [&](u32 c) {
      const u32 sl = (c % 3) * 24576;
#pragma unroll
      for (u32 s = 0; s < 4; ++s) {
        const u32 sw = ((2 * s + h) ^ rsw) << 4;
        bf16x8 xf = ldsr(sl + (th * 32 + r32) * 128 + sw);
        ur = mfma32(xf, ldsr(sl + 8192 + (ph * 32 + r32) * 128 + sw), ur);
        ui = mfma32(xf, ldsr(sl + 16384 + (ph * 32 + r32) * 128 + sw), ui);
      }
    };
    waitvl_bar<12>(); bcomp(0);
    waitvl_bar<6>(); bdma(3); bcomp(1);
    waitvl_bar<6>(); bdma(4); bcomp(2);
    waitvl_bar<6>(); bdma(5); bcomp(3);
    waitvl_bar<6>(); bdma(6); bcomp(4);
    waitvl_bar<6>(); bdma(7); bcomp(5);
    waitvl_bar<6>(); bcomp(6);
    waitvl_bar<0>(); bcomp(7);
  }
  ST(1);
  float er = 0.f, ei = 0.f;
#pragma unroll
  for (int i = 0; i < 16; ++i) {
    float xr = gr * ur[i] - gi * ui[i], xi = -(gr * ui[i] + gi * ur[i]);
    ur[i] = xr; ui[i] = xi;
    cstep(er, ei, ar, ai, xr, xi);
  }
  float a16r = ar, a16i = ai;
#pragma unroll
  for (int i = 0; i < 4; ++i) cmul(a16r, a16i, a16r, a16i, a16r, a16i);
  const u32 eb = 73728 + (ph * 32 + r32) * 8;
  { f32x2 e2 = {er, ei}; *(LAS f32x2*)(size_t)(eb + (th * 2 + h) * 512) = e2; }
  waitvl_bar<0>();
  f32x2 es[4];
#pragma unroll
  for (int s = 0; s < 4; ++s) es[s] = *(LAS f32x2*)(size_t)(eb + s * 512);
  if (th == 0) {
    float tr = es[0].x, ti = es[0].y;
#pragma unroll
    for (int s = 1; s < 4; ++s) cstep(tr, ti, a16r, a16i, es[s].x, es[s].y);
    u32x2 ev = {__builtin_bit_cast(u32, tr), __builtin_bit_cast(u32, ti)};
    st8c(ev, h == 0 ? WS_E + (j * 32 + g) * 512 + (ph * 32 + r32) * 8 : OOB, sws);
    WAITV(0);
    st8c(tag, lane == 0 ? WS_EF + ((j * 2 + ph) * 32 + g) * 8 : OOB, sws);
  }
  ST(2);
  float a64r = a16r, a64i = a16i;
  cmul(a64r, a64i, a64r, a64i, a64r, a64i);
  cmul(a64r, a64i, a64r, a64i, a64r, a64i);
  float akr = a64r, aki = a64i;
#pragma unroll
  for (int i = 0; i < 4; ++i) cmul(akr, aki, akr, aki, akr, aki);
  poll<4>(WS_EF + ((j * 2 + ph) * 32 + r32) * 8, r32 < g, tag, sws);
  ST(3);
  float cr = 0.f, ci = 0.f;
  {
    const __amdgpu_buffer_rsrc_t re = __builtin_amdgcn_make_buffer_rsrc(a.ws + WS_E + j * 16384, 0, 16384, 0x20000);
    u32x2 ev[16];
#pragma unroll
    for (int m = 0; m < 16; ++m) {
      int gp = (int)g - 1 - 16 * (int)h - m;
      ev[m] = __builtin_amdgcn_raw_buffer_load_b64(re, gp >= 0 ? (u32)gp * 512 + (ph * 32 + r32) * 8 : OOB, 0, 16);
    }
#pragma unroll
    for (int m = 15; m >= 0; --m) { const f32x2 e2 = __builtin_bit_cast(f32x2, ev[m]); cstep(cr, ci, a64r, a64i, e2.x, e2.y); }
    float orr = __shfl_xor(cr, 32), oi = __shfl_xor(ci, 32);
    float lr_ = h ? orr : cr, li_ = h ? oi : ci, hr_ = h ? cr : orr, hi_ = h ? ci : oi;
    if (g > 16) { cstep(hr_, hi_, akr, aki, lr_, li_); cr = hr_; ci = hi_; } else { cr = lr_; ci = li_; }
#pragma unroll
    for (u32 s = 0; s < 3; ++s) {
      float tr = cr, ti = ci;
      cstep(tr, ti, a16r, a16i, es[s].x, es[s].y);
      const bool use = s < th * 2 + h;
      cr = use ? tr : cr; ci = use ? ti : ci;
    }
  }
#pragma unroll
  for (int i = 0; i < 16; ++i) { cstep(cr, ci, ar, ai, ur[i], ui[i]); ur[i] = cr; ui[i] = ci; }
  {
    const u32 off = WS_H + (g * 8 + j) * 16384 + (ph * 2 + th) * 2048 + h * 512;
#pragma unroll
    for (u32 i = 0; i < 2; ++i) {
      u32x4 pr, pi;
#pragma unroll
      for (int e = 0; e < 4; ++e) { pr[e] = pk2(ur[8 * i + 2 * e], ur[8 * i + 2 * e + 1]); pi[e] = pk2(ui[8 * i + 2 * e], ui[8 * i + 2 * e + 1]); }
      const u32 o = off + i * 1024 + ((r32 ^ ((2 * i + h) << 2)) << 4);
      st16c(pr, o, sws);
      st16c(pi, o + 8192, sws);
    }
  }
  waitvl_bar<0>();
  st8c(tag, (w == 0 && lane == 0) ? WS_HF + g * 256 + j * 8 : OOB, sws);
  ST(4);
  const u32 col = j * 64 + ph * 32 + r32;
  f32x16 acc;
  {
    const float dk = bf2f(a.dsk[col]);
    const u16* xp = a.xb + (size_t)(g * 64 + th * 32 + h * 4) * 512 + col;
#pragma unroll
    for (int i = 0; i < 16; ++i) acc[i] = bf2f(xp[((i & 3) + 8 * (i >> 2)) * 512]) * dk;
    asm volatile("" : "+v"(acc));
  }
  poll<4>(WS_HF + g * 256 + (lane & 7) * 8, true, tag, sws);
  ST(5);
  if (g == 31 && j == 0) st8c(tag, (w == 0 && lane == 0) ? WS_EPB : OOB, sws);
  {
    const u32x4 scr = mk_srd(a.cre, 512 * 1024), sci = mk_srd(a.cim, 512 * 1024);
    auto cdma = [&](u32 c) {
      u32 jj = ((c >> 1) + j) & 7, sl = (c % 5) * 16384 + w * 2048;
      dma2c(sl, lane * 16, sws, WS_H + (g * 8 + jj) * 16384 + (c & 1) * 8192 + w * 2048);
      dma2(sl + 8192, vr0, vr1, (c & 1) ? sci : scr, (j * 64 + w * 16) * 1024 + jj * 128);
    };
    const u32 q4 = (lane & 15) >> 2, p4 = lane & 3, blk = (lane >> 4) & 1;
    const u32 tb0 = (th * 2 + (p4 >> 1)) * 1024 + blk * 512 + (((8 * h + q4) ^ ((2 * (p4 >> 1) + blk) << 2)) << 4) + (p4 & 1) * 8;
    const u32 tb1 = tb0 ^ 64;
    auto ccomp = [&](u32 c) {
      const u32 sl = (c % 5) * 16384;
#pragma unroll
      for (u32 s = 0; s < 4; ++s) {
        const u32 o = sl + (s >> 1) * 4096 + (s & 1) * 256;
        acc = mfma32(ldstr(tb0 + o, tb1 + o), ldsr(sl + 8192 + (ph * 32 + r32) * 128 + (((2 * s + h) ^ rsw) << 4)), acc);
      }
    };
    cdma(0); cdma(1); cdma(2); cdma(3); cdma(4);
    waitvl_bar<16>(); ccomp(0);
#pragma unroll
    for (u32 c = 1; c < 12; ++c) { waitvl_bar<12>(); cdma(c + 4); ccomp(c); }
    waitvl_bar<12>(); ccomp(12);
    waitvl_bar<8>(); ccomp(13);
    waitvl_bar<4>(); ccomp(14);
    waitvl_bar<0>(); ccomp(15);
  }
  ST(6);
  float* op = a.out + (size_t)(g * 64 + th * 32 + h * 4) * 1024 + 512 + col;
#pragma unroll
  for (int i = 0; i < 16; ++i) op[((i & 3) + 8 * (i >> 2)) * 1024] = acc[i];
  ST(7);
  STFLUSH;
}

__global__ void __launch_bounds__(256, 2) fused_kernel(Args a) {
  __shared__ __attribute__((aligned(16))) char lds[81920];
  u64 tf = 0;
  tf = __builtin_amdgcn_s_memrealtime();
  asm volatile("" ::"v"((u32)(size_t)(LAS char*)lds));
  const u32 lane = threadIdx.x & 63, w = __builtin_amdgcn_readfirstlane(threadIdx.x >> 6), b = blockIdx.x;
  if (b < 256) role_a(a, b, w, lane, tf); else {
    role_b(a, b - 256, w, lane);
  }
}

extern "C" void kernel_launch(void* const* d_in, const int* in_sizes, int n_in, void* d_out, int out_size, void* d_ws,
                              size_t ws_size, hipStream_t stream) {
  Args a;
  a.xa = (const u16*)d_in[0]; a.wq = (const u16*)d_in[1]; a.wk = (const u16*)d_in[2]; a.wv = (const u16*)d_in[3];
  a.wo = (const u16*)d_in[4]; a.xb = (const u16*)d_in[5]; a.lre = (const u16*)d_in[6]; a.lim = (const u16*)d_in[7];
  a.bre = (const u16*)d_in[8]; a.bim = (const u16*)d_in[9]; a.cre = (const u16*)d_in[10]; a.cim = (const u16*)d_in[11];
  a.ldt = (const u16*)d_in[12]; a.dsk = (const u16*)d_in[13];
  a.out = (float*)d_out; a.ws = (char*)d_ws;
  fused_kernel<<<512, 256, 0, stream>>>(a);
}
```

```cpp
#define AUXA 17
#define AUXB 17
#include <hip/hip_runtime.h>
typedef unsigned short u16;
typedef unsigned u32;
typedef unsigned long long u64;
typedef float f32x2 __attribute__((ext_vector_type(2)));
typedef float f32x4 __attribute__((ext_vector_type(4)));
typedef float f32x8 __attribute__((ext_vector_type(8)));
typedef float f32x16 __attribute__((ext_vector_type(16)));
typedef __bf16 bf16x2 __attribute__((ext_vector_type(2)));
typedef __bf16 bf16x8 __attribute__((ext_vector_type(8)));
typedef __bf16 bf16x16 __attribute__((ext_vector_type(16)));
typedef short s16x4 __attribute__((ext_vector_type(4)));
typedef u32 u32x2 __attribute__((ext_vector_type(2)));
typedef u32 u32x4 __attribute__((ext_vector_type(4)));
#define LAS __attribute__((address_space(3)))
#define DEVI __device__ __forceinline__

constexpr u32 WS_EPA = 0, WS_EPB = 256;
constexpr u32 WS_KVF = 4096;
constexpr u32 WS_EF = 8192;
constexpr u32 WS_HF = 16384;
constexpr u32 WS_OF = 32768;
constexpr u32 WS_KV = 65536;
constexpr u32 WS_E = 262144;
constexpr u32 WS_O = 524288;
constexpr u32 WS_H = WS_O + 2097152;
constexpr u32 WS_END = WS_H + 4194304;
constexpr u32 WS_DBG = WS_END;
constexpr u32 OOB = 0x80000000u;

struct Args {
  const u16 *xa, *wq, *wk, *wv, *wo, *xb, *lre, *lim, *bre, *bim, *cre, *cim, *ldt, *dsk;
  float* out;
  char* ws;
};

DEVI u32x4 mk_srd(const void* p, u32 bytes) {
  u64 a = (u64)p;
  u32x4 r = {(u32)a, (u32)(a >> 32) & 0xffffu, bytes, 0x20000u};
  return r;
}
DEVI void dma2(u32 m0v, u32 v0, u32 v1, u32x4 srd, u32 soff) {
  asm volatile("s_mov_b32 m0, %0\n\ts_nop 0\n\tbuffer_load_dwordx4 %1, %3, %4 offen lds\n\tbuffer_load_dwordx4 %2, %3, %4 offen offset:1024 lds"
               ::"s"(m0v), "v"(v0), "v"(v1), "s"(srd), "s"(soff) : "memory");
}
DEVI void dma2c(u32 m0v, u32 v0, u32x4 srd, u32 soff) {
  asm volatile("s_mov_b32 m0, %0\n\ts_nop 0\n\tbuffer_load_dwordx4 %1, %2, %3 offen sc1 lds\n\tbuffer_load_dwordx4 %1, %2, %3 offen offset:1024 sc1 lds"
               ::"s"(m0v), "v"(v0), "s"(srd), "s"(soff) : "memory");
}
DEVI void dma4(u32 m0v, u32 v0, u32x4 srd, u32 soff) {
  asm volatile("s_mov_b32 m0, %0\n\ts_nop 0\n\tbuffer_load_dwordx4 %1, %2, %3 offen lds\n\tbuffer_load_dwordx4 %1, %2, %3 offen offset:1024 lds\n\t"
               "buffer_load_dwordx4 %1, %2, %3 offen offset:2048 lds\n\tbuffer_load_dwordx4 %1, %2, %3 offen offset:3072 lds"
               ::"s"(m0v), "v"(v0), "s"(srd), "s"(soff) : "memory");
}
DEVI void dma4c(u32 m0v, u32 v0, u32x4 srd, u32 soff) {
  asm volatile("s_mov_b32 m0, %0\n\ts_nop 0\n\tbuffer_load_dwordx4 %1, %2, %3 offen sc1 lds\n\tbuffer_load_dwordx4 %1, %2, %3 offen offset:1024 sc1 lds\n\t"
               "buffer_load_dwordx4 %1, %2, %3 offen offset:2048 sc1 lds\n\tbuffer_load_dwordx4 %1, %2, %3 offen offset:3072 sc1 lds"
               ::"s"(m0v), "v"(v0), "s"(srd), "s"(soff) : "memory");
}
DEVI void st8c(u32x2 v, u32 voff, u32x4 srd) {
  asm volatile("buffer_store_dwordx2 %0, %1, %2, 0 offen sc1" ::"v"(v), "v"(voff), "s"(srd) : "memory");
}
DEVI void st16c(u32x4 v, u32 voff, u32x4 srd) {
  asm volatile("buffer_store_dwordx4 %0, %1, %2, 0 offen sc1\n\ts_nop 1" ::"v"(v), "v"(voff), "s"(srd) : "memory");
}
#define WAITV(n) asm volatile("s_waitcnt vmcnt(" #n ")" ::: "memory")
#define WAITVL(n) asm volatile("s_waitcnt vmcnt(" #n ") lgkmcnt(0)" ::: "memory")
#define BAR() asm volatile("s_barrier" ::: "memory")
template <int N> DEVI void waitvl_bar() { asm volatile("s_waitcnt vmcnt(%0) lgkmcnt(0)\n\ts_barrier" ::"i"(N) : "memory"); }

template <int SLEEP> DEVI void poll(u32 voff, bool need, u32x2 tag, u32x4 srd) {
  for (u32 it = 0; it < (1u << 18); ++it) {
    u32x2 f;
    asm volatile("buffer_load_dwordx2 %0, %1, %2, 0 offen sc1\n\ts_waitcnt vmcnt(0)" : "=&v"(f) : "v"(voff), "s"(srd) : "memory");
    bool bad = need && (f.x != tag.x || f.y != tag.y);
    if (__builtin_amdgcn_ballot_w64(bad) == 0) break;
    __builtin_amdgcn_s_sleep(SLEEP);
  }
}
DEVI u32 pk2(float a, float b) {
  f32x2 x = {a, b};
  return __builtin_bit_cast(u32, __builtin_convertvector(x, bf16x2));
}
DEVI float bf2f(u32 v) { return __builtin_bit_cast(float, v << 16); }
DEVI bf16x8 ldsr(u32 a) { return *(const LAS bf16x8*)(size_t)a; }
DEVI bf16x8 ldstr(u32 a0, u32 a1) {
  s16x4 x = __builtin_amdgcn_ds_read_tr16_b64_v4i16((LAS s16x4*)(size_t)a0);
  s16x4 y = __builtin_amdgcn_ds_read_tr16_b64_v4i16((LAS s16x4*)(size_t)a1);
  return __builtin_bit_cast(bf16x8, __builtin_shufflevector(x, y, 0, 1, 2, 3, 4, 5, 6, 7));
}
DEVI f32x16 mfma32(bf16x8 a, bf16x8 b, f32x16 c) { return __builtin_amdgcn_mfma_f32_32x32x16_bf16(a, b, c, 0, 0, 0); }
DEVI f32x4 mfma16(bf16x8 a, bf16x8 b, f32x4 c) { return __builtin_amdgcn_mfma_f32_16x16x32_bf16(a, b, c, 0, 0, 0); }

#define STDECL
#define STC(v)
#define ST(i)
#define STFLUSH

#define EX(r) "v_exp_f32 v" #r ", v" #r "\n\t"
#define SM(o, vv, p) "v_smfmac_f32_32x32x32_bf16 " o ", " vv ", " p ", v156\n\t"
#define OA "v[96:111]"
#define OB "v[112:127]"
#define QK(s, q) "v_mfma_f32_32x32x16_bf16 " s ", v[136:139], " q ", 0\n\t"
#define IT(X0, X1, Y0, Y1, KO, V0O, V1O, WT, E1, E2)                                        \
  EX(64) EX(65) SM(OB, Y1, "v[88:95]")                                                      \
  "ds_read_b128 " Y0 ", %[va] offset:" #V0O "\n\tds_read_b128 " Y1 ", %[va] offset:" #V1O "\n\t" \
  EX(66) EX(67) EX(68) QK("v[80:95]", "v[132:135]")                                         \
  "ds_read_b128 v[136:139], %[ka] offset:" #KO "\n\t"                                       \
  EX(69) EX(70) EX(71) EX(72) EX(73) SM(OA, X0, "v[64:71]")                                 \
  EX(74) EX(75) EX(76) EX(77) EX(78) EX(79) EX(80) EX(81) SM(OA, X1, "v[72:79]")            \
  EX(82) EX(83) EX(84) WT QK("v[64:79]", "v[128:131]") E1                                   \
  EX(85) EX(86) EX(87) EX(88) EX(89) SM(OB, X0, "v[80:87]") E2                              \
  EX(90) EX(91) EX(92) EX(93) EX(94) EX(95)
#define VA0 "v[140:143]"
#define VA1 "v[144:147]"
#define VB0 "v[148:151]"
#define VB1 "v[152:155]"
#define WL "s_waitcnt lgkmcnt(0)\n\t"

DEVI void role_a(const Args& a, u32 b, u32 w, u32 lane, u64 tf) {
  const u32 xcd = b & 7, idx = b >> 3, g = xcd * 4 + (idx >> 3), j = idx & 7;
  const u32 r32 = lane & 31, h = lane >> 5, th = w & 1, hp = w >> 1;
  const u32x4 sws = mk_srd(a.ws, WS_END), sxa = mk_srd(a.xa, 2048 * 1024), swq = mk_srd(a.wq, 512 * 1024);
  const u32x4 swkv = mk_srd((j & 1) ? a.wv : a.wk, 512 * 32);
  __builtin_amdgcn_s_setprio(3);
  STDECL;
  ST(0);
  u32x2 ep;
  asm volatile("buffer_load_dwordx2 %0, off, %1, 0 sc1" : "=v"(ep) : "s"(sws) : "memory");
  dma4(65536 + w * 4096, lane * 16, swkv, w * 4096);
  u32x4 xf0, xf1, xf2, xf3;
  {
    u32 vo = (lane & 15) * 1024 + (lane >> 4) * 16, so = (g * 64 + (j >> 1) * 16) * 1024 + w * 256;
    asm volatile("buffer_load_dwordx4 %0, %4, %5, %6 offen\n\tbuffer_load_dwordx4 %1, %4, %5, %6 offen offset:64\n\t"
                 "buffer_load_dwordx4 %2, %4, %5, %6 offen offset:128\n\tbuffer_load_dwordx4 %3, %4, %5, %6 offen offset:192"
                 : "=&v"(xf0), "=&v"(xf1), "=&v"(xf2), "=&v"(xf3) : "v"(vo), "s"(sxa), "s"(so) : "memory");
  }
  const u32 vr0 = (lane >> 3) * 1024 + (((lane & 7) ^ (lane >> 4)) << 4);
  const u32 vr1 = (lane >> 3) * 1024 + (((lane & 7) ^ (4 + (lane >> 4))) << 4) + 7168;
  const u32 vt0 = (lane >> 3) * 1024 + (((lane & 7) ^ (((lane >> 4) & 1) << 2)) << 4);
  const u32 vt1 = vt0 + 7168;
  const u32 rot = ((j + g) & 1) * 4;
  auto qdma = [&](u32 c) {
    u32 kc = (c + rot) & 7, sl = (c & 3) * 16384 + w * 2048;
    dma2(sl, vr0, vr1, sxa, (g * 64 + w * 16) * 1024 + kc * 128);
    dma2(sl + 8192, vt0, vt1, swq, (kc * 64 + w * 16) * 1024 + j * 128);
  };
  qdma(0); qdma(1); qdma(2); qdma(3);
  asm volatile("s_waitcnt vmcnt(16)" : "+v"(ep), "+v"(xf0), "+v"(xf1), "+v"(xf2), "+v"(xf3)::"memory");
  ST(1);
  u32x2 tag;
  {
    u64 e = (((u64)__builtin_amdgcn_readfirstlane(ep.y) << 32) | (u32)__builtin_amdgcn_readfirstlane(ep.x)) + 1;
    tag.x = (u32)e; tag.y = (u32)(e >> 32);
  }
  {
    const u32 base = 65536 + w * 4096 + ((lane >> 4) * 8 + ((lane & 15) >> 2)) * 32 + (lane & 3) * 8;
    bf16x8 wf[4], xf[4] = {__builtin_bit_cast(bf16x8, xf0), __builtin_bit_cast(bf16x8, xf1), __builtin_bit_cast(bf16x8, xf2), __builtin_bit_cast(bf16x8, xf3)};
#pragma unroll
    for (int i = 0; i < 4; ++i) wf[i] = ldstr(base + i * 1024, base + i * 1024 + 128);
    f32x4 acc = {0, 0, 0, 0};
    if (j & 1) {
#pragma unroll
      for (int i = 0; i < 4; ++i) acc = mfma16(xf[i], wf[i], acc);
    } else {
#pragma unroll
      for (int i = 0; i < 4; ++i) acc = mfma16(wf[i], xf[i], acc);
    }
    *(LAS f32x4*)(size_t)(65536 + w * 4096 + lane * 16) = acc;
    asm volatile("s_waitcnt lgkmcnt(0)\n\ts_barrier" ::: "memory");
    f32x4 s = *(LAS f32x4*)(size_t)(65536 + lane * 16);
#pragma unroll
    for (int i = 1; i < 4; ++i) s += *(LAS f32x4*)(size_t)(65536 + i * 4096 + lane * 16);
    u32x2 pv = {pk2(s[0], s[1]), pk2(s[2], s[3])};
    const u32 tq = j >> 1, g4 = lane >> 4, c16 = lane & 15;
    u32 off = WS_KV + (g * 2 + (tq >> 1)) * 2048;
    if (j & 1) off += 1024 + (tq & 1) * 512 + ((g4 >> 1) * 16 + c16) * 16 + (g4 & 1) * 8;
    else off += ((g4 & 1) * 32 + (tq & 1) * 16 + c16) * 16 + (g4 >> 1) * 8;
    st8c(pv, w == 0 ? off : OOB, sws);
  }
  ST(2);
  f32x16 qacc = {0, 0, 0, 0, 0, 0, 0, 0, 0, 0, 0, 0, 0, 0, 0, 0};
  const u32 rrow = (th * 32 + r32) * 128, rsw = (r32 >> 1) & 7;
  const u32 q4 = (lane & 15) >> 2, p4 = lane & 3, blk = (lane >> 4) & 1;
  const u32 trb = q4 * 128 + (((hp * 4 + blk * 2 + (p4 >> 1)) ^ ((q4 >> 1) << 2)) << 4) + (p4 & 1) * 8;
  {
    auto qcomp = [&](u32 c) {
      const u32 sl = (c & 3) * 16384;
#pragma unroll
      for (u32 s = 0; s < 4; ++s) {
        bf16x8 xb = ldsr(sl + rrow + (((2 * s + h) ^ rsw) << 4));
        u32 ta = sl + 8192 + trb + h * 1024 + s * 2048;
        qacc = mfma32(ldstr(ta, ta + 512), xb, qacc);
      }
    };
    waitvl_bar<13>(); qcomp(0);
    waitvl_bar<9>(); qdma(4);
    if (lane < 32) { u32x4 o = {0x3f803f80u, 0x3f803f80u, 0x3f803f80u, 0x3f803f80u};
      *(LAS u32x4*)(size_t)(65536 + (w * 2 + (lane >> 4)) * 2048 + ((lane >> 3) & 1) * 512 + (lane & 7) * 16) = o; }
    qcomp(1);
    waitvl_bar<9>(); qdma(5); qcomp(2);
    waitvl_bar<9>(); qdma(6); qcomp(3);
    WAITV(12);
    st8c(tag, (w == 0 && lane == 0) ? WS_KVF + (g * 8 + j) * 8 : OOB, sws);
    ST(3);
    waitvl_bar<9>(); qdma(7); qcomp(4);
    waitvl_bar<9>(); qcomp(5);
    waitvl_bar<5>(); qcomp(6);
    waitvl_bar<0>(); qcomp(7);
  }
  const float qs = 0.25f * 1.44269504088896340736f;
  u32x4 qa, qb;
#pragma unroll
  for (int i = 0; i < 4; ++i) { qa[i] = pk2(qacc[2 * i] * qs, qacc[2 * i + 1] * qs); qb[i] = pk2(qacc[8 + 2 * i] * qs, qacc[9 + 2 * i] * qs); }
  ST(4);
  poll<1>(WS_KVF + (w * 64 + lane) * 8, true, tag, sws);
  waitvl_bar<0>();
  ST(5);
  if (b == 0) st8c(tag, (w == 0 && lane == 0) ? WS_EPA : OOB, sws);
  f32x16 oa, ob;
  {
    const u32x4 skv = mk_srd(a.ws + WS_KV, 131072), swo = mk_srd(a.wo, 512 * 1024);
    u32 so = ((xcd + 1) & 7) * 16384;
    const u32 vl = lane * 16 + w * 4096;
#pragma unroll
    for (u32 c = 0; c < 4; ++c) { dma4c(c * 16384 + w * 4096, vl, skv, so); so = (so + 16384) & 0x1ffff; }
    u32 ka = lane * 16;
    u32 va = r32 < 16 ? (h * 16 + r32) * 16 : 64512 + ((r32 >= 20 && r32 < 28) ? 64 : 0);
    const u32 vinc = r32 < 16 ? 16384 : 0;
    u32 d0 = vl, d1 = vl, d2 = vl, d3 = vl;
    const u32 wv0 = vt0 + w * 32768 + j * 128;
    const u32 wso = (g & 3) * 131072;
    u32 m0v = w * 4096;
    const u32 m0lim = w * 4096 + 65536;
    asm volatile(
        "s_mov_b32 s40, %[k0]\n\ts_mov_b32 s41, %[k1]\n\ts_mov_b32 s42, %[k2]\n\ts_mov_b32 s43, %[k3]\n\t"
        "s_mov_b32 s44, 0x4000\n\ts_mov_b32 s45, 0x1ffff\n\ts_mov_b32 s46, 0x4000\n\ts_mov_b32 s47, 0\n\t"
        "s_mov_b32 m0, %[m0v]\n\t"
        "v_mov_b32 v156, 0xdddddddd\n\t"
        "v_mov_b32 v88, 0\n\tv_mov_b32 v89, 0\n\tv_mov_b32 v90, 0\n\tv_mov_b32 v91, 0\n\tv_mov_b32 v92, 0\n\tv_mov_b32 v93, 0\n\tv_mov_b32 v94, 0\n\tv_mov_b32 v95, 0\n\t"
        "v_mov_b32 v152, 0\n\tv_mov_b32 v153, 0\n\tv_mov_b32 v154, 0\n\tv_mov_b32 v155, 0\n\t"
        "v_mov_b32 v96, 0\n\tv_mov_b32 v97, 0\n\tv_mov_b32 v98, 0\n\tv_mov_b32 v99, 0\n\tv_mov_b32 v100, 0\n\tv_mov_b32 v101, 0\n\tv_mov_b32 v102, 0\n\tv_mov_b32 v103, 0\n\t"
        "v_mov_b32 v104, 0\n\tv_mov_b32 v105, 0\n\tv_mov_b32 v106, 0\n\tv_mov_b32 v107, 0\n\tv_mov_b32 v108, 0\n\tv_mov_b32 v109, 0\n\tv_mov_b32 v110, 0\n\tv_mov_b32 v111, 0\n\t"
        "v_mov_b32 v112, 0\n\tv_mov_b32 v113, 0\n\tv_mov_b32 v114, 0\n\tv_mov_b32 v115, 0\n\tv_mov_b32 v116, 0\n\tv_mov_b32 v117, 0\n\tv_mov_b32 v118, 0\n\tv_mov_b32 v119, 0\n\t"
        "v_mov_b32 v120, 0\n\tv_mov_b32 v121, 0\n\tv_mov_b32 v122, 0\n\tv_mov_b32 v123, 0\n\tv_mov_b32 v124, 0\n\tv_mov_b32 v125, 0\n\tv_mov_b32 v126, 0\n\tv_mov_b32 v127, 0\n\t"
        "s_waitcnt vmcnt(12)\n\ts_barrier\n\t"
        "ds_read_b128 v[136:139], %[ka]\n\tds_read_b128 " VA0 ", %[va] offset:1024\n\tds_read_b128 " VA1 ", %[va] offset:1536\n\t"
        "s_setprio 0\n\t"
        WL QK("v[64:79]", "v[128:131]") "s_nop 11\n\t"
        "1:\n\t"
        IT(VA0, VA1, VB0, VB1, 2048, 3072, 3584, WL, "", "")
        IT(VB0, VB1, VA0, VA1, 4096, 5120, 5632, WL, "", "")
        IT(VA0, VA1, VB0, VB1, 6144, 7168, 7680, WL, "", "")
        IT(VB0, VB1, VA0, VA1, 8192, 9216, 9728, WL, "", "")
        IT(VA0, VA1, VB0, VB1, 10240, 11264, 11776, WL, "", "")
        IT(VB0, VB1, VA0, VA1, 12288, 13312, 13824, WL, "", "")
        IT(VA0, VA1, VB0, VB1, 14336, 15360, 15872, "s_waitcnt vmcnt(8) lgkmcnt(0)\n\t",
           "s_barrier\n\t"
           "buffer_load_dwordx4 %[d0], s[40:43], %[so] offen sc1 lds\n\tbuffer_load_dwordx4 %[d1], s[40:43], %[so] offen offset:1024 sc1 lds\n\t"
           "v_add_u16 %[ka], s46, %[ka]\n\tv_add_u16 %[va], %[vinc], %[va]\n\t",
           "buffer_load_dwordx4 %[d2], s[40:43], %[so] offen offset:2048 sc1 lds\n\tbuffer_load_dwordx4 %[d3], s[40:43], %[so] offen offset:3072 sc1 lds\n\t")
        IT(VB0, VB1, VA0, VA1, 0, 1024, 1536, WL,
           "s_add_u32 %[so], %[so], s44\n\ts_and_b32 %[so], %[so], s45\n\t",
           "s_add_u32 m0, m0, s46\n\ts_cmp_lg_u32 m0, %[m0lim]\n\t")
        "s_cbranch_scc1 1b\n\t"
        "s_cmp_lg_u32 s47, 0\n\ts_cbranch_scc1 2f\n\t"
        "s_mov_b32 s47, 1\n\ts_mov_b32 s40, %[w0]\n\ts_mov_b32 s41, %[w1]\n\ts_mov_b32 s42, %[w2]\n\t"
        "s_mov_b32 s44, 0x20000\n\ts_mov_b32 s45, 0x7ffff\n\ts_mov_b32 %[so], %[wso]\n\ts_mov_b32 m0, %[m0v]\n\t"
        "v_mov_b32 %[d0], %[wv0]\n\tv_add_u32 %[d1], 7168, %[wv0]\n\tv_add_u32 %[d2], 14336, %[wv0]\n\tv_add_u32 %[d3], 21504, %[wv0]\n\t"
        "s_branch 1b\n\t"
        "2:\n\t"
        "s_setprio 3\n\ts_nop 0\n\t"
        SM(OB, VB1, "v[88:95]")
        "s_nop 15\n\t"
        : "=&{v[96:111]}"(oa), "=&{v[112:127]}"(ob), [ka] "+v"(ka), [va] "+v"(va), [so] "+s"(so),
          [d0] "+v"(d0), [d1] "+v"(d1), [d2] "+v"(d2), [d3] "+v"(d3)
        : "{v[128:131]}"(qa), "{v[132:135]}"(qb), [vinc] "v"(vinc), [wv0] "v"(wv0), [wso] "s"(wso), [m0v] "s"(m0v), [m0lim] "s"(m0lim),
          [k0] "s"(skv.x), [k1] "s"(skv.y), [k2] "s"(skv.z), [k3] "s"(skv.w), [w0] "s"(swo.x), [w1] "s"(swo.y), [w2] "s"(swo.z)
        : "memory", "scc", "s40", "s41", "s42", "s43", "s44", "s45", "s46", "s47",
          "v64", "v65", "v66", "v67", "v68", "v69", "v70", "v71", "v72", "v73", "v74", "v75", "v76", "v77", "v78", "v79",
          "v80", "v81", "v82", "v83", "v84", "v85", "v86", "v87", "v88", "v89", "v90", "v91", "v92", "v93", "v94", "v95",
          "v136", "v137", "v138", "v139", "v140", "v141", "v142", "v143", "v144", "v145", "v146", "v147",
          "v148", "v149", "v150", "v151", "v152", "v153", "v154", "v155", "v156");
  }
  ST(6);
  {
    float ma = 0.f;
#pragma unroll
    for (int i = 0; i < 8; ++i) ma += __builtin_fabsf(oa[i]) * 0.0625f + __builtin_fabsf(ob[i]) * 0.0625f;
    const bool ok = oa[8] >= 0x1p-64f && oa[8] <= 0x1p100f && ob[8] >= 0x1p-64f && ob[8] <= 0x1p100f && ma <= 3.0e38f;
    if (__builtin_amdgcn_ballot_w64(!ok) != 0) {
      const __amdgpu_buffer_rsrc_t rk = __builtin_amdgcn_make_buffer_rsrc(a.ws + WS_KV, 0, 131072, 0x20000);
      const u32x4 ones = {0x3f803f80u, 0x3f803f80u, 0x3f803f80u, 0x3f803f80u};
#pragma unroll 1
      for (int t = 0; t < 2; ++t) {
        const bf16x8 q = __builtin_bit_cast(bf16x8, t ? qb : qa);
        f32x16 z = {0, 0, 0, 0, 0, 0, 0, 0, 0, 0, 0, 0, 0, 0, 0, 0};
        float m = -__builtin_inff();
#pragma unroll 1
        for (u32 kt = 0; kt < 64; ++kt) {
          u32x4 kf = __builtin_amdgcn_raw_buffer_load_b128(rk, lane * 16, kt * 2048, 16);
          f32x16 s = mfma32(__builtin_bit_cast(bf16x8, kf), q, z);
#pragma unroll
          for (int i = 0; i < 16; ++i) m = __builtin_fmaxf(m, s[i]);
        }
        m = __builtin_fmaxf(m, __shfl_xor(m, 32));
        f32x16 o = z;
#pragma unroll 1
        for (u32 kt = 0; kt < 64; ++kt) {
          u32x4 kf = __builtin_amdgcn_raw_buffer_load_b128(rk, lane * 16, kt * 2048, 16);
          u32x4 v0 = __builtin_amdgcn_raw_buffer_load_b128(rk, (h * 16 + (r32 & 15)) * 16 + 1024, kt * 2048, 16);
          u32x4 v1 = __builtin_amdgcn_raw_buffer_load_b128(rk, (h * 16 + (r32 & 15)) * 16 + 1536, kt * 2048, 16);
          if (r32 >= 16) { v0 = ones; v1 = ones; }
          f32x16 s = mfma32(__builtin_bit_cast(bf16x8, kf), q, z);
          f32x8 p0, p1;
#pragma unroll
          for (int i = 0; i < 8; ++i) { p0[i] = __builtin_amdgcn_exp2f(s[i] - m); p1[i] = __builtin_amdgcn_exp2f(s[8 + i] - m); }
          o = __builtin_amdgcn_smfmac_f32_32x32x32_bf16(__builtin_bit_cast(bf16x8, v0), __builtin_bit_cast(bf16x16, p0), o, (int)0xdddddddd, 0, 0);
          o = __builtin_amdgcn_smfmac_f32_32x32x32_bf16(__builtin_bit_cast(bf16x8, v1), __builtin_bit_cast(bf16x16, p1), o, (int)0xdddddddd, 0, 0);
        }
        if (t) ob = o; else oa = o;
      }
    }
  }
  {
    const float ia = __builtin_amdgcn_rcpf(oa[8]), ib = __builtin_amdgcn_rcpf(ob[8]);
    u32x4 pa, pb;
#pragma unroll
    for (int i = 0; i < 4; ++i) { pa[i] = pk2(oa[2 * i] * ia, oa[2 * i + 1] * ia); pb[i] = pk2(ob[2 * i] * ib, ob[2 * i + 1] * ib); }
    const u32 off = WS_O + g * 65536 + ((j * 4 + hp * 2) * 2 + th) * 1024 + lane * 16;
    st16c(pa, off, sws);
    st16c(pb, off + 2048, sws);
  }
  waitvl_bar<2>();
  bf16x8 wf[32];
#pragma unroll
  for (u32 i = 0; i < 32; ++i) { u32 ta = trb + h * 512 + i * 2048; wf[i] = ldstr(ta, ta + 1024); }
  waitvl_bar<0>();
  st8c(tag, (w == 0 && lane == 0) ? WS_OF + g * 256 + j * 8 : OOB, sws);
  ST(7);
  poll<1>(WS_OF + g * 256 + (lane & 7) * 8, true, tag, sws);
  ST(8);
#pragma unroll
  for (u32 c = 0; c < 4; ++c) dma4c(c * 16384 + w * 4096, lane * 16, sws, WS_O + g * 65536 + (((g & 3) + c) & 3) * 16384 + w * 4096);
  f32x16 acc = {0, 0, 0, 0, 0, 0, 0, 0, 0, 0, 0, 0, 0, 0, 0, 0};
  auto ocomp = [&](u32 c) {
#pragma unroll
    for (u32 i = 0; i < 8; ++i) acc = mfma32(ldsr(c * 16384 + i * 2048 + th * 1024 + lane * 16), wf[c * 8 + i], acc);
  };
  waitvl_bar<12>(); ocomp(0);
  waitvl_bar<8>(); ocomp(1);
  waitvl_bar<4>(); ocomp(2);
  waitvl_bar<0>(); ocomp(3);
  ST(9);
  float* op = a.out + (size_t)(g * 64 + th * 32 + h * 4) * 1024 + j * 64 + hp * 32 + r32;
  {
    const __amdgpu_buffer_rsrc_t ro = __builtin_amdgcn_make_buffer_rsrc(a.out, 0, 8388608, 0x20000);
    const u32 vo = (u32)((char*)op - (char*)a.out);
#pragma unroll
    for (int i = 0; i < 16; ++i) { const float x = acc[i]; __builtin_amdgcn_raw_buffer_store_b32(__builtin_bit_cast(u32, x), ro, vo, ((i & 3) + 8 * (i >> 2)) * 4096, AUXA); }
  }
  ST(10);
  STFLUSH;
}

DEVI void cmul(float& r, float& i, float ar, float ai, float br, float bi) { r = ar * br - ai * bi; i = ar * bi + ai * br; }
DEVI void cstep(float& r, float& i, float ar, float ai, float ur, float ui) {
  float nr = __builtin_fmaf(r, ar, __builtin_fmaf(-i, ai, ur)), ni = __builtin_fmaf(i, ar, __builtin_fmaf(r, ai, ui));
  r = nr; i = ni;
}

DEVI void role_b(const Args& a, u32 b, u32 w, u32 lane) {
  const u32 xcd = b & 7, idx = b >> 3, g = xcd * 4 + (idx >> 3), j = idx & 7;
  const u32 r32 = lane & 31, h = lane >> 5, th = w & 1, ph = w >> 1;
  const u32x4 sws = mk_srd(a.ws, WS_END), sxb = mk_srd(a.xb, 2048 * 1024);
  __builtin_amdgcn_s_setprio(2);
  STDECL;
  ST(0);
  u32x2 ep;
  u32 plr, pli, pdt;
  {
    const u32x4 s0 = mk_srd(a.lre, 1024), s1 = mk_srd(a.lim, 1024), s2 = mk_srd(a.ldt, 1024);
    const u32 vo = (j * 64 + ph * 32 + r32) * 2;
    asm volatile("buffer_load_dwordx2 %0, off, %4, 0 offset:256 sc1\n\tbuffer_load_ushort %1, %8, %5, 0 offen\n\t"
                 "buffer_load_ushort %2, %8, %6, 0 offen\n\tbuffer_load_ushort %3, %8, %7, 0 offen"
                 : "=&v"(ep), "=&v"(plr), "=&v"(pli), "=&v"(pdt) : "s"(sws), "s"(s0), "s"(s1), "s"(s2), "v"(vo) : "memory");
  }
  const u32 vr0 = (lane >> 3) * 1024 + (((lane & 7) ^ (lane >> 4)) << 4);
  const u32 vr1 = (lane >> 3) * 1024 + (((lane & 7) ^ (4 + (lane >> 4))) << 4) + 7168;
  const u32 rsw = (r32 >> 1) & 7;
  f32x16 ur = {0, 0, 0, 0, 0, 0, 0, 0, 0, 0, 0, 0, 0, 0, 0, 0}, ui = ur;
  float ar, ai, gr, gi;
  u32x2 tag;
  {
    const u32x4 sbr = mk_srd(a.bre, 512 * 1024), sbi = mk_srd(a.bim, 512 * 1024);
    const u32 xrow = (16 * h + ((lane >> 3) & 3)) * 1024;
    const u32 vx0 = xrow + (((lane & 7) ^ (lane >> 4)) << 4);
    const u32 vx1 = xrow + (((lane & 7) ^ (4 + (lane >> 4))) << 4) + 3072;
    const u32 rot = (j + (g & 3) * 2) & 7;
    auto bdma = [&](u32 c) {
      u32 kc = (c + rot) & 7, sl = (c % 3) * 24576 + w * 2048;
      dma2(sl, vx0, vx1, sxb, (g * 64 + ph * 32 + th * 8) * 1024 + kc * 128);
      dma2(sl + 8192, vr0, vr1, sbr, (j * 64 + w * 16) * 1024 + kc * 128);
      dma2(sl + 16384, vr0, vr1, sbi, (j * 64 + w * 16) * 1024 + kc * 128);
    };
    bdma(0); bdma(1); bdma(2);
    asm volatile("s_waitcnt vmcnt(18)" : "+v"(ep), "+v"(plr), "+v"(pli), "+v"(pdt)::"memory");
    {
      u64 e = (((u64)__builtin_amdgcn_readfirstlane(ep.y) << 32) | (u32)__builtin_amdgcn_readfirstlane(ep.x)) + 1;
      tag.x = (u32)e; tag.y = (u32)(e >> 32);
    }
    {
      const float lr = bf2f(plr), li = bf2f(pli);
      const float dt = bf2f(pk2(expf(bf2f(pdt)), 0.f) & 0xffffu);
      const float e = expf(dt * lr);
      float sn, cs;
      sincosf(dt * li, &sn, &cs);
      ar = e * cs; ai = e * sn;
      const float nr = ar - 1.f, ni = ai;
      const bool sw = __builtin_fabsf(lr) >= __builtin_fabsf(li);
      const float big = sw ? lr : li, sm = sw ? li : lr, rt = sm / big, den = big + sm * rt;
      const float x = sw ? nr : ni, y = sw ? ni : nr;
      gr = 2.f * (x + y * rt) / den;
      gi = 2.f * (y - x * rt) / den;
      gi = sw ? gi : -gi;
      ai = -ai;
    }
    auto bcomp = [&](u32 c) {
      const u32 sl = (c % 3) * 24576;
#pragma unroll
      for (u32 s = 0; s < 4; ++s) {
        const u32 sw = ((2 * s + h) ^ rsw) << 4;
        bf16x8 xf = ldsr(sl + (th * 32 + r32) * 128 + sw);
        ur = mfma32(xf, ldsr(sl + 8192 + (ph * 32 + r32) * 128 + sw), ur);
        ui = mfma32(xf, ldsr(sl + 16384 + (ph * 32 + r32) * 128 + sw), ui);
      }
    };
    waitvl_bar<12>(); bcomp(0);
    waitvl_bar<6>(); bdma(3); bcomp(1);
    waitvl_bar<6>(); bdma(4); bcomp(2);
    waitvl_bar<6>(); bdma(5); bcomp(3);
    waitvl_bar<6>(); bdma(6); bcomp(4);
    waitvl_bar<6>(); bdma(7); bcomp(5);
    waitvl_bar<6>(); bcomp(6);
    waitvl_bar<0>(); bcomp(7);
  }
  ST(1);
  float er = 0.f, ei = 0.f;
#pragma unroll
  for (int i = 0; i < 16; ++i) {
    float xr = gr * ur[i] - gi * ui[i], xi = -(gr * ui[i] + gi * ur[i]);
    ur[i] = xr; ui[i] = xi;
    cstep(er, ei, ar, ai, xr, xi);
  }
  float a16r = ar, a16i = ai;
#pragma unroll
  for (int i = 0; i < 4; ++i) cmul(a16r, a16i, a16r, a16i, a16r, a16i);
  const u32 eb = 73728 + (ph * 32 + r32) * 8;
  { f32x2 e2 = {er, ei}; *(LAS f32x2*)(size_t)(eb + (th * 2 + h) * 512) = e2; }
  waitvl_bar<0>();
  f32x2 es[4];
#pragma unroll
  for (int s = 0; s < 4; ++s) es[s] = *(LAS f32x2*)(size_t)(eb + s * 512);
  if (th == 0) {
    float tr = es[0].x, ti = es[0].y;
#pragma unroll
    for (int s = 1; s < 4; ++s) cstep(tr, ti, a16r, a16i, es[s].x, es[s].y);
    u32x2 ev = {__builtin_bit_cast(u32, tr), __builtin_bit_cast(u32, ti)};
    st8c(ev, h == 0 ? WS_E + (j * 32 + g) * 512 + (ph * 32 + r32) * 8 : OOB, sws);
    WAITV(0);
    st8c(tag, lane == 0 ? WS_EF + ((j * 2 + ph) * 32 + g) * 8 : OOB, sws);
  }
  ST(2);
  float a64r = a16r, a64i = a16i;
  cmul(a64r, a64i, a64r, a64i, a64r, a64i);
  cmul(a64r, a64i, a64r, a64i, a64r, a64i);
  float akr = a64r, aki = a64i;
#pragma unroll
  for (int i = 0; i < 4; ++i) cmul(akr, aki, akr, aki, akr, aki);
  poll<4>(WS_EF + ((j * 2 + ph) * 32 + r32) * 8, r32 < g, tag, sws);
  ST(3);
  float cr = 0.f, ci = 0.f;
  {
    const __amdgpu_buffer_rsrc_t re = __builtin_amdgcn_make_buffer_rsrc(a.ws + WS_E + j * 16384, 0, 16384, 0x20000);
    u32x2 ev[16];
#pragma unroll
    for (int m = 0; m < 16; ++m) {
      int gp = (int)g - 1 - 16 * (int)h - m;
      ev[m] = __builtin_amdgcn_raw_buffer_load_b64(re, gp >= 0 ? (u32)gp * 512 + (ph * 32 + r32) * 8 : OOB, 0, 16);
    }
#pragma unroll
    for (int m = 15; m >= 0; --m) { const f32x2 e2 = __builtin_bit_cast(f32x2, ev[m]); cstep(cr, ci, a64r, a64i, e2.x, e2.y); }
    float orr = __shfl_xor(cr, 32), oi = __shfl_xor(ci, 32);
    float lr_ = h ? orr : cr, li_ = h ? oi : ci, hr_ = h ? cr : orr, hi_ = h ? ci : oi;
    if (g > 16) { cstep(hr_, hi_, akr, aki, lr_, li_); cr = hr_; ci = hi_; } else { cr = lr_; ci = li_; }
#pragma unroll
    for (u32 s = 0; s < 3; ++s) {
      float tr = cr, ti = ci;
      cstep(tr, ti, a16r, a16i, es[s].x, es[s].y);
      const bool use = s < th * 2 + h;
      cr = use ? tr : cr; ci = use ? ti : ci;
    }
  }
#pragma unroll
  for (int i = 0; i < 16; ++i) { cstep(cr, ci, ar, ai, ur[i], ui[i]); ur[i] = cr; ui[i] = ci; }
  {
    const u32 off = WS_H + (g * 8 + j) * 16384 + (ph * 2 + th) * 2048 + h * 512;
#pragma unroll
    for (u32 i = 0; i < 2; ++i) {
      u32x4 pr, pi;
#pragma unroll
      for (int e = 0; e < 4; ++e) { pr[e] = pk2(ur[8 * i + 2 * e], ur[8 * i + 2 * e + 1]); pi[e] = pk2(ui[8 * i + 2 * e], ui[8 * i + 2 * e + 1]); }
      const u32 o = off + i * 1024 + ((r32 ^ ((2 * i + h) << 2)) << 4);
      st16c(pr, o, sws);
      st16c(pi, o + 8192, sws);
    }
  }
  waitvl_bar<0>();
  st8c(tag, (w == 0 && lane == 0) ? WS_HF + g * 256 + j * 8 : OOB, sws);
  ST(4);
  const u32 col = j * 64 + ph * 32 + r32;
  f32x16 acc;
  {
    const float dk = bf2f(a.dsk[col]);
    const u16* xp = a.xb + (size_t)(g * 64 + th * 32 + h * 4) * 512 + col;
#pragma unroll
    for (int i = 0; i < 16; ++i) acc[i] = bf2f(xp[((i & 3) + 8 * (i >> 2)) * 512]) * dk;
    asm volatile("" : "+v"(acc));
  }
  poll<4>(WS_HF + g * 256 + (lane & 7) * 8, true, tag, sws);
  ST(5);
  if (g == 31 && j == 0) st8c(tag, (w == 0 && lane == 0) ? WS_EPB : OOB, sws);
  {
    const u32x4 scr = mk_srd(a.cre, 512 * 1024), sci = mk_srd(a.cim, 512 * 1024);
    auto cdma = [&](u32 c) {
      u32 jj = ((c >> 1) + j) & 7, sl = (c % 5) * 16384 + w * 2048;
      dma2c(sl, lane * 16, sws, WS_H + (g * 8 + jj) * 16384 + (c & 1) * 8192 + w * 2048);
      dma2(sl + 8192, vr0, vr1, (c & 1) ? sci : scr, (j * 64 + w * 16) * 1024 + jj * 128);
    };
    const u32 q4 = (lane & 15) >> 2, p4 = lane & 3, blk = (lane >> 4) & 1;
    const u32 tb0 = (th * 2 + (p4 >> 1)) * 1024 + blk * 512 + (((8 * h + q4) ^ ((2 * (p4 >> 1) + blk) << 2)) << 4) + (p4 & 1) * 8;
    const u32 tb1 = tb0 ^ 64;
    auto ccomp = [&](u32 c) {
      const u32 sl = (c % 5) * 16384;
#pragma unroll
      for (u32 s = 0; s < 4; ++s) {
        const u32 o = sl + (s >> 1) * 4096 + (s & 1) * 256;
        acc = mfma32(ldstr(tb0 + o, tb1 + o), ldsr(sl + 8192 + (ph * 32 + r32) * 128 + (((2 * s + h) ^ rsw) << 4)), acc);
      }
    };
    cdma(0); cdma(1); cdma(2); cdma(3); cdma(4);
    waitvl_bar<16>(); ccomp(0);
#pragma unroll
    for (u32 c = 1; c < 12; ++c) { waitvl_bar<12>(); cdma(c + 4); ccomp(c); }
    waitvl_bar<12>(); ccomp(12);
    waitvl_bar<8>(); ccomp(13);
    waitvl_bar<4>(); ccomp(14);
    waitvl_bar<0>(); ccomp(15);
  }
  ST(6);
  float* op = a.out + (size_t)(g * 64 + th * 32 + h * 4) * 1024 + 512 + col;
  {
    const __amdgpu_buffer_rsrc_t ro = __builtin_amdgcn_make_buffer_rsrc(a.out, 0, 8388608, 0x20000);
    const u32 vo = (u32)((char*)op - (char*)a.out);
#pragma unroll
    for (int i = 0; i < 16; ++i) { const float x = acc[i]; __builtin_amdgcn_raw_buffer_store_b32(__builtin_bit_cast(u32, x), ro, vo, ((i & 3) + 8 * (i >> 2)) * 4096, AUXB); }
  }
  ST(7);
  STFLUSH;
}

__global__ void __launch_bounds__(256, 2) fused_kernel(Args a) {
  __shared__ __attribute__((aligned(16))) char lds[81920];
  u64 tf = 0;
  asm volatile("" ::"v"((u32)(size_t)(LAS char*)lds));
  const u32 lane = threadIdx.x & 63, w = __builtin_amdgcn_readfirstlane(threadIdx.x >> 6), b = blockIdx.x;
  if (b < 256) role_a(a, b, w, lane, tf); else {
    role_b(a, b - 256, w, lane);
  }
}

extern "C" void kernel_launch(void* const* d_in, const int* in_sizes, int n_in, void* d_out, int out_size, void* d_ws,
                              size_t ws_size, hipStream_t stream) {
  Args a;
  a.xa = (const u16*)d_in[0]; a.wq = (const u16*)d_in[1]; a.wk = (const u16*)d_in[2]; a.wv = (const u16*)d_in[3];
  a.wo = (const u16*)d_in[4]; a.xb = (const u16*)d_in[5]; a.lre = (const u16*)d_in[6]; a.lim = (const u16*)d_in[7];
  a.bre = (const u16*)d_in[8]; a.bim = (const u16*)d_in[9]; a.cre = (const u16*)d_in[10]; a.cim = (const u16*)d_in[11];
  a.ldt = (const u16*)d_in[12]; a.dsk = (const u16*)d_in[13];
  a.out = (float*)d_out; a.ws = (char*)d_ws;
  fused_kernel<<<512, 256, 0, stream>>>(a);
}
```

```cpp
#define TITER 7
#include <hip/hip_runtime.h>
#define QROT 0
#define BROT 0
#define BEARLY 0
#define BDELAY0 0
#define PSL 4
#define KFS 32
#define TBODY 7
#define PCNT 1
#define ADELAY 6
#define B1DS 8
#define B1DM 8
#define BSTART2 7500
#define PACEN 2
#define PACE __builtin_amdgcn_s_sleep(PACEN)
#define EDLY 0
#define HDLY 0
#define TSL 4
#define BSTART 4000
#define FPOS 5
#define BDELAY 0
#define TDELAY 0
#define AUXA 17
#define AUXB 17
typedef unsigned short u16;
typedef unsigned u32;
typedef unsigned long long u64;
typedef float f32x2 __attribute__((ext_vector_type(2)));
typedef float f32x4 __attribute__((ext_vector_type(4)));
typedef float f32x8 __attribute__((ext_vector_type(8)));
typedef float f32x16 __attribute__((ext_vector_type(16)));
typedef __bf16 bf16x2 __attribute__((ext_vector_type(2)));
typedef __bf16 bf16x8 __attribute__((ext_vector_type(8)));
typedef __bf16 bf16x16 __attribute__((ext_vector_type(16)));
typedef short s16x4 __attribute__((ext_vector_type(4)));
typedef u32 u32x2 __attribute__((ext_vector_type(2)));
typedef u32 u32x4 __attribute__((ext_vector_type(4)));
#define LAS __attribute__((address_space(3)))
#define DEVI __device__ __forceinline__

constexpr u32 FS = 128;
constexpr u32 WS_EP = 0;
constexpr u32 WS_KVP = 4096;
constexpr u32 WS_KVF = 32768;
constexpr u32 WS_RF = 65536;
constexpr u32 WS_TF = 98304;
constexpr u32 WS_EF = 131072;
constexpr u32 WS_HF = 262144;
constexpr u32 WS_KV = 393216;
constexpr u32 WS_E = 524288;
constexpr u32 WS_OF = 655360;
constexpr u32 WS_O = 1048576;
constexpr u32 WS_H = WS_O + 2097152;
constexpr u32 WS_END = WS_H + 4194304;
constexpr u32 WS_DBG = WS_END;
constexpr u32 OOB = 0x80000000u;

struct Args {
  const u16 *xa, *wq, *wk, *wv, *wo;
  float* out;
  char* ws;
  const u16 *xb, *lre, *lim, *bre, *bim, *cre, *cim, *ldt, *dsk;
};
typedef u64 u64x8 __attribute__((ext_vector_type(8)));

DEVI u32x4 mk_srd(const void* p, u32 bytes) {
  u64 a = (u64)p;
  u32x4 r = {(u32)a, (u32)(a >> 32) & 0xffffu, bytes, 0x20000u};
  return r;
}
DEVI void dma2(u32 m0v, u32 v0, u32 v1, u32x4 srd, u32 soff) {
  asm volatile("s_mov_b32 m0, %0\n\ts_nop 0\n\tbuffer_load_dwordx4 %1, %3, %4 offen lds\n\tbuffer_load_dwordx4 %2, %3, %4 offen offset:1024 lds"
               ::"s"(m0v), "v"(v0), "v"(v1), "s"(srd), "s"(soff) : "memory");
}
DEVI void dma2c(u32 m0v, u32 v0, u32x4 srd, u32 soff) {
  asm volatile("s_mov_b32 m0, %0\n\ts_nop 0\n\tbuffer_load_dwordx4 %1, %2, %3 offen sc1 lds\n\tbuffer_load_dwordx4 %1, %2, %3 offen offset:1024 sc1 lds"
               ::"s"(m0v), "v"(v0), "s"(srd), "s"(soff) : "memory");
}
DEVI void dma4(u32 m0v, u32 v0, u32x4 srd, u32 soff) {
  asm volatile("s_mov_b32 m0, %0\n\ts_nop 0\n\tbuffer_load_dwordx4 %1, %2, %3 offen lds\n\tbuffer_load_dwordx4 %1, %2, %3 offen offset:1024 lds\n\t"
               "buffer_load_dwordx4 %1, %2, %3 offen offset:2048 lds\n\tbuffer_load_dwordx4 %1, %2, %3 offen offset:3072 lds"
               ::"s"(m0v), "v"(v0), "s"(srd), "s"(soff) : "memory");
}
DEVI void dma4c(u32 m0v, u32 v0, u32x4 srd, u32 soff) {
  asm volatile("s_mov_b32 m0, %0\n\ts_nop 0\n\tbuffer_load_dwordx4 %1, %2, %3 offen sc1 lds\n\tbuffer_load_dwordx4 %1, %2, %3 offen offset:1024 sc1 lds\n\t"
               "buffer_load_dwordx4 %1, %2, %3 offen offset:2048 sc1 lds\n\tbuffer_load_dwordx4 %1, %2, %3 offen offset:3072 sc1 lds"
               ::"s"(m0v), "v"(v0), "s"(srd), "s"(soff) : "memory");
}
DEVI void dma1(u32 m0v, u32 v0, u32x4 srd, u32 soff) {
  asm volatile("s_mov_b32 m0, %0\n\ts_nop 0\n\tbuffer_load_dwordx4 %1, %2, %3 offen lds" ::"s"(m0v), "v"(v0), "s"(srd), "s"(soff) : "memory");
}
DEVI void dma4v(u32 m0v, u32 v0, u32 v1, u32 v2, u32 v3, u32x4 srd, u32 soff) {
  asm volatile("s_mov_b32 m0, %0\n\ts_nop 0\n\tbuffer_load_dwordx4 %1, %5, %6 offen lds\n\tbuffer_load_dwordx4 %2, %5, %6 offen offset:1024 lds\n\t"
               "buffer_load_dwordx4 %3, %5, %6 offen offset:2048 lds\n\tbuffer_load_dwordx4 %4, %5, %6 offen offset:3072 lds"
               ::"s"(m0v), "v"(v0), "v"(v1), "v"(v2), "v"(v3), "s"(srd), "s"(soff) : "memory");
}
template <int OFF> DEVI u64x8 args_request() {
  u64x8 k;
  asm volatile("s_load_dwordx16 %0, %1, %2" : "=&s"(k) : "s"(__builtin_amdgcn_kernarg_segment_ptr()), "i"(OFF));
  return k;
}
template <int OFF> DEVI void dma1c(u32 m0v, u32 v0, u32x4 srd, u32 soff) {
  asm volatile("s_mov_b32 m0, %0\n\ts_nop 0\n\tbuffer_load_dwordx4 %1, %2, %3 offen offset:%4 sc1 lds" ::"s"(m0v), "v"(v0), "s"(srd), "s"(soff), "i"(OFF) : "memory");
}
template <int OFF> DEVI void dma1o(u32 m0v, u32 v0, u32x4 srd, u32 soff) {
  asm volatile("s_mov_b32 m0, %0\n\ts_nop 0\n\tbuffer_load_dwordx4 %1, %2, %3 offen offset:%4 lds" ::"s"(m0v), "v"(v0), "s"(srd), "s"(soff), "i"(OFF) : "memory");
}
DEVI void dmaq(u32 m0v, u32 v0, u32 v1, u32x4 sx, u32 ox, u32 v2, u32 v3, u32x4 sw, u32 ow) {
  asm volatile("s_mov_b32 m0, %0\n\ts_nop 0\n\tbuffer_load_dwordx4 %5, %7, %8 offen offset:2048 lds\n\tbuffer_load_dwordx4 %6, %7, %8 offen offset:3072 lds\n\t"
               "buffer_load_dwordx4 %1, %3, %4 offen lds\n\tbuffer_load_dwordx4 %2, %3, %4 offen offset:1024 lds"
               ::"s"(m0v), "v"(v0), "v"(v1), "s"(sx), "s"(ox), "v"(v2), "v"(v3), "s"(sw), "s"(ow) : "memory");
}
DEVI void st8c(u32x2 v, u32 voff, u32x4 srd) {
  asm volatile("buffer_store_dwordx2 %0, %1, %2, 0 offen sc1" ::"v"(v), "v"(voff), "s"(srd) : "memory");
}
DEVI void st16c(u32x4 v, u32 voff, u32x4 srd) {
  asm volatile("buffer_store_dwordx4 %0, %1, %2, 0 offen sc1\n\ts_nop 1" ::"v"(v), "v"(voff), "s"(srd) : "memory");
}
DEVI void st16s(u32x4 v, u32 voff, u32x4 srd, u32 soff) {
  asm volatile("buffer_store_dwordx4 %0, %1, %2, %3 offen sc1\n\ts_nop 1" ::"v"(v), "v"(voff), "s"(srd), "s"(soff) : "memory");
}
#define WAITV(n) asm volatile("s_waitcnt vmcnt(" #n ")" ::: "memory")
#define WAITVL(n) asm volatile("s_waitcnt vmcnt(" #n ") lgkmcnt(0)" ::: "memory")
#define BAR() asm volatile("s_barrier" ::: "memory")
template <int N> DEVI void waitvl_bar() { asm volatile("s_waitcnt vmcnt(%0) lgkmcnt(0)\n\ts_barrier" ::"i"(N) : "memory"); }

template <int SLEEP> DEVI void poll(u32 voff, bool need, u32x2 tag, u32x4 srd) {
#pragma unroll 1
  for (u32 it = 0; it < (1u << 18); ++it) {
    u32x2 f;
    asm volatile("buffer_load_dwordx2 %0, %1, %2, 0 offen sc1\n\ts_waitcnt vmcnt(0)" : "=&v"(f) : "v"(voff), "s"(srd) : "memory");
    if (__builtin_amdgcn_ballot_w64(__builtin_bit_cast(u64, f) != __builtin_bit_cast(u64, tag)) == 0) break;
    if (SLEEP) __builtin_amdgcn_s_sleep(SLEEP);
  }
}
DEVI u32 pk2(float a, float b) {
  f32x2 x = {a, b};
  return __builtin_bit_cast(u32, __builtin_convertvector(x, bf16x2));
}
DEVI float bf2f(u32 v) { return __builtin_bit_cast(float, v << 16); }
DEVI bf16x8 ldsr(u32 a) { return *(const LAS bf16x8*)(size_t)a; }
DEVI bf16x8 ldstr(u32 a0, u32 a1) {
  s16x4 x = __builtin_amdgcn_ds_read_tr16_b64_v4i16((LAS s16x4*)(size_t)a0);
  s16x4 y = __builtin_amdgcn_ds_read_tr16_b64_v4i16((LAS s16x4*)(size_t)a1);
  return __builtin_bit_cast(bf16x8, __builtin_shufflevector(x, y, 0, 1, 2, 3, 4, 5, 6, 7));
}
#define SCHED() __builtin_amdgcn_sched_barrier(0)
DEVI f32x16 mfma32(bf16x8 a, bf16x8 b, f32x16 c) { return __builtin_amdgcn_mfma_f32_32x32x16_bf16(a, b, c, 0, 0, 0); }
DEVI f32x4 mfma16(bf16x8 a, bf16x8 b, f32x4 c) { return __builtin_amdgcn_mfma_f32_16x16x32_bf16(a, b, c, 0, 0, 0); }

#define SW 0
#define STDECL
#define STC(v)
#define ST(i)
#define STFLUSH

#define SK(i)
#define SB(i)
#define SQ(i)
#define SA(i) ST(i)
#define STR2(x) #x
#define STR(x) STR2(x)
#define EX(b, i) "v_exp_f32 v[" #b "+" #i "], v[" #b "+" #i "]\n\t"
#define SM(o, vv, b, i) "v_smfmac_f32_32x32x32_bf16 " o ", " vv ", v[" #b "+" #i ":" #b "+" #i "+7], v156\n\t"
#define OA "v[96:111]"
#define OB "v[112:127]"
#define QA "v[128:131]"
#define QB "v[132:135]"
#define KF "v[136:139]"
#define QK(b, k, q) "v_mfma_f32_32x32x16_bf16 v[" #b ":" #b "+15], " k ", " q ", 0\n\t"
#define WL "s_waitcnt lgkmcnt(0)\n\t"
#define IT(A, B, AN, BN, X0, X1, Y0, Y1, KO, VO, E0, E1, E2, E3, E4, E5)                              \
  EX(BN, 13) EX(BN, 14) EX(BN, 15) EX(A, 0) EX(A, 1) SM(OB, Y1, BN, 8) WL E0                          \
  "ds_read_b128 " Y0 ", %[va] offset:" #VO "+1024\n\tds_read_b128 " Y1 ", %[va] offset:" #VO "+1536\n\t" \
  EX(A, 2) EX(A, 3) EX(A, 4) EX(A, 5) EX(A, 6) QK(AN, KF, QA) E1                                      \
  EX(A, 7) EX(A, 8) EX(A, 9) EX(A, 10) EX(A, 11) EX(A, 12) SM(OA, X0, A, 0) E2                        \
  EX(A, 13) EX(A, 14) EX(A, 15) EX(B, 0) EX(B, 1) SM(OA, X1, A, 8) E3                                 \
  EX(B, 2) EX(B, 3) EX(B, 4) EX(B, 5) EX(B, 6) QK(BN, KF, QB) E4                                      \
  "ds_read_b128 " KF ", %[ka] offset:" #KO "\n\t"                                                   \
  EX(B, 7) EX(B, 8) EX(B, 9) EX(B, 10) EX(B, 11) EX(B, 12) SM(OB, X0, B, 0) E5
#define VA0 "v[140:143]"
#define VA1 "v[144:147]"
#define VB0 "v[148:151]"
#define VB1 "v[152:155]"
#define ITE(KO, VO, E0, E1, E2, E3, E4, E5) IT(64, 80, 160, 176, VA0, VA1, VB0, VB1, KO, VO, E0, E1, E2, E3, E4, E5)
#define ITO(KO, VO, E0, E1, E2, E3, E4, E5) IT(160, 176, 64, 80, VB0, VB1, VA0, VA1, KO, VO, E0, E1, E2, E3, E4, E5)
#define STEP "s_add_u32 %[so], %[so], s44\n\t"
#define NEXT "s_add_u32 %[so], %[so], s50\n\t"
#define MASK "s_and_b32 %[so], %[so], s45\n\t"
#define M0A "s_add_u32 m0, m0, s46\n\t"
#define M0M "s_and_b32 m0, m0, 0xffff\n\t"
#define BUMP "v_add_u16 %[ka], s46, %[ka]\n\tv_add_u16 %[va], %[vinc], %[va]\n\t"
#define TRG(i) TRG_(i, TITER)
#define TRG_(i, t) TRG__(i, t)
#define TRG__(i, t) ".if " #i " == " #t "\n\tbuffer_store_dwordx2 %[tag], %[tfo], %[sws], 0 offen sc1\n\t.endif\n\t"
#define LT0 ""
#define LT1 ""
#define LTO
#define ADMA(i, o) "buffer_load_dwordx4 %[dv], s[40:43], %[so] offen offset:" #o " sc1 lds\n\t"
#define ABAR "s_barrier\n\t"
#define BPRIO 0
#define PRHI "s_setprio 3\n\t"
#define PRLO "s_setprio 1\n\t"

DEVI void role_a(u32 b, u32 w, u32 lane, u64 tf) {
  u64x8 k0 = args_request<0>();
  const u32 xcd = b & 7, idx = b >> 3, g = xcd * 4 + (idx >> 3), j = idx & 7;
  const u32 r32 = lane & 31, h = lane >> 5, th = w & 1, hp = w >> 1;
  __builtin_amdgcn_s_setprio(2);
  STDECL;
  u32 l16 = lane * 16, xa_[4];
#pragma unroll
  for (u32 u = 0; u < 4; ++u) xa_[u] = (th * 2 + (r32 >> 4)) * 4096 + (r32 & 15) * 128 + (((2 * u + h) ^ ((r32 >> 1) & 7)) << 4);
  u32 vr0 = (lane >> 3) * 1024 + (((lane & 7) ^ (lane >> 4)) << 4);
  u32 vr1 = (lane >> 3) * 1024 + (((lane & 7) ^ (4 + (lane >> 4))) << 4) + 7168;
  u32 vt0 = (lane >> 3) * 1024 + (((lane & 7) ^ (((lane >> 4) & 1) << 2)) << 4) + j * 128;
  u32 vt1 = vt0 + 7168;
  u32 trb;
  {
    const u32 q4 = (lane & 15) >> 2, p4 = lane & 3, blk = (lane >> 4) & 1;
    trb = q4 * 128 + (((hp * 4 + blk * 2 + (p4 >> 1)) ^ ((q4 >> 1) << 2)) << 4) + (p4 & 1) * 8;
  }
  u32 ta_ = 2048 + trb + h * 1024, tw_ = trb + h * 512;
  f32x16 oa = {0, 0, 0, 0, 0, 0, 0, 0, 0, 0, 0, 0, 0, 0, 0, 0}, ob = oa;
  u32 vl = l16 + w * 4096, ka = l16, va, vinc = (~r32 & 16) << 10;
  {
    const u32 z = 192 - (((r32 - 20) >> 3) == 0 ? 64 : 0), o = (r32 & 4) << 4;
    const u32 c = 64512 + ((r32 & 27) == 16 ? o : z), v = (h * 16 + r32) * 16;
    va = r32 < 16 ? v : c;
  }
  u32 dv = vl;
  f32x8 sinit = {0.f, 0.f, 0.f, 0.f, 0.f, -__builtin_inff(), -__builtin_inff(), -__builtin_inff()};
  u32x4 vinit = {0, 0, 0, 0};
  u32 sidx = 0xddddddddu;
  u32 fll = lane == 0 ? 0 : OOB, fl0 = fll | (w == 0 ? 0 : OOB);
  u32 f0a = WS_KVF + ((xcd * 32 + 32 + lane) & 255) * FS, fwa = WS_KVP + (w * 64 + lane) * 8;
  u32 onea = 65536 + (w * 2 + h) * 2048 + ((lane >> 4) & 1) * 512 + (lane & 15) * 16, onev = (lane & 8) ? 0u : 0x3f803f80u;
  asm volatile("" : "+{v[96:111]}"(oa), "+{v[112:127]}"(ob), "+{v[184:191]}"(sinit), "+{v[152:155]}"(vinit), "+{v156}"(sidx), "+v"(ka), "+v"(va),
               "+v"(vinc), "+v"(dv), "+v"(fl0), "+v"(fll), "+v"(f0a), "+v"(fwa), "+v"(onea), "+v"(onev), "+v"(vl));
  u32 xb_[4], tb_ = ta_ + 65536;
#pragma unroll
  for (u32 u = 0; u < 4; ++u) xb_[u] = xa_[u] + 65536;
  asm volatile("" : "+v"(l16), "+v"(xa_[0]), "+v"(xa_[1]), "+v"(xa_[2]), "+v"(xa_[3]), "+v"(xb_[0]), "+v"(xb_[1]), "+v"(xb_[2]), "+v"(xb_[3]),
               "+v"(vr0), "+v"(vr1), "+v"(vt0), "+v"(vt1), "+v"(ta_), "+v"(tb_), "+v"(tw_));
  asm volatile("s_waitcnt lgkmcnt(0)" : "+s"(k0));
  Args a;
  a.xa = (const u16*)k0[0]; a.wq = (const u16*)k0[1]; a.wk = (const u16*)k0[2]; a.wv = (const u16*)k0[3]; a.wo = (const u16*)k0[4];
  a.out = (float*)k0[5]; a.ws = (char*)k0[6];
  char* const ws = a.ws;
  const u32x4 sws = mk_srd(ws, WS_END), sxa = mk_srd(a.xa, 2048 * 1024), swq = mk_srd(a.wq - 1024, 512 * 1024 + 2048);
  ST(0);
  if (ADELAY) __builtin_amdgcn_s_sleep(ADELAY);
  u32x2 ep;
  asm volatile("buffer_load_dwordx2 %0, off, %1, 0 sc1" : "=v"(ep) : "s"(sws) : "memory");
  const u32 rot = QROT == 0 ? 0 : QROT == 2 ? (g & 1) * 4 : (g & 3) * 2;
  constexpr u32 QSL[8] = {16384, 32768, 49152, 0, 65536, 16384, 32768, 49152};
  auto qd = [&](u32 c) {
    const u32 kc = (c + rot) & 7;
    dmaq(QSL[c] + w * 4096, vr0, vr1, sxa, (g * 64 + w * 16) * 1024 + kc * 128, vt0, vt1, swq, (kc * 64 + w * 16) * 1024);
  };
  qd(0); qd(1); qd(2); qd(3);
  asm volatile("s_waitcnt vmcnt(12)\n\ts_barrier" : "+v"(ep)::"memory");
  ST(1);
  u32x2 tag;
  {
    u64 e = (((u64)__builtin_amdgcn_readfirstlane(ep.y) << 32) | (u32)__builtin_amdgcn_readfirstlane(ep.x)) + 1;
    tag.x = (u32)e; tag.y = (u32)(e >> 32);
  }
  const u32x4 skv = mk_srd(ws + WS_KV, 131072), swo = mk_srd(a.wo, 512 * 1024);
  u32 so = ((xcd + 1) & 7) * 16384;
  u32x4 qa, qb;
  {
    f32x16 qacc = {0, 0, 0, 0, 0, 0, 0, 0, 0, 0, 0, 0, 0, 0, 0, 0};
    bf16x8 fx[2][4], fw[2][4];
    auto step = [&](u32 c) {
      const u32 n = c + 4, kc = (n + rot) & 7;
#pragma unroll
      for (u32 s = 0; s < 4; ++s) {
        SCHED();
        if (c > 0) qacc = mfma32(fw[(c - 1) & 1][s], fx[(c - 1) & 1][s], qacc);
        SCHED();
        if (c < 8) {
          const u32 ta = c == 4 ? tb_ + s * 4096 : ta_ + QSL[c & 7] + s * 4096;
          fw[c & 1][s] = ldstr(ta, ta + 512);
          fx[c & 1][s] = c == 4 ? ldsr(xb_[s]) : ldsr(xa_[s] + QSL[c & 7]);
        }
        SCHED();
        if (n < 8) {
          const u32 m = QSL[n & 7] + w * 4096, ox = (g * 64 + w * 16) * 1024 + kc * 128, ow = (kc * 64 + w * 16) * 1024;
          if (s == 0) dma1o<2048>(m, vt0, swq, ow);
          if (s == 1) dma1o<3072>(m, vt1, swq, ow);
          if (s == 2) dma1o<0>(m, vr0, sxa, ox);
          if (s == 3) dma1o<1024>(m, vr1, sxa, ox);
        }

      }
      SCHED();
    };
    step(0);
    waitvl_bar<12>(); SQ(2); step(1);
    waitvl_bar<12>(); SQ(3); step(2);
    waitvl_bar<12>(); SQ(4); step(3);
    __builtin_amdgcn_s_setprio(3);
    st8c(tag, fl0 + WS_RF + (g * 8 + j) * FS, sws);
    WAITV(13); BAR(); SQ(5); SA(2);
    u32x2 f0 = tag;
    auto fload = [&]() { if (w == 0) asm volatile("buffer_load_dwordx2 %0, %1, %2, 0 offen sc1" : "+v"(f0) : "v"(f0a), "s"(sws) : "memory"); };
    step(4);
    waitvl_bar<9>(); SQ(6);
    *(LAS u32x4*)(size_t)onea = u32x4{onev, onev, onev, onev};
    step(5);
    waitvl_bar<5>(); SQ(7);
    step(6);
    WAITV(1); BAR(); SQ(8); SA(3);
    fload();
    step(7);
    step(8);
    asm volatile("s_waitcnt vmcnt(0)" : "+v"(f0)::"memory");
    SQ(9);
    if (__builtin_amdgcn_ballot_w64(f0.x != tag.x || f0.y != tag.y) != 0) poll<0>(f0a, true, tag, sws);
    BAR(); SQ(10); SA(4);
#pragma unroll
    for (u32 u = 0; u < 4; ++u) dma1c<0>(u * 4096 + w * 1024, l16, skv, so + u * 4096 + w * 1024);
    so = (so + 16384) & 0x1ffff;
    const float qs = 0.25f * 1.44269504088896340736f;
#pragma unroll
    for (int i = 0; i < 4; ++i) { qa[i] = pk2(qacc[2 * i] * qs, qacc[2 * i + 1] * qs); qb[i] = pk2(qacc[8 + 2 * i] * qs, qacc[9 + 2 * i] * qs); }
    asm volatile("" : "+{v[128:131]}"(qa), "+{v[132:135]}"(qb));
  }
  const u32 wv0 = vt0;
  const u32 wso = (j * 65536 + w * 32768) & 0x7ffff;
  SA(5); SQ(11);
  {
    const u32 m0v = w * 4096 + 16384;
    const u32 tfo = fl0 + WS_TF + (g * 8 + j) * FS;
    const u64 tag64 = ((u64)tag.y << 32) | tag.x;
    asm volatile(
        "s_mov_b32 s40, %[k0]\n\ts_mov_b32 s41, %[k1]\n\ts_mov_b32 s42, 0x80000\n\ts_mov_b32 s43, 0x20000\n\t"
        "s_mov_b32 s44, 0\n\ts_mov_b32 s45, 0x1ffff\n\ts_mov_b32 s46, 0x4000\n\ts_mov_b32 s47, 1\n\ts_mov_b32 s50, 0x4000\n\t"
        "s_mov_b32 m0, %[m0v]\n\t"
        "s_branch 7f\n\t"
        "4:\n\t"
        "s_mov_b32 s59, 0x40000\n\t"
        "6:\n\t"
        "buffer_load_dwordx2 v[158:159], %[fwa], %[sws], 0 offen sc1\n\ts_sub_u32 s59, s59, 1\n\ts_cbranch_scc1 5f\n\t"
        "s_waitcnt vmcnt(0)\n\tv_cmp_ne_u64 vcc, %[tag64], v[158:159]\n\ts_nop 4\n\ts_cbranch_vccnz 6b\n\ts_branch 5f\n\t"
        "7:\n\t"
        "s_waitcnt vmcnt(3) lgkmcnt(0)\n\ts_barrier\n\t"
        "ds_read_b128 " VB0 ", %[ka]\n\tds_read_b128 " KF ", %[ka] offset:2048\n\t"
        "ds_read_b128 " VA0 ", %[va] offset:1024\n\tds_read_b128 " VA1 ", %[va] offset:1536\n\t"
        "s_waitcnt lgkmcnt(3)\n\t" QK(64, VB0, QA) QK(80, VB0, QB) "s_nop 7\n\t"
        ITE(4096, 2048, LT0, "", "", "s_waitcnt vmcnt(0)\n\t" ABAR, "", ADMA(0, 0))
        ITO(6144, 4096, "", LT1, ADMA(1, 1024), ADMA(2, 2048), "", ADMA(3, 3072))
        ITE(8192, 6144, NEXT, MASK, "buffer_load_dwordx2 v[158:159], %[fwa], %[sws], 0 offen sc1\n\t", M0A PRLO, "", "")
        ITO(10240, 8192, "", "", "", "", "", "")
        ITE(12288, 10240, "", "", "", "", "", "")
        ITO(14336, 12288, "", "", "", "", "", "")
        ITE(0, 14336, "", "", "s_waitcnt vmcnt(0)\n\tv_cmp_ne_u64 vcc, %[tag64], v[158:159]\n\t", "s_cbranch_vccnz 4b\n\t5:\n\t" WL ABAR BUMP, "", PRHI ADMA(0, 0))
        ITO(2048, 0, "", "", ADMA(1, 1024), ADMA(2, 2048), "", ADMA(3, 3072))
        PRLO NEXT MASK M0A
        "1:\n\t"
        ITE(4096, 2048, LT0, "", PRHI ADMA(0, 0), PRLO STEP, "", "")
        ITO(6144, 4096, "", LT1, PRHI ADMA(1, 1024), PRLO STEP, "", "")
        ITE(8192, 6144, "", "", PRHI ADMA(2, 2048), PRLO STEP, "", "")
        ITO(10240, 8192, "", "", PRHI ADMA(3, 3072), PRLO NEXT, MASK, M0A M0M)
        ITE(12288, 10240, "s_cmp_eq_u32 s47, 5\n\t", "s_cselect_b32 s40, %[w0], s40\n\t", "s_cselect_b32 s41, %[w1], s41\n\ts_cselect_b32 %[so], %[wso], %[so]\n\t",
            "s_cselect_b32 s44, 7168, s44\n\ts_cselect_b32 s50, 109568, s50\n\t", "s_cselect_b32 s45, 0x7ffff, s45\n\t", "s_cselect_b64 vcc, -1, 0\n\t")
        ITO(14336, 12288, "", "", "v_cndmask_b32 %[dv], %[dv], %[wv0], vcc\n\t", "", "", "")
        ITE(0, 14336, "", "", "", "s_waitcnt vmcnt(4) lgkmcnt(0)\n\t" ABAR BUMP, "", "")
        ITO(2048, 0, "", "", "s_add_u32 s47, s47, 1\n\ts_cmp_lg_u32 s47, 7\n\t", "", "", "")
        "s_cbranch_scc1 1b\n\t"
        ITE(4096, 2048, LT0, TRG(0), PRHI ADMA(0, 0), PRLO STEP, "", PRHI ADMA(1, 1024))
        ITO(6144, 4096, PRLO STEP, LT1 TRG(1), PRHI ADMA(2, 2048), PRLO STEP, "", PRHI ADMA(3, 3072))
        ITE(8192, 6144, PRLO NEXT, MASK TRG(2), M0A, "", "", PRHI ADMA(0, 0))
        ITO(10240, 8192, PRLO STEP, TRG(3), PRHI ADMA(1, 1024), PRLO STEP, "", PRHI ADMA(2, 2048))
        ITE(12288, 10240, PRLO STEP, TRG(4), PRHI ADMA(3, 3072), PRLO NEXT, MASK, M0A)
        ITO(14336, 12288, "", TRG(5), "", "", "", "")
        ITE(0, 14336, "", TRG(6), "", WL ABAR, "", PRHI ADMA(0, 0))
        ITO(2048, 0, STEP, TRG(7), ADMA(1, 1024), STEP ADMA(2, 2048), STEP, ADMA(3, 3072))
        EX(176, 13) EX(176, 14) EX(176, 15)
        "s_setprio 3\n\ts_waitcnt lgkmcnt(0)\n\t"
        SM(OB, VB1, 176, 8)
        : "+{v[96:111]}"(oa), "+{v[112:127]}"(ob), "+{v[184:191]}"(sinit), "+{v[152:155]}"(vinit), [ka] "+v"(ka), [va] "+v"(va), [so] "+s"(so),
          [dv] "+v"(dv) LTO
        : "{v[128:131]}"(qa), "{v[132:135]}"(qb), "{v156}"(sidx), [vinc] "v"(vinc), [wv0] "v"(wv0), [wso] "s"(wso),
          [m0v] "s"(m0v), [tag] "v"(tag), [tag64] "s"(tag64), [tfo] "v"(tfo), [fwa] "v"(fwa), [sws] "s"(sws),
          [k0] "s"(skv.x), [k1] "s"(skv.y), [w0] "s"(swo.x), [w1] "s"(swo.y)
        : "memory", "scc", "vcc", "s40", "s41", "s42", "s43", "s44", "s45", "s46", "s47", "s48", "s49", "s50", "s59",
          "v64", "v65", "v66", "v67", "v68", "v69", "v70", "v71", "v72", "v73", "v74", "v75", "v76", "v77", "v78", "v79", "v80", "v81", "v82", "v83", "v84", "v85", "v86", "v87", "v88", "v89", "v90", "v91", "v92", "v93", "v94", "v95", "v136", "v137", "v138", "v139", "v140", "v141", "v142", "v143", "v144", "v145", "v146", "v147", "v148", "v149", "v150", "v151", "v158", "v159", "v160", "v161", "v162", "v163", "v164", "v165", "v166", "v167", "v168", "v169", "v170", "v171", "v172", "v173", "v174", "v175", "v176", "v177", "v178", "v179", "v180", "v181", "v182", "v183");
  }
  const u32 ooff = WS_O + g * 65536 + ((j * 4 + hp * 2) * 2 + th) * 1024 + l16;
  const u32 olds = 65536 + (hp * 4 + th) * 1024 + l16;
  auto opub = [&](const f32x16& o, u32 t) {
    const float inv = __builtin_amdgcn_rcpf(o[8]);
    u32x4 p;
#pragma unroll
    for (int i = 0; i < 4; ++i) p[i] = pk2(o[2 * i] * inv, o[2 * i + 1] * inv);
    st16c(p, ooff + t * 2048, sws);
    *(LAS u32x4*)(size_t)(olds + t * 2048) = p;
  };
  opub(oa, 0);
  asm volatile("s_nop 7" : "+{v[112:127]}"(ob));
  opub(ob, 1);
  SA(6);
  {
    float ma = 0.f;
#pragma unroll
    for (int i = 0; i < 8; ++i) { ma = __builtin_fmaf(oa[i], 0.f, ma); ma = __builtin_fmaf(ob[i], 0.f, ma); }
    const bool ok = oa[8] >= 0x1p-64f && oa[8] <= 0x1p100f && ob[8] >= 0x1p-64f && ob[8] <= 0x1p100f && ma == 0.f;
    if (__builtin_expect(__builtin_amdgcn_ballot_w64(!ok) != 0, 0)) {
      WAITV(0);
      const __amdgpu_buffer_rsrc_t rk = __builtin_amdgcn_make_buffer_rsrc(ws + WS_KV, 0, 131072, 0x20000);
      const u32x4 ones = {0x3f803f80u, 0x3f803f80u, 0x3f803f80u, 0x3f803f80u};
#pragma unroll 1
      for (int t = 0; t < 2; ++t) {
        const bf16x8 q = __builtin_bit_cast(bf16x8, t ? qb : qa);
        f32x16 z = {0, 0, 0, 0, 0, 0, 0, 0, 0, 0, 0, 0, 0, 0, 0, 0};
        float m = -__builtin_inff();
#pragma unroll 1
        for (u32 kt = 0; kt < 64; ++kt) {
          u32x4 kf = __builtin_amdgcn_raw_buffer_load_b128(rk, lane * 16, kt * 2048, 16);
          f32x16 s = mfma32(__builtin_bit_cast(bf16x8, kf), q, z);
#pragma unroll
          for (int i = 0; i < 16; ++i) m = __builtin_fmaxf(m, s[i]);
        }
        m = __builtin_fmaxf(m, __shfl_xor(m, 32));
        f32x16 o = z;
#pragma unroll 1
        for (u32 kt = 0; kt < 64; ++kt) {
          u32x4 kf = __builtin_amdgcn_raw_buffer_load_b128(rk, lane * 16, kt * 2048, 16);
          u32x4 v0 = __builtin_amdgcn_raw_buffer_load_b128(rk, (h * 16 + (r32 & 15)) * 16 + 1024, kt * 2048, 16);
          u32x4 v1 = __builtin_amdgcn_raw_buffer_load_b128(rk, (h * 16 + (r32 & 15)) * 16 + 1536, kt * 2048, 16);
          if (r32 >= 16) { v0 = ones; v1 = ones; }
          f32x16 s = mfma32(__builtin_bit_cast(bf16x8, kf), q, z);
          f32x8 p0, p1;
#pragma unroll
          for (int i = 0; i < 8; ++i) { p0[i] = __builtin_amdgcn_exp2f(s[i] - m); p1[i] = __builtin_amdgcn_exp2f(s[8 + i] - m); }
          o = __builtin_amdgcn_smfmac_f32_32x32x32_bf16(__builtin_bit_cast(bf16x8, v0), __builtin_bit_cast(bf16x16, p0), o, (int)0xdddddddd, 0, 0);
          o = __builtin_amdgcn_smfmac_f32_32x32x32_bf16(__builtin_bit_cast(bf16x8, v1), __builtin_bit_cast(bf16x16, p1), o, (int)0xdddddddd, 0, 0);
        }
        opub(o, t);
      }
    }
  }
  f32x16 acc = {0, 0, 0, 0, 0, 0, 0, 0, 0, 0, 0, 0, 0, 0, 0, 0};
  {
    bf16x8 wf[32], fo[2][4];
    const u32 ob_ = th * 1024 + l16;
    auto wread = [&](u32 i0, u32 i1) {
#pragma unroll
      for (u32 i = i0; i < i1; ++i) wf[i] = ldstr(tw_ + i * 2048, tw_ + i * 2048 + 1024);
      SCHED();
    };
    auto odma = [&](u32 c) { dma2c((c - 1) * 8192 + w * 2048, l16, sws, WS_O + g * 65536 + ((j + c) & 7) * 8192 + w * 2048); };
    auto oread = [&](u32 c) {
#pragma unroll
      for (u32 i = 0; i < 4; ++i) fo[c & 1][i] = ldsr(ob_ + (c ? (c - 1) * 8192 : 65536) + i * 2048);
      SCHED();
    };
    auto omul = [&](u32 c) {
#pragma unroll
      for (u32 i = 0; i < 4; ++i) acc = mfma32(fo[c & 1][i], wf[c * 4 + i], acc);
      SCHED();
    };
    waitvl_bar<6>();
    oread(0); wread(0, 12);
    WAITV(0);
    st8c(tag, fll + WS_OF + (g * 32 + j * 4 + w) * FS, sws);
    SA(7);
    wread(12, 24);
    BAR();
    wread(24, 32);
    if (w == 0) poll<0>(WS_OF + (g * 32 + r32) * FS, true, tag, sws);
    waitvl_bar<0>();
    SA(8);
    auto ostep = [&](u32 c) {
#pragma unroll
      for (u32 i = 0; i < 4; ++i) {
        SCHED();
        acc = mfma32(fo[(c - 1) & 1][i], wf[(c - 1) * 4 + i], acc);
        SCHED();
        fo[c & 1][i] = ldsr(ob_ + (c - 1) * 8192 + i * 2048);
      }
      SCHED();
    };
    odma(1); odma(2); odma(3); odma(4); odma(5);
    WAITV(8); BAR(); ostep(1); odma(6);
    WAITV(8); BAR(); ostep(2); odma(7);
    WAITV(8); BAR(); ostep(3);
    WAITV(6); BAR(); ostep(4);
    WAITV(4); BAR(); ostep(5);
    WAITV(2); BAR(); ostep(6);
    WAITV(0); BAR(); oread(7); omul(6); omul(7);
  }
  SA(9);
  float* op = a.out + (size_t)(g * 64 + th * 32 + h * 4) * 1024 + j * 64 + hp * 32 + r32;
  {
    const __amdgpu_buffer_rsrc_t ro = __builtin_amdgcn_make_buffer_rsrc(a.out, 0, 8388608, 0x20000);
    const u32 vo = (u32)((char*)op - (char*)a.out);
#pragma unroll
    for (int i = 0; i < 16; ++i) { const float x = acc[i]; __builtin_amdgcn_raw_buffer_store_b32(__builtin_bit_cast(u32, x), ro, vo, ((i & 3) + 8 * (i >> 2)) * 4096, AUXA); }
  }
  SA(10);
  STFLUSH;
}

DEVI void cmul(float& r, float& i, float ar, float ai, float br, float bi) { r = ar * br - ai * bi; i = ar * bi + ai * br; }
DEVI void cstep(float& r, float& i, float ar, float ai, float ur, float ui) {
  float nr = __builtin_fmaf(r, ar, __builtin_fmaf(-i, ai, ur)), ni = __builtin_fmaf(i, ar, __builtin_fmaf(r, ai, ui));
  r = nr; i = ni;
}

DEVI f32x2 fma2(f32x2 a, f32x2 b, f32x2 c) { return __builtin_elementwise_fma(a, b, c); }
DEVI void cstep2(f32x2& r, f32x2& i, float ar, float ai, f32x2 ur, f32x2 ui) {
  const f32x2 a = {ar, ar}, b = {ai, ai};
  const f32x2 nr = fma2(r, a, fma2(-i, b, ur)), ni = fma2(i, a, fma2(r, b, ui));
  r = nr; i = ni;
}

DEVI void role_b(u32 b, u32 w, u32 lane) {
  u64x8 k0 = args_request<0>(), k1 = args_request<64>();
  const u32 xcd = b & 7, idx = b >> 3, g = xcd * 4 + (idx >> 3), j = idx & 7;
  const u32 c16 = lane & 15, g4 = lane >> 4, r32 = lane & 31, h = lane >> 5, th = w & 1, pt = w >> 1;
  __builtin_amdgcn_s_setprio(3);
  STDECL;
  u32 vr0 = (lane >> 3) * 1024 + (((lane & 7) ^ (lane >> 4)) << 4);
  u32 vr1 = (lane >> 3) * 1024 + (((lane & 7) ^ (4 + (lane >> 4))) << 4) + 7168;
  const u32 xrow = (32 * h + 8 * ((lane >> 3) & 1) + ((lane >> 4) & 1)) * 1024;
  u32 vx0 = xrow + (((lane & 7) ^ (lane >> 4)) << 4);
  u32 vx1 = xrow + (((lane & 7) ^ (4 + (lane >> 4))) << 4) + 1024;
  u32 ra[4], rb[4];
#pragma unroll
  for (u32 s = 0; s < 4; ++s) { ra[s] = r32 * 128 + (((2 * s + h) ^ ((r32 >> 1) & 7)) << 4); rb[s] = ra[s] + 8192 + w * 4096; }
  u32 vo = (j * 64 + w * 16 + c16) * 2, vd = (j * 64 + pt * 32 + r32) * 2, l16 = lane * 16, fll = lane == 0 ? 0 : OOB;
  asm volatile("" : "+v"(vr0), "+v"(vr1), "+v"(vx0), "+v"(vx1), "+v"(ra[0]), "+v"(ra[1]), "+v"(ra[2]), "+v"(ra[3]),
               "+v"(rb[0]), "+v"(rb[1]), "+v"(rb[2]), "+v"(rb[3]), "+v"(vo), "+v"(vd), "+v"(l16), "+v"(fll));
  u32 xs_[4], xk_[4], red = 65536 + l16;
#pragma unroll
  for (u32 u = 0; u < 4; ++u) {
    xs_[u] = (4 * u + g4) * 1024 + ((c16 ^ (4 * u + g4)) << 4) - u * 1024;
    xk_[u] = w * 4096 + c16 * 256 + (((4 * u + g4) ^ c16) << 4);
  }
  u32 kvrd = 65536 + w * 4096 + ((lane >> 4) * 8 + ((lane & 15) >> 2)) * 32 + (lane & 3) * 8;
  u32 kvoff;
  {
    const u32 tq = j >> 1;
    const u32 ov = 1024 + (tq & 1) * 512 + ((g4 >> 1) * 16 + c16) * 16 + (g4 & 1) * 8;
    const u32 ok = ((g4 & 1) * 32 + (tq & 1) * 16 + c16) * 16 + (g4 >> 1) * 8;
    const u32 mv = 0u - (j & 1);
    kvoff = (WS_KV + (g * 2 + (tq >> 1)) * 2048 + ((ov & mv) | (ok & ~mv)));
  }
  u32 kvfo = lane == 0 ? WS_KVF + (g * 8 + j) * FS : WS_KVP + (g * 8 + j) * 8;
  kvfo = lane < 2 ? kvfo : OOB;
  asm volatile("" : "+v"(kvfo), "+v"(xs_[0]), "+v"(xs_[1]), "+v"(xs_[2]), "+v"(xs_[3]), "+v"(xk_[0]), "+v"(xk_[1]), "+v"(xk_[2]), "+v"(xk_[3]), "+v"(red), "+v"(kvrd), "+v"(kvoff));
  asm volatile("s_waitcnt lgkmcnt(0)" : "+s"(k0), "+s"(k1));
  Args a;
  a.out = (float*)k0[5]; a.ws = (char*)k0[6]; a.xb = (const u16*)k0[7];
  a.lre = (const u16*)k1[0]; a.lim = (const u16*)k1[1]; a.bre = (const u16*)k1[2]; a.bim = (const u16*)k1[3];
  a.cre = (const u16*)k1[4]; a.cim = (const u16*)k1[5]; a.ldt = (const u16*)k1[6]; a.dsk = (const u16*)k1[7];
  char* const ws = a.ws;
  const u32x4 sws = mk_srd(ws, WS_END), sxb = mk_srd(a.xb, 2048 * 1024);
  ST(0);
  const u64 tb = __builtin_amdgcn_s_memtime();
  dma4v(w * 4096, xs_[0], xs_[1], xs_[2], xs_[3], mk_srd((const void*)k0[0], 2048 * 1024), (g * 64 + (j >> 1) * 16) * 1024 + w * 256);
  dma4(65536 + w * 4096, l16, mk_srd((const void*)((j & 1) ? k0[3] : k0[2]), 512 * 32), w * 4096);
  u32x2 ep;
  u32 plr, pli, pdt, pdk;
  {
    const u32x4 s0 = mk_srd(a.lre, 1024), s1 = mk_srd(a.lim, 1024), s2 = mk_srd(a.ldt, 1024), s3 = mk_srd(a.dsk, 1024);
    asm volatile("buffer_load_dwordx2 %0, off, %5, 0 sc1\n\tbuffer_load_ushort %1, %10, %6, 0 offen\n\t"
                 "buffer_load_ushort %2, %10, %7, 0 offen\n\tbuffer_load_ushort %3, %10, %8, 0 offen\n\tbuffer_load_ushort %4, %11, %9, 0 offen"
                 : "=&v"(ep), "=&v"(plr), "=&v"(pli), "=&v"(pdt), "=&v"(pdk) : "s"(sws), "s"(s0), "s"(s1), "s"(s2), "s"(s3), "v"(vo), "v"(vd) : "memory");
  }
  f32x16 ur = {0, 0, 0, 0, 0, 0, 0, 0, 0, 0, 0, 0, 0, 0, 0, 0}, ui = ur;
  float ar, ai, gr, gi, a8r, a8i, a16r, a16i, a64r, a64i, akr, aki;
  u32x4 dg0, dg1;
  const u32 stg = 32768 + w * 12288;
  u32 rc0[4], rc1[4], tb0, tb1, rxw, rxr, vout, xw, xr_, eoff, efo, efp, hfo, hfp, rfa, tfa, vq, ho0, ho1, evo[8];
  u32x2 tag, tagv;
  {
    const u32x4 sbr = mk_srd(a.bre, 512 * 1024), sbi = mk_srd(a.bim, 512 * 1024);
    const u32 rot = BROT == 0 ? 0 : (j + (g & 3) * 2) & 7;
    auto bdx = [&](u32 c) { dma2((c % 3) * 24576 + w * 2048, vx0, vx1, sxb, (g * 64 + pt * 16 + th * 4) * 1024 + ((c + rot) & 7) * 128); };
    auto bdr = [&](u32 c) { dma2((c % 3) * 24576 + 8192 + w * 4096, vr0, vr1, sbr, (j * 64 + w * 16) * 1024 + ((c + rot) & 7) * 128); };
    auto bdi = [&](u32 c) { dma2((c % 3) * 24576 + 10240 + w * 4096, vr0, vr1, sbi, (j * 64 + w * 16) * 1024 + ((c + rot) & 7) * 128); };
    WAITV(5);
    SK(1);
    {
      bf16x8 wf[4], xf[4];
#pragma unroll
      for (int i = 0; i < 4; ++i) { xf[i] = ldsr(xk_[i]); wf[i] = ldstr(kvrd + i * 1024, kvrd + i * 1024 + 128); }
      f32x4 acc = {0, 0, 0, 0};
      if (j & 1) {
#pragma unroll
        for (int i = 0; i < 4; ++i) acc = mfma16(xf[i], wf[i], acc);
      } else {
#pragma unroll
        for (int i = 0; i < 4; ++i) acc = mfma16(wf[i], xf[i], acc);
      }
      *(LAS f32x4*)(size_t)(red + w * 4096) = acc;
      waitvl_bar<63>();
      SK(2);
      if (w == 0) {
        f32x4 s0 = *(LAS f32x4*)(size_t)(red), s1 = *(LAS f32x4*)(size_t)(red + 4096), s2 = *(LAS f32x4*)(size_t)(red + 8192), s3 = *(LAS f32x4*)(size_t)(red + 12288);
        s0 = (s0 + s1) + (s2 + s3);
        st8c(u32x2{pk2(s0[0], s0[1]), pk2(s0[2], s0[3])}, kvoff, sws);
      }
    }
    SK(3);
    asm volatile("s_waitcnt vmcnt(0)" : "+v"(ep), "+v"(plr), "+v"(pli), "+v"(pdt), "+v"(pdk)::"memory");
    {
      u64 e = (((u64)__builtin_amdgcn_readfirstlane(ep.y) << 32) | (u32)__builtin_amdgcn_readfirstlane(ep.x)) + 1;
      tag.x = (u32)e; tag.y = (u32)(e >> 32);
    }
    tagv = tag;
    if (w == 0) st8c(tag, kvfo, sws);
    __builtin_amdgcn_s_setprio(2);
    SB(1);
    {
      const float lr = bf2f(plr), li = bf2f(pli);
      const float dt = bf2f(pk2(expf(bf2f(pdt)), 0.f) & 0xffffu);
      const float e = expf(dt * lr);
      float sn, cs;
      sincosf(dt * li, &sn, &cs);
      ar = e * cs; ai = e * sn;
      const float nr = ar - 1.f, ni = ai;
      const bool sw = __builtin_fabsf(lr) >= __builtin_fabsf(li);
      const float big = sw ? lr : li, sm = sw ? li : lr, rt = sm / big, den = big + sm * rt;
      const float x = sw ? nr : ni, y = sw ? ni : nr;
      gr = 2.f * (x + y * rt) / den;
      gi = 2.f * (y - x * rt) / den;
      gi = sw ? gi : -gi;
      ai = -ai;
      a8r = ar; a8i = ai;
#pragma unroll
      for (int i = 0; i < 3; ++i) cmul(a8r, a8i, a8r, a8i, a8r, a8i);
      cmul(a16r, a16i, a8r, a8i, a8r, a8i);
      cmul(a64r, a64i, a16r, a16i, a16r, a16i);
      cmul(a64r, a64i, a64r, a64i, a64r, a64i);
      akr = a64r; aki = a64i;
#pragma unroll
      for (int i = 0; i < 3; ++i) cmul(akr, aki, akr, aki, akr, aki);
      const u32 dv = pdk << ((r32 & 1) * 16), di = (r32 >> 1) - 4 * h;
#pragma unroll
      for (u32 i = 0; i < 4; ++i) { dg0[i] = di == i ? dv : 0u; dg1[i] = di == 8 + i ? dv : 0u; }
      asm volatile("" : "+v"(ar), "+v"(ai), "+v"(gr), "+v"(gi), "+v"(a8r), "+v"(a8i), "+v"(a16r), "+v"(a16i), "+v"(a64r), "+v"(a64i),
                   "+v"(akr), "+v"(aki), "+v"(dg0), "+v"(dg1));
      SCHED();
    }
    {
      const u32 q4 = c16 >> 2, p4 = lane & 3, m = (g4 & 1) * 2 + (p4 >> 1);
      tb0 = stg + m * 256 + (((8 * h + q4) ^ (m << 2)) << 4) + (p4 & 1) * 8;
      tb1 = tb0 ^ 64;
#pragma unroll
      for (u32 s = 0; s < 4; ++s) { rc0[s] = ra[s] + pt * 12288 + 16384; rc1[s] = rc0[s] ^ 4096; }
      rxw = 49152 + w * 4096 + l16; rxr = rxw ^ 8192;
      vout = (g * 64 + th * 32 + h * 4) * 4096 + 2048 + (j * 64 + pt * 32 + r32) * 4;
      xw = stg + lane * 8; xr_ = stg + c16 * 8;
      eoff = g4 == 0 ? WS_E + ((j * 4 + w) * 32 + g) * 128 + c16 * 8 : OOB;
      efo = fll + WS_EF + ((j * 4 + w) * 32 + g) * FS;
      efp = WS_EF + ((j * 4 + w) * 32 + (r32 < g ? r32 : g)) * FS;
      hfo = fll + WS_HF + (g * 32 + j * 4 + w) * FS;
      hfp = WS_HF + (g * 32 + r32) * FS;
      rfa = WS_RF + (g * 8 + j) * FS; tfa = WS_TF + (g * 8 + j) * FS;
      vq = (g * 64 + th * 32 + r32) * 1024 + j * 128 + pt * 64 + h * 16;
      const u32 off = WS_H + g * 131072 + h * 65536 + (j * 4 + w) * 1024, m0_ = (g4 & 1) * 2;
      ho0 = off + m0_ * 256 + ((c16 ^ (m0_ << 2)) << 4);
      ho1 = off + (m0_ + 1) * 256 + ((c16 ^ ((m0_ + 1) << 2)) << 4);
      const int gb = (int)g - 8 - 8 * (int)g4;
#pragma unroll
      for (int i = 0; i < 8; ++i) evo[i] = gb + i >= 0 ? (u32)(gb + i) * 128 + c16 * 8 : OOB;
      asm volatile("" : "+v"(rc0[0]), "+v"(rc0[1]), "+v"(rc0[2]), "+v"(rc0[3]), "+v"(rc1[0]), "+v"(rc1[1]), "+v"(rc1[2]), "+v"(rc1[3]),
                   "+v"(tb0), "+v"(tb1), "+v"(rxw), "+v"(rxr), "+v"(vout), "+v"(xw), "+v"(xr_), "+v"(eoff), "+v"(efo), "+v"(efp), "+v"(hfo), "+v"(hfp));
      asm volatile("" : "+v"(rfa), "+v"(tfa), "+v"(vq), "+v"(ho0), "+v"(ho1), "+v"(evo[0]), "+v"(evo[1]), "+v"(evo[2]), "+v"(evo[3]),
                   "+v"(evo[4]), "+v"(evo[5]), "+v"(evo[6]), "+v"(evo[7]), "+v"(tagv));
    }
    SB(2);
    while (__builtin_amdgcn_s_memtime() - tb < BSTART) __builtin_amdgcn_s_sleep(2);
    __builtin_amdgcn_s_setprio(BPRIO);
    BAR();
    bdx(0); bdr(0); bdi(0); bdx(1); bdr(1); bdi(1);
    bf16x8 fx[2][8], fb[2][4];
    auto step = [&](u32 c, bool rd, bool mul) {
      const u32 sl = (c % 3) * 24576, n = c + 2, p = (c - 1) & 1;
#pragma unroll
      for (u32 s = 0; s < 4; ++s) {
        SCHED();
        if (mul) ur = mfma32(fx[p][2 * s], fb[p][s], ur);
        SCHED();
        if (rd) { fb[c & 1][s] = ldsr(rb[s] + sl); fx[c & 1][2 * s] = ldsr(ra[s] + sl); }
        SCHED();
        if (mul) ui = mfma32(fx[p][2 * s + 1], fb[p][s], ui);
        SCHED();
        if (rd) fx[c & 1][2 * s + 1] = ldsr(ra[s] + sl + 4096);
        SCHED();
        if (rd && c > 1 && n < 8 && s == 0) bdx(n);
        if (rd && c > 1 && n < 8 && s == 1) bdr(n);
        if (rd && c > 1 && n < 8 && s == 2) bdi(n);
      }
      SCHED();
    };
    SB(3);
    waitvl_bar<6>(); SB(4); step(0, true, false);
    waitvl_bar<0>(); SB(5); step(1, true, true);
    step(2, false, true);
    while (__builtin_amdgcn_s_memtime() - tb < BSTART2) __builtin_amdgcn_s_sleep(2);
    bdx(2); PACE; bdr(2); PACE; bdi(2); PACE; bdx(3); PACE; bdr(3); PACE; bdi(3);
    waitvl_bar<6>(); SB(6); step(2, true, false);
    waitvl_bar<6>(); SB(7); step(3, true, true);
    waitvl_bar<6>(); SB(8); step(4, true, true);
    waitvl_bar<6>(); SB(9); step(5, true, true);
    waitvl_bar<6>(); SB(10); step(6, true, true);
    waitvl_bar<0>(); step(7, true, true);
    waitvl_bar<0>(); step(8, false, true);
  }
  __builtin_amdgcn_s_setprio(2);
  ST(1);
  f32x2 er = {0.f, 0.f}, ei = {0.f, 0.f};
  {
#pragma unroll
    for (int i = 0; i < 16; ++i) {
      const float p = ur[i], q = ui[i];
      const auto r = __builtin_amdgcn_permlane16_swap(__builtin_bit_cast(u32, p), __builtin_bit_cast(u32, q), false, false);
      const u32 r0 = r[0], r1 = r[1];
      ur[i] = __builtin_bit_cast(float, r0); ui[i] = __builtin_bit_cast(float, r1);
    }
    const f32x2 g_r = {gr, gr}, g_i = {gi, gi};
#pragma unroll
    for (int m = 0; m < 8; ++m) {
      const f32x2 xr = {ur[2 * m], ur[2 * m + 1]}, xi = {ui[2 * m], ui[2 * m + 1]};
      const f32x2 yr = fma2(g_r, xr, -(g_i * xi)), yi = fma2(-g_r, xi, -(g_i * xr));
      ur[2 * m] = yr.x; ur[2 * m + 1] = yr.y; ui[2 * m] = yi.x; ui[2 * m + 1] = yi.y;
      if (m == 0) { er = yr; ei = yi; } else cstep2(er, ei, ar, ai, yr, yi);
    }
  }
  f32x2 es[4];
  {
    float tr = er.x, ti = ei.x;
    cstep(tr, ti, a8r, a8i, er.y, ei.y);
    *(volatile LAS f32x2*)(size_t)xw = f32x2{tr, ti};
#pragma unroll
    for (int s = 0; s < 4; ++s) es[s] = *(volatile LAS f32x2*)(size_t)(xr_ + s * 128);
    tr = es[0].x; ti = es[0].y;
#pragma unroll
    for (int s = 1; s < 4; ++s) cstep(tr, ti, a16r, a16i, es[s].x, es[s].y);
    u32x2 ev = {__builtin_bit_cast(u32, tr), __builtin_bit_cast(u32, ti)};
    st8c(ev, eoff, sws);
  }
  const u32x4 scr = mk_srd(a.cre, 512 * 1024), sci = mk_srd(a.cim, 512 * 1024);
  auto cdma = [&](u32 c) {
    const u32 sl = (c % 5) * 16384 + w * 2048, so = (j * 64 + w * 16) * 1024 + c * 128;
    dma2(sl, vr0, vr1, scr, so);
    dma2(sl + 8192, vr0, vr1, sci, so);
  };
  u32x4 xq0, xq1;
  u32x2 rf;
  asm volatile("buffer_load_dwordx2 %0, %1, %2, 0 offen sc1" : "=v"(rf) : "v"(rfa), "s"(sws) : "memory");
  cdma(0); cdma(1);
  {
    asm volatile("buffer_load_dwordx4 %0, %2, %3, 0 offen\n\tbuffer_load_dwordx4 %1, %2, %3, 0 offen offset:32" : "=&v"(xq0), "=&v"(xq1) : "v"(vq), "s"(sxb) : "memory");
  }
  asm volatile("s_waitcnt vmcnt(10)" : "+v"(rf)::"memory");
  if (__builtin_amdgcn_ballot_w64(rf.x != tag.x || rf.y != tag.y) != 0) poll<4>(rfa, true, tag, sws);
  st8c(tagv, efo, sws);
  ST(2);
  asm volatile("s_waitcnt vmcnt(1)" : "+v"(xq0), "+v"(xq1)::"memory");
  if (EDLY) __builtin_amdgcn_s_sleep(EDLY);
  poll<PSL>(efp, true, tag, sws);
  ST(3);
  {
    const __amdgpu_buffer_rsrc_t re = __builtin_amdgcn_make_buffer_rsrc(ws + WS_E + (j * 4 + w) * 4096, 0, 4096, 0x20000);
    u32x2 ev[8];
#pragma unroll
    for (int m = 0; m < 8; ++m) ev[m] = __builtin_amdgcn_raw_buffer_load_b64(re, evo[m], 0, 16);
    float cr, ci;
    { const f32x2 e2 = __builtin_bit_cast(f32x2, ev[0]); cr = e2.x; ci = e2.y; }
#pragma unroll
    for (int m = 1; m < 8; ++m) { const f32x2 e2 = __builtin_bit_cast(f32x2, ev[m]); cstep(cr, ci, a64r, a64i, e2.x, e2.y); }
    *(volatile LAS f32x2*)(size_t)xw = f32x2{cr, ci};
    f32x2 ps[4];
#pragma unroll
    for (int s = 0; s < 4; ++s) ps[s] = *(volatile LAS f32x2*)(size_t)(xr_ + s * 128);
    cr = ps[3].x; ci = ps[3].y;
#pragma unroll
    for (int k = 2; k >= 0; --k) {
      if (8u * (k + 1) < g) cstep(cr, ci, akr, aki, ps[k].x, ps[k].y); else { cr = ps[k].x; ci = ps[k].y; }
    }
#pragma unroll
    for (u32 s = 0; s < 3; ++s) {
      float tr = cr, ti = ci;
      cstep(tr, ti, a16r, a16i, es[s].x, es[s].y);
      cr = s < g4 ? tr : cr; ci = s < g4 ? ti : ci;
    }
    float br = cr, bi = ci;
    cstep(br, bi, a8r, a8i, er.x, ei.x);
    f32x2 hr = {cr, br}, hi = {ci, bi};
#pragma unroll
    for (int m = 0; m < 8; ++m) {
      cstep2(hr, hi, ar, ai, f32x2{ur[2 * m], ur[2 * m + 1]}, f32x2{ui[2 * m], ui[2 * m + 1]});
      ur[2 * m] = hr.x; ur[2 * m + 1] = hr.y; ui[2 * m] = hi.x; ui[2 * m + 1] = hi.y;
    }
#pragma unroll
    for (u32 i = 0; i < 2; ++i) {
      u32x4 pr, pi;
#pragma unroll
      for (int t = 0; t < 4; ++t) { pr[t] = pk2(ur[4 * t + i], ur[4 * t + 2 + i]); pi[t] = pk2(ui[4 * t + i], ui[4 * t + 2 + i]); }
      st16s(pr, i ? ho1 : ho0, sws, 0);
      st16s(pi, i ? ho1 : ho0, sws, 32768);
    }
    WAITV(0);
    st8c(tagv, hfo, sws);
  }
  ST(4);
  f32x16 acc[2];
  {
    const f32x16 z = {0, 0, 0, 0, 0, 0, 0, 0, 0, 0, 0, 0, 0, 0, 0, 0};
    acc[0] = mfma32(__builtin_bit_cast(bf16x8, xq0), __builtin_bit_cast(bf16x8, dg0), z);
    acc[0] = mfma32(__builtin_bit_cast(bf16x8, xq1), __builtin_bit_cast(bf16x8, dg1), acc[0]);
    asm volatile("" : "+v"(acc[0]));
  }
  if (HDLY) __builtin_amdgcn_s_sleep(HDLY);
  if (w == 0) poll<PSL>(hfp, true, tag, sws);
  BAR();
  ST(5);
  u32 rz0[4], rz1[4];
#pragma unroll
  for (u32 s = 0; s < 4; ++s) { rz0[s] = rc0[s] - 16384; rz1[s] = rc1[s] - 16384; }
  asm volatile("" : "+v"(rz0[0]), "+v"(rz0[1]), "+v"(rz0[2]), "+v"(rz0[3]), "+v"(rz1[0]), "+v"(rz1[1]), "+v"(rz1[2]), "+v"(rz1[3]));
  __builtin_amdgcn_s_setprio(BPRIO);
  bf16x8 hf[32];
  {
    auto hdma = [&](u32 u) { dma4c(stg + (u % 3) * 4096, l16, sws, WS_H + g * 131072 + th * 65536 + pt * 32768 + u * 4096); };
    auto hread = [&](u32 u) {
#pragma unroll
      for (u32 s = 0; s < 4; ++s) hf[u * 4 + s] = ldstr(tb0 + (u % 3) * 4096 + s * 1024, tb1 + (u % 3) * 4096 + s * 1024);
    };
    hdma(0); hdma(1); hdma(2);
    WAITV(8); hread(0); WAITVL(8); hdma(3);
    WAITV(8); hread(1); WAITVL(8); hdma(4);
    WAITV(8); hread(2); WAITVL(8); hdma(5);
    WAITV(8); hread(3); WAITVL(8); hdma(6);
    WAITV(8); hread(4); WAITVL(8); hdma(7);
    WAITV(8); hread(5);
    WAITV(4); hread(6);
    WAITV(0); hread(7);
  }
  waitvl_bar<0>();
  ST(6);
  {
    bf16x8 fc[8];
    auto cread = [&](u32 i) {
      const u32 o = ((i >> 3) % 5) * 16384, s = (i >> 1) & 3;
      fc[i & 7] = o ? ldsr(((i & 1) ? rc0[s] : rc1[s]) + (o - 16384)) : ldsr((i & 1) ? rz0[s] : rz1[s]);
    };
    auto crun = [&](u32 i0, u32 i1) {
      const f32x16 z = {0, 0, 0, 0, 0, 0, 0, 0, 0, 0, 0, 0, 0, 0, 0, 0};
#pragma unroll
      for (u32 i = i0; i < i1; ++i) {
        SCHED();
        if (i & 1) acc[0] = mfma32(hf[i >> 1], fc[i & 7], acc[0]); else acc[1] = mfma32(hf[i >> 1], fc[i & 7], i ? acc[1] : z);
        SCHED();
        if (i + 6 < 64) cread(i + 6);
      }
      SCHED();
    };
    cdma(2); cdma(3); cdma(4);
    waitvl_bar<0>();
#pragma unroll
    for (u32 i = 0; i < 6; ++i) cread(i);
    crun(0, 18);
    waitvl_bar<63>();
    cdma(5); cdma(6); cdma(7);
    if (w == 0) { __builtin_amdgcn_s_setprio(2); poll<TSL>(tfa, true, tag, sws); }
    if (g == 31 && j == 0 && w == 0) st8c(tag, fll + WS_EP, sws);
    __builtin_amdgcn_s_setprio(BPRIO);
    waitvl_bar<0>();
    ST(7);
    crun(18, 64);
  }
  __builtin_amdgcn_s_setprio(2);
  ST(8);
  waitvl_bar<63>();
#pragma unroll
  for (int i = 0; i < 4; ++i) *(LAS f32x4*)(size_t)(rxw + i * 1024) = f32x4{acc[1][4 * i], acc[1][4 * i + 1], acc[1][4 * i + 2], acc[1][4 * i + 3]};
  waitvl_bar<63>();
  {
    const __amdgpu_buffer_rsrc_t ro = __builtin_amdgcn_make_buffer_rsrc(a.out, 0, 8388608, 0x20000);
#pragma unroll
    for (int i = 0; i < 4; ++i) {
      const f32x4 p = *(LAS f32x4*)(size_t)(rxr + i * 1024);
#pragma unroll
      for (int e = 0; e < 4; ++e) { const float x = acc[0][4 * i + e] + p[e]; __builtin_amdgcn_raw_buffer_store_b32(__builtin_bit_cast(u32, x), ro, vout, (e + 8 * i) * 4096, AUXB); }
    }
  }
  ST(9);
  STFLUSH;
}

__global__ void __launch_bounds__(256, 2) fused_kernel(Args) {
  __shared__ __attribute__((aligned(16))) char lds[81920];
  u64 tf = 0;
  asm volatile("" ::"v"((u32)(size_t)(LAS char*)lds));
  const u32 lane = threadIdx.x & 63, w = __builtin_amdgcn_readfirstlane(threadIdx.x >> 6), b = blockIdx.x;
  if (__builtin_expect(b < 256, 1)) role_a(b, w, lane, tf); else {
    role_b(b - 256, w, lane);
  }
}

extern "C" void kernel_launch(void* const* d_in, const int* in_sizes, int n_in, void* d_out, int out_size, void* d_ws,
                              size_t ws_size, hipStream_t stream) {
  Args a;
  a.xa = (const u16*)d_in[0]; a.wq = (const u16*)d_in[1]; a.wk = (const u16*)d_in[2]; a.wv = (const u16*)d_in[3];
  a.wo = (const u16*)d_in[4]; a.xb = (const u16*)d_in[5]; a.lre = (const u16*)d_in[6]; a.lim = (const u16*)d_in[7];
  a.bre = (const u16*)d_in[8]; a.bim = (const u16*)d_in[9]; a.cre = (const u16*)d_in[10]; a.cim = (const u16*)d_in[11];
  a.ldt = (const u16*)d_in[12]; a.dsk = (const u16*)d_in[13];
  a.out = (float*)d_out; a.ws = (char*)d_ws;
  fused_kernel<<<512, 256, 0, stream>>>(a);
}
```

```cpp
#define TOUCH 1
#include <hip/hip_runtime.h>
#define QROT 0
#define BROT 0
#define BEARLY 0
#define BDELAY0 0
#define PSL 4
#define KFS 32
#define TBODY 7
#define TITER 7
#define PCNT 1
#define ADELAY 6
#define B1DS 8
#define B1DM 8
#define BSTART2 7500
#define PACEN 2
#define PACE __builtin_amdgcn_s_sleep(PACEN)
#define EDLY 0
#define HDLY 0
#define TSL 4
#define BSTART 4000
#define FPOS 5
#define BDELAY 0
#define TDELAY 0
#define AUXA 17
#define AUXB 17
typedef unsigned short u16;
typedef unsigned u32;
typedef unsigned long long u64;
typedef float f32x2 __attribute__((ext_vector_type(2)));
typedef float f32x4 __attribute__((ext_vector_type(4)));
typedef float f32x8 __attribute__((ext_vector_type(8)));
typedef float f32x16 __attribute__((ext_vector_type(16)));
typedef __bf16 bf16x2 __attribute__((ext_vector_type(2)));
typedef __bf16 bf16x8 __attribute__((ext_vector_type(8)));
typedef __bf16 bf16x16 __attribute__((ext_vector_type(16)));
typedef short s16x4 __attribute__((ext_vector_type(4)));
typedef u32 u32x2 __attribute__((ext_vector_type(2)));
typedef u32 u32x4 __attribute__((ext_vector_type(4)));
#define LAS __attribute__((address_space(3)))
#define DEVI __device__ __forceinline__

constexpr u32 FS = 128;
constexpr u32 WS_EP = 0;
constexpr u32 WS_KVP = 4096;
constexpr u32 WS_KVF = 32768;
constexpr u32 WS_RF = 65536;
constexpr u32 WS_TF = 98304;
constexpr u32 WS_EF = 131072;
constexpr u32 WS_HF = 262144;
constexpr u32 WS_KV = 393216;
constexpr u32 WS_E = 524288;
constexpr u32 WS_OF = 655360;
constexpr u32 WS_O = 1048576;
constexpr u32 WS_H = WS_O + 2097152;
constexpr u32 WS_END = WS_H + 4194304;
constexpr u32 WS_DBG = WS_END;
constexpr u32 OOB = 0x80000000u;

struct Args {
  const u16 *xa, *wq, *wk, *wv, *wo;
  float* out;
  char* ws;
  const u16 *xb, *lre, *lim, *bre, *bim, *cre, *cim, *ldt, *dsk;
};
typedef u64 u64x8 __attribute__((ext_vector_type(8)));

DEVI u32x4 mk_srd(const void* p, u32 bytes) {
  u64 a = (u64)p;
  u32x4 r = {(u32)a, (u32)(a >> 32) & 0xffffu, bytes, 0x20000u};
  return r;
}
DEVI void dma2(u32 m0v, u32 v0, u32 v1, u32x4 srd, u32 soff) {
  asm volatile("s_mov_b32 m0, %0\n\ts_nop 0\n\tbuffer_load_dwordx4 %1, %3, %4 offen lds\n\tbuffer_load_dwordx4 %2, %3, %4 offen offset:1024 lds"
               ::"s"(m0v), "v"(v0), "v"(v1), "s"(srd), "s"(soff) : "memory");
}
DEVI void dma2c(u32 m0v, u32 v0, u32x4 srd, u32 soff) {
  asm volatile("s_mov_b32 m0, %0\n\ts_nop 0\n\tbuffer_load_dwordx4 %1, %2, %3 offen sc1 lds\n\tbuffer_load_dwordx4 %1, %2, %3 offen offset:1024 sc1 lds"
               ::"s"(m0v), "v"(v0), "s"(srd), "s"(soff) : "memory");
}
DEVI void dma4(u32 m0v, u32 v0, u32x4 srd, u32 soff) {
  asm volatile("s_mov_b32 m0, %0\n\ts_nop 0\n\tbuffer_load_dwordx4 %1, %2, %3 offen lds\n\tbuffer_load_dwordx4 %1, %2, %3 offen offset:1024 lds\n\t"
               "buffer_load_dwordx4 %1, %2, %3 offen offset:2048 lds\n\tbuffer_load_dwordx4 %1, %2, %3 offen offset:3072 lds"
               ::"s"(m0v), "v"(v0), "s"(srd), "s"(soff) : "memory");
}
DEVI void dma4c(u32 m0v, u32 v0, u32x4 srd, u32 soff) {
  asm volatile("s_mov_b32 m0, %0\n\ts_nop 0\n\tbuffer_load_dwordx4 %1, %2, %3 offen sc1 lds\n\tbuffer_load_dwordx4 %1, %2, %3 offen offset:1024 sc1 lds\n\t"
               "buffer_load_dwordx4 %1, %2, %3 offen offset:2048 sc1 lds\n\tbuffer_load_dwordx4 %1, %2, %3 offen offset:3072 sc1 lds"
               ::"s"(m0v), "v"(v0), "s"(srd), "s"(soff) : "memory");
}
DEVI void dma1(u32 m0v, u32 v0, u32x4 srd, u32 soff) {
  asm volatile("s_mov_b32 m0, %0\n\ts_nop 0\n\tbuffer_load_dwordx4 %1, %2, %3 offen lds" ::"s"(m0v), "v"(v0), "s"(srd), "s"(soff) : "memory");
}
DEVI void dma4v(u32 m0v, u32 v0, u32 v1, u32 v2, u32 v3, u32x4 srd, u32 soff) {
  asm volatile("s_mov_b32 m0, %0\n\ts_nop 0\n\tbuffer_load_dwordx4 %1, %5, %6 offen lds\n\tbuffer_load_dwordx4 %2, %5, %6 offen offset:1024 lds\n\t"
               "buffer_load_dwordx4 %3, %5, %6 offen offset:2048 lds\n\tbuffer_load_dwordx4 %4, %5, %6 offen offset:3072 lds"
               ::"s"(m0v), "v"(v0), "v"(v1), "v"(v2), "v"(v3), "s"(srd), "s"(soff) : "memory");
}
template <int OFF> DEVI u64x8 args_request() {
  u64x8 k;
  asm volatile("s_load_dwordx16 %0, %1, %2" : "=&s"(k) : "s"(__builtin_amdgcn_kernarg_segment_ptr()), "i"(OFF));
  return k;
}
template <int OFF> DEVI void dma1c(u32 m0v, u32 v0, u32x4 srd, u32 soff) {
  asm volatile("s_mov_b32 m0, %0\n\ts_nop 0\n\tbuffer_load_dwordx4 %1, %2, %3 offen offset:%4 sc1 lds" ::"s"(m0v), "v"(v0), "s"(srd), "s"(soff), "i"(OFF) : "memory");
}
template <int OFF> DEVI void dma1o(u32 m0v, u32 v0, u32x4 srd, u32 soff) {
  asm volatile("s_mov_b32 m0, %0\n\ts_nop 0\n\tbuffer_load_dwordx4 %1, %2, %3 offen offset:%4 lds" ::"s"(m0v), "v"(v0), "s"(srd), "s"(soff), "i"(OFF) : "memory");
}
DEVI void dmaq(u32 m0v, u32 v0, u32 v1, u32x4 sx, u32 ox, u32 v2, u32 v3, u32x4 sw, u32 ow) {
  asm volatile("s_mov_b32 m0, %0\n\ts_nop 0\n\tbuffer_load_dwordx4 %5, %7, %8 offen offset:2048 lds\n\tbuffer_load_dwordx4 %6, %7, %8 offen offset:3072 lds\n\t"
               "buffer_load_dwordx4 %1, %3, %4 offen lds\n\tbuffer_load_dwordx4 %2, %3, %4 offen offset:1024 lds"
               ::"s"(m0v), "v"(v0), "v"(v1), "s"(sx), "s"(ox), "v"(v2), "v"(v3), "s"(sw), "s"(ow) : "memory");
}
DEVI void st8c(u32x2 v, u32 voff, u32x4 srd) {
  asm volatile("buffer_store_dwordx2 %0, %1, %2, 0 offen sc1" ::"v"(v), "v"(voff), "s"(srd) : "memory");
}
DEVI void st16c(u32x4 v, u32 voff, u32x4 srd) {
  asm volatile("buffer_store_dwordx4 %0, %1, %2, 0 offen sc1\n\ts_nop 1" ::"v"(v), "v"(voff), "s"(srd) : "memory");
}
DEVI void st16s(u32x4 v, u32 voff, u32x4 srd, u32 soff) {
  asm volatile("buffer_store_dwordx4 %0, %1, %2, %3 offen sc1\n\ts_nop 1" ::"v"(v), "v"(voff), "s"(srd), "s"(soff) : "memory");
}
#define WAITV(n) asm volatile("s_waitcnt vmcnt(" #n ")" ::: "memory")
#define WAITVL(n) asm volatile("s_waitcnt vmcnt(" #n ") lgkmcnt(0)" ::: "memory")
#define BAR() asm volatile("s_barrier" ::: "memory")
template <int N> DEVI void waitvl_bar() { asm volatile("s_waitcnt vmcnt(%0) lgkmcnt(0)\n\ts_barrier" ::"i"(N) : "memory"); }

template <int SLEEP> DEVI void poll(u32 voff, bool need, u32x2 tag, u32x4 srd) {
#pragma unroll 1
  for (u32 it = 0; it < (1u << 18); ++it) {
    u32x2 f;
    asm volatile("buffer_load_dwordx2 %0, %1, %2, 0 offen sc1\n\ts_waitcnt vmcnt(0)" : "=&v"(f) : "v"(voff), "s"(srd) : "memory");
    if (__builtin_amdgcn_ballot_w64(__builtin_bit_cast(u64, f) != __builtin_bit_cast(u64, tag)) == 0) break;
    if (SLEEP) __builtin_amdgcn_s_sleep(SLEEP);
  }
}
DEVI u32 pk2(float a, float b) {
  f32x2 x = {a, b};
  return __builtin_bit_cast(u32, __builtin_convertvector(x, bf16x2));
}
DEVI float bf2f(u32 v) { return __builtin_bit_cast(float, v << 16); }
DEVI bf16x8 ldsr(u32 a) { return *(const LAS bf16x8*)(size_t)a; }
DEVI bf16x8 ldstr(u32 a0, u32 a1) {
  s16x4 x = __builtin_amdgcn_ds_read_tr16_b64_v4i16((LAS s16x4*)(size_t)a0);
  s16x4 y = __builtin_amdgcn_ds_read_tr16_b64_v4i16((LAS s16x4*)(size_t)a1);
  return __builtin_bit_cast(bf16x8, __builtin_shufflevector(x, y, 0, 1, 2, 3, 4, 5, 6, 7));
}
#define SCHED() __builtin_amdgcn_sched_barrier(0)
DEVI f32x16 mfma32(bf16x8 a, bf16x8 b, f32x16 c) { return __builtin_amdgcn_mfma_f32_32x32x16_bf16(a, b, c, 0, 0, 0); }
DEVI f32x4 mfma16(bf16x8 a, bf16x8 b, f32x4 c) { return __builtin_amdgcn_mfma_f32_16x16x32_bf16(a, b, c, 0, 0, 0); }

#define SW 0
#define STDECL
#define STC(v)
#define ST(i)
#define STFLUSH

#define SK(i)
#define SB(i)
#define SQ(i)
#define SA(i) ST(i)
#define STR2(x) #x
#define STR(x) STR2(x)
#define EX(b, i) "v_exp_f32 v[" #b "+" #i "], v[" #b "+" #i "]\n\t"
#define SM(o, vv, b, i) "v_smfmac_f32_32x32x32_bf16 " o ", " vv ", v[" #b "+" #i ":" #b "+" #i "+7], v156\n\t"
#define OA "v[96:111]"
#define OB "v[112:127]"
#define QA "v[128:131]"
#define QB "v[132:135]"
#define KF "v[136:139]"
#define QK(b, k, q) "v_mfma_f32_32x32x16_bf16 v[" #b ":" #b "+15], " k ", " q ", 0\n\t"
#define WL "s_waitcnt lgkmcnt(0)\n\t"
#define IT(A, B, AN, BN, X0, X1, Y0, Y1, KO, VO, E0, E1, E2, E3, E4, E5)                              \
  EX(BN, 13) EX(BN, 14) EX(BN, 15) EX(A, 0) EX(A, 1) SM(OB, Y1, BN, 8) WL E0                          \
  "ds_read_b128 " Y0 ", %[va] offset:" #VO "+1024\n\tds_read_b128 " Y1 ", %[va] offset:" #VO "+1536\n\t" \
  EX(A, 2) EX(A, 3) EX(A, 4) EX(A, 5) EX(A, 6) QK(AN, KF, QA) E1                                      \
  EX(A, 7) EX(A, 8) EX(A, 9) EX(A, 10) EX(A, 11) EX(A, 12) SM(OA, X0, A, 0) E2                        \
  EX(A, 13) EX(A, 14) EX(A, 15) EX(B, 0) EX(B, 1) SM(OA, X1, A, 8) E3                                 \
  EX(B, 2) EX(B, 3) EX(B, 4) EX(B, 5) EX(B, 6) QK(BN, KF, QB) E4                                      \
  "ds_read_b128 " KF ", %[ka] offset:" #KO "\n\t"                                                   \
  EX(B, 7) EX(B, 8) EX(B, 9) EX(B, 10) EX(B, 11) EX(B, 12) SM(OB, X0, B, 0) E5
#define VA0 "v[140:143]"
#define VA1 "v[144:147]"
#define VB0 "v[148:151]"
#define VB1 "v[152:155]"
#define ITE(KO, VO, E0, E1, E2, E3, E4, E5) IT(64, 80, 160, 176, VA0, VA1, VB0, VB1, KO, VO, E0, E1, E2, E3, E4, E5)
#define ITO(KO, VO, E0, E1, E2, E3, E4, E5) IT(160, 176, 64, 80, VB0, VB1, VA0, VA1, KO, VO, E0, E1, E2, E3, E4, E5)
#define STEP "s_add_u32 %[so], %[so], s44\n\t"
#define NEXT "s_add_u32 %[so], %[so], s50\n\t"
#define MASK "s_and_b32 %[so], %[so], s45\n\t"
#define M0A "s_add_u32 m0, m0, s46\n\t"
#define M0M "s_and_b32 m0, m0, 0xffff\n\t"
#define BUMP "v_add_u16 %[ka], s46, %[ka]\n\tv_add_u16 %[va], %[vinc], %[va]\n\t"
#define TRG(i) TRG_(i, TITER)
#define TRG_(i, t) TRG__(i, t)
#define TRG__(i, t) ".if " #i " == " #t "\n\tbuffer_store_dwordx2 %[tag], %[tfo], %[sws], 0 offen sc1\n\t.endif\n\t"
#define LT0 ""
#define LT1 ""
#define LTO
#define ADMA(i, o) "buffer_load_dwordx4 %[dv], s[40:43], %[so] offen offset:" #o " sc1 lds\n\t"
#define ABAR "s_barrier\n\t"
#define BPRIO 0
#define PRHI "s_setprio 3\n\t"
#define PRLO "s_setprio 1\n\t"

DEVI void role_a(u32 b, u32 w, u32 lane, u64 tf) {
  u64x8 k0 = args_request<0>();
  const u32 xcd = b & 7, idx = b >> 3, g = xcd * 4 + (idx >> 3), j = idx & 7;
  const u32 r32 = lane & 31, h = lane >> 5, th = w & 1, hp = w >> 1;
  __builtin_amdgcn_s_setprio(2);
  STDECL;
  u32 l16 = lane * 16, xa_[4];
#pragma unroll
  for (u32 u = 0; u < 4; ++u) xa_[u] = (th * 2 + (r32 >> 4)) * 4096 + (r32 & 15) * 128 + (((2 * u + h) ^ ((r32 >> 1) & 7)) << 4);
  u32 vr0 = (lane >> 3) * 1024 + (((lane & 7) ^ (lane >> 4)) << 4);
  u32 vr1 = (lane >> 3) * 1024 + (((lane & 7) ^ (4 + (lane >> 4))) << 4) + 7168;
  u32 vt0 = (lane >> 3) * 1024 + (((lane & 7) ^ (((lane >> 4) & 1) << 2)) << 4) + j * 128;
  u32 vt1 = vt0 + 7168;
  u32 trb;
  {
    const u32 q4 = (lane & 15) >> 2, p4 = lane & 3, blk = (lane >> 4) & 1;
    trb = q4 * 128 + (((hp * 4 + blk * 2 + (p4 >> 1)) ^ ((q4 >> 1) << 2)) << 4) + (p4 & 1) * 8;
  }
  u32 ta_ = 2048 + trb + h * 1024, tw_ = trb + h * 512;
  f32x16 oa = {0, 0, 0, 0, 0, 0, 0, 0, 0, 0, 0, 0, 0, 0, 0, 0}, ob = oa;
  u32 vl = l16 + w * 4096, ka = l16, va, vinc = (~r32 & 16) << 10;
  {
    const u32 z = 192 - (((r32 - 20) >> 3) == 0 ? 64 : 0), o = (r32 & 4) << 4;
    const u32 c = 64512 + ((r32 & 27) == 16 ? o : z), v = (h * 16 + r32) * 16;
    va = r32 < 16 ? v : c;
  }
  u32 dv = vl;
  f32x8 sinit = {0.f, 0.f, 0.f, 0.f, 0.f, -__builtin_inff(), -__builtin_inff(), -__builtin_inff()};
  u32x4 vinit = {0, 0, 0, 0};
  u32 sidx = 0xddddddddu;
  u32 fll = lane == 0 ? 0 : OOB, fl0 = fll | (w == 0 ? 0 : OOB);
  u32 f0a = WS_KVF + ((xcd * 32 + 32 + lane) & 255) * FS, fwa = WS_KVP + (w * 64 + lane) * 8;
  u32 onea = 65536 + (w * 2 + h) * 2048 + ((lane >> 4) & 1) * 512 + (lane & 15) * 16, onev = (lane & 8) ? 0u : 0x3f803f80u;
  asm volatile("" : "+{v[96:111]}"(oa), "+{v[112:127]}"(ob), "+{v[184:191]}"(sinit), "+{v[152:155]}"(vinit), "+{v156}"(sidx), "+v"(ka), "+v"(va),
               "+v"(vinc), "+v"(dv), "+v"(fl0), "+v"(fll), "+v"(f0a), "+v"(fwa), "+v"(onea), "+v"(onev), "+v"(vl));
  u32 xb_[4], tb_ = ta_ + 65536;
#pragma unroll
  for (u32 u = 0; u < 4; ++u) xb_[u] = xa_[u] + 65536;
  asm volatile("" : "+v"(l16), "+v"(xa_[0]), "+v"(xa_[1]), "+v"(xa_[2]), "+v"(xa_[3]), "+v"(xb_[0]), "+v"(xb_[1]), "+v"(xb_[2]), "+v"(xb_[3]),
               "+v"(vr0), "+v"(vr1), "+v"(vt0), "+v"(vt1), "+v"(ta_), "+v"(tb_), "+v"(tw_));
  asm volatile("s_waitcnt lgkmcnt(0)" : "+s"(k0));
  Args a;
  a.xa = (const u16*)k0[0]; a.wq = (const u16*)k0[1]; a.wk = (const u16*)k0[2]; a.wv = (const u16*)k0[3]; a.wo = (const u16*)k0[4];
  a.out = (float*)k0[5]; a.ws = (char*)k0[6];
  char* const ws = a.ws;
  const u32x4 sws = mk_srd(ws, WS_END), sxa = mk_srd(a.xa, 2048 * 1024), swq = mk_srd(a.wq - 1024, 512 * 1024 + 2048);
  ST(0);
  if (ADELAY) __builtin_amdgcn_s_sleep(ADELAY);
  u32x2 ep;
  asm volatile("buffer_load_dwordx2 %0, off, %1, 0 sc1" : "=v"(ep) : "s"(sws) : "memory");
  const u32 rot = QROT == 0 ? 0 : QROT == 2 ? (g & 1) * 4 : (g & 3) * 2;
  constexpr u32 QSL[8] = {16384, 32768, 49152, 0, 65536, 16384, 32768, 49152};
  auto qd = [&](u32 c) {
    const u32 kc = (c + rot) & 7;
    dmaq(QSL[c] + w * 4096, vr0, vr1, sxa, (g * 64 + w * 16) * 1024 + kc * 128, vt0, vt1, swq, (kc * 64 + w * 16) * 1024);
  };
  qd(0); qd(1); qd(2); qd(3);
  asm volatile("s_waitcnt vmcnt(12)\n\ts_barrier" : "+v"(ep)::"memory");
  ST(1);
  u32x2 tag;
  {
    u64 e = (((u64)__builtin_amdgcn_readfirstlane(ep.y) << 32) | (u32)__builtin_amdgcn_readfirstlane(ep.x)) + 1;
    tag.x = (u32)e; tag.y = (u32)(e >> 32);
  }
  const u32x4 skv = mk_srd(ws + WS_KV, 131072), swo = mk_srd(a.wo, 512 * 1024);
  u32 so = ((xcd + 1) & 7) * 16384;
  u32x4 qa, qb;
  {
    f32x16 qacc = {0, 0, 0, 0, 0, 0, 0, 0, 0, 0, 0, 0, 0, 0, 0, 0};
    bf16x8 fx[2][4], fw[2][4];
    auto step = [&](u32 c) {
      const u32 n = c + 4, kc = (n + rot) & 7;
#pragma unroll
      for (u32 s = 0; s < 4; ++s) {
        SCHED();
        if (c > 0) qacc = mfma32(fw[(c - 1) & 1][s], fx[(c - 1) & 1][s], qacc);
        SCHED();
        if (c < 8) {
          const u32 ta = c == 4 ? tb_ + s * 4096 : ta_ + QSL[c & 7] + s * 4096;
          fw[c & 1][s] = ldstr(ta, ta + 512);
          fx[c & 1][s] = c == 4 ? ldsr(xb_[s]) : ldsr(xa_[s] + QSL[c & 7]);
        }
        SCHED();
        if (n < 8) {
          const u32 m = QSL[n & 7] + w * 4096, ox = (g * 64 + w * 16) * 1024 + kc * 128, ow = (kc * 64 + w * 16) * 1024;
          if (s == 0) dma1o<2048>(m, vt0, swq, ow);
          if (s == 1) dma1o<3072>(m, vt1, swq, ow);
          if (s == 2) dma1o<0>(m, vr0, sxa, ox);
          if (s == 3) dma1o<1024>(m, vr1, sxa, ox);
        }

      }
      SCHED();
    };
    step(0);
    waitvl_bar<12>(); SQ(2); step(1);
    waitvl_bar<12>(); SQ(3); step(2);
    waitvl_bar<12>(); SQ(4); step(3);
    __builtin_amdgcn_s_setprio(3);
    st8c(tag, fl0 + WS_RF + (g * 8 + j) * FS, sws);
    WAITV(13); BAR(); SQ(5); SA(2);
    u32x2 f0 = tag;
    auto fload = [&]() { if (w == 0) asm volatile("buffer_load_dwordx2 %0, %1, %2, 0 offen sc1" : "+v"(f0) : "v"(f0a), "s"(sws) : "memory"); };
    step(4);
    waitvl_bar<9>(); SQ(6);
    *(LAS u32x4*)(size_t)onea = u32x4{onev, onev, onev, onev};
    step(5);
    waitvl_bar<5>(); SQ(7);
    step(6);
    WAITV(1); BAR(); SQ(8); SA(3);
    fload();
    step(7);
    step(8);
    asm volatile("s_waitcnt vmcnt(0)" : "+v"(f0)::"memory");
    SQ(9);
    if (__builtin_amdgcn_ballot_w64(f0.x != tag.x || f0.y != tag.y) != 0) poll<0>(f0a, true, tag, sws);
    BAR(); SQ(10); SA(4);
#pragma unroll
    for (u32 u = 0; u < 4; ++u) dma1c<0>(u * 4096 + w * 1024, l16, skv, so + u * 4096 + w * 1024);
    so = (so + 16384) & 0x1ffff;
    const float qs = 0.25f * 1.44269504088896340736f;
#pragma unroll
    for (int i = 0; i < 4; ++i) { qa[i] = pk2(qacc[2 * i] * qs, qacc[2 * i + 1] * qs); qb[i] = pk2(qacc[8 + 2 * i] * qs, qacc[9 + 2 * i] * qs); }
    asm volatile("" : "+{v[128:131]}"(qa), "+{v[132:135]}"(qb));
  }
  const u32 wv0 = vt0;
  const u32 wso = (j * 65536 + w * 32768) & 0x7ffff;
  SA(5); SQ(11);
  {
    const u32 m0v = w * 4096 + 16384;
    const u32 tfo = fl0 + WS_TF + (g * 8 + j) * FS;
    const u64 tag64 = ((u64)tag.y << 32) | tag.x;
    asm volatile(
        "s_mov_b32 s40, %[k0]\n\ts_mov_b32 s41, %[k1]\n\ts_mov_b32 s42, 0x80000\n\ts_mov_b32 s43, 0x20000\n\t"
        "s_mov_b32 s44, 0\n\ts_mov_b32 s45, 0x1ffff\n\ts_mov_b32 s46, 0x4000\n\ts_mov_b32 s47, 1\n\ts_mov_b32 s50, 0x4000\n\t"
        "s_mov_b32 m0, %[m0v]\n\t"
        "s_branch 7f\n\t"
        "4:\n\t"
        "s_mov_b32 s59, 0x40000\n\t"
        "6:\n\t"
        "buffer_load_dwordx2 v[158:159], %[fwa], %[sws], 0 offen sc1\n\ts_sub_u32 s59, s59, 1\n\ts_cbranch_scc1 5f\n\t"
        "s_waitcnt vmcnt(0)\n\tv_cmp_ne_u64 vcc, %[tag64], v[158:159]\n\ts_nop 4\n\ts_cbranch_vccnz 6b\n\ts_branch 5f\n\t"
        "7:\n\t"
        "s_waitcnt vmcnt(3) lgkmcnt(0)\n\ts_barrier\n\t"
        "ds_read_b128 " VB0 ", %[ka]\n\tds_read_b128 " KF ", %[ka] offset:2048\n\t"
        "ds_read_b128 " VA0 ", %[va] offset:1024\n\tds_read_b128 " VA1 ", %[va] offset:1536\n\t"
        "s_waitcnt lgkmcnt(3)\n\t" QK(64, VB0, QA) QK(80, VB0, QB) "s_nop 7\n\t"
        ITE(4096, 2048, LT0, "", "", "s_waitcnt vmcnt(0)\n\t" ABAR, "", ADMA(0, 0))
        ITO(6144, 4096, "", LT1, ADMA(1, 1024), ADMA(2, 2048), "", ADMA(3, 3072))
        ITE(8192, 6144, NEXT, MASK, "buffer_load_dwordx2 v[158:159], %[fwa], %[sws], 0 offen sc1\n\t", M0A PRLO, "", "")
        ITO(10240, 8192, "", "", "", "", "", "")
        ITE(12288, 10240, "", "", "", "", "", "")
        ITO(14336, 12288, "", "", "", "", "", "")
        ITE(0, 14336, "", "", "s_waitcnt vmcnt(0)\n\tv_cmp_ne_u64 vcc, %[tag64], v[158:159]\n\t", "s_cbranch_vccnz 4b\n\t5:\n\t" WL ABAR BUMP, "", PRHI ADMA(0, 0))
        ITO(2048, 0, "", "", ADMA(1, 1024), ADMA(2, 2048), "", ADMA(3, 3072))
        PRLO NEXT MASK M0A
        "1:\n\t"
        ITE(4096, 2048, LT0, "", PRHI ADMA(0, 0), PRLO STEP, "", "")
        ITO(6144, 4096, "", LT1, PRHI ADMA(1, 1024), PRLO STEP, "", "")
        ITE(8192, 6144, "", "", PRHI ADMA(2, 2048), PRLO STEP, "", "")
        ITO(10240, 8192, "", "", PRHI ADMA(3, 3072), PRLO NEXT, MASK, M0A M0M)
        ITE(12288, 10240, "s_cmp_eq_u32 s47, 5\n\t", "s_cselect_b32 s40, %[w0], s40\n\t", "s_cselect_b32 s41, %[w1], s41\n\ts_cselect_b32 %[so], %[wso], %[so]\n\t",
            "s_cselect_b32 s44, 7168, s44\n\ts_cselect_b32 s50, 109568, s50\n\t", "s_cselect_b32 s45, 0x7ffff, s45\n\t", "s_cselect_b64 vcc, -1, 0\n\t")
        ITO(14336, 12288, "", "", "v_cndmask_b32 %[dv], %[dv], %[wv0], vcc\n\t", "", "", "")
        ITE(0, 14336, "", "", "", "s_waitcnt vmcnt(4) lgkmcnt(0)\n\t" ABAR BUMP, "", "")
        ITO(2048, 0, "", "", "s_add_u32 s47, s47, 1\n\ts_cmp_lg_u32 s47, 7\n\t", "", "", "")
        "s_cbranch_scc1 1b\n\t"
        ITE(4096, 2048, LT0, TRG(0), PRHI ADMA(0, 0), PRLO STEP, "", PRHI ADMA(1, 1024))
        ITO(6144, 4096, PRLO STEP, LT1 TRG(1), PRHI ADMA(2, 2048), PRLO STEP, "", PRHI ADMA(3, 3072))
        ITE(8192, 6144, PRLO NEXT, MASK TRG(2), M0A, "", "", PRHI ADMA(0, 0))
        ITO(10240, 8192, PRLO STEP, TRG(3), PRHI ADMA(1, 1024), PRLO STEP, "", PRHI ADMA(2, 2048))
        ITE(12288, 10240, PRLO STEP, TRG(4), PRHI ADMA(3, 3072), PRLO NEXT, MASK, M0A)
        ITO(14336, 12288, "", TRG(5), "", "", "", "")
        ITE(0, 14336, "", TRG(6), "", WL ABAR, "", PRHI ADMA(0, 0))
        ITO(2048, 0, STEP, TRG(7), ADMA(1, 1024), STEP ADMA(2, 2048), STEP, ADMA(3, 3072))
        EX(176, 13) EX(176, 14) EX(176, 15)
        "s_setprio 3\n\ts_waitcnt lgkmcnt(0)\n\t"
        SM(OB, VB1, 176, 8)
        : "+{v[96:111]}"(oa), "+{v[112:127]}"(ob), "+{v[184:191]}"(sinit), "+{v[152:155]}"(vinit), [ka] "+v"(ka), [va] "+v"(va), [so] "+s"(so),
          [dv] "+v"(dv) LTO
        : "{v[128:131]}"(qa), "{v[132:135]}"(qb), "{v156}"(sidx), [vinc] "v"(vinc), [wv0] "v"(wv0), [wso] "s"(wso),
          [m0v] "s"(m0v), [tag] "v"(tag), [tag64] "s"(tag64), [tfo] "v"(tfo), [fwa] "v"(fwa), [sws] "s"(sws),
          [k0] "s"(skv.x), [k1] "s"(skv.y), [w0] "s"(swo.x), [w1] "s"(swo.y)
        : "memory", "scc", "vcc", "s40", "s41", "s42", "s43", "s44", "s45", "s46", "s47", "s48", "s49", "s50", "s59",
          "v64", "v65", "v66", "v67", "v68", "v69", "v70", "v71", "v72", "v73", "v74", "v75", "v76", "v77", "v78", "v79", "v80", "v81", "v82", "v83", "v84", "v85", "v86", "v87", "v88", "v89", "v90", "v91", "v92", "v93", "v94", "v95", "v136", "v137", "v138", "v139", "v140", "v141", "v142", "v143", "v144", "v145", "v146", "v147", "v148", "v149", "v150", "v151", "v158", "v159", "v160", "v161", "v162", "v163", "v164", "v165", "v166", "v167", "v168", "v169", "v170", "v171", "v172", "v173", "v174", "v175", "v176", "v177", "v178", "v179", "v180", "v181", "v182", "v183");
  }
  const u32 ooff = WS_O + g * 65536 + ((j * 4 + hp * 2) * 2 + th) * 1024 + l16;
  const u32 olds = 65536 + (hp * 4 + th) * 1024 + l16;
  auto opub = [&](const f32x16& o, u32 t) {
    const float inv = __builtin_amdgcn_rcpf(o[8]);
    u32x4 p;
#pragma unroll
    for (int i = 0; i < 4; ++i) p[i] = pk2(o[2 * i] * inv, o[2 * i + 1] * inv);
    st16c(p, ooff + t * 2048, sws);
    *(LAS u32x4*)(size_t)(olds + t * 2048) = p;
  };
  opub(oa, 0);
  asm volatile("s_nop 7" : "+{v[112:127]}"(ob));
  opub(ob, 1);
  SA(6);
  {
    float ma = 0.f;
#pragma unroll
    for (int i = 0; i < 8; ++i) { ma = __builtin_fmaf(oa[i], 0.f, ma); ma = __builtin_fmaf(ob[i], 0.f, ma); }
    const bool ok = oa[8] >= 0x1p-64f && oa[8] <= 0x1p100f && ob[8] >= 0x1p-64f && ob[8] <= 0x1p100f && ma == 0.f;
    if (__builtin_expect(__builtin_amdgcn_ballot_w64(!ok) != 0, 0)) {
      WAITV(0);
      const __amdgpu_buffer_rsrc_t rk = __builtin_amdgcn_make_buffer_rsrc(ws + WS_KV, 0, 131072, 0x20000);
      const u32x4 ones = {0x3f803f80u, 0x3f803f80u, 0x3f803f80u, 0x3f803f80u};
#pragma unroll 1
      for (int t = 0; t < 2; ++t) {
        const bf16x8 q = __builtin_bit_cast(bf16x8, t ? qb : qa);
        f32x16 z = {0, 0, 0, 0, 0, 0, 0, 0, 0, 0, 0, 0, 0, 0, 0, 0};
        float m = -__builtin_inff();
#pragma unroll 1
        for (u32 kt = 0; kt < 64; ++kt) {
          u32x4 kf = __builtin_amdgcn_raw_buffer_load_b128(rk, lane * 16, kt * 2048, 16);
          f32x16 s = mfma32(__builtin_bit_cast(bf16x8, kf), q, z);
#pragma unroll
          for (int i = 0; i < 16; ++i) m = __builtin_fmaxf(m, s[i]);
        }
        m = __builtin_fmaxf(m, __shfl_xor(m, 32));
        f32x16 o = z;
#pragma unroll 1
        for (u32 kt = 0; kt < 64; ++kt) {
          u32x4 kf = __builtin_amdgcn_raw_buffer_load_b128(rk, lane * 16, kt * 2048, 16);
          u32x4 v0 = __builtin_amdgcn_raw_buffer_load_b128(rk, (h * 16 + (r32 & 15)) * 16 + 1024, kt * 2048, 16);
          u32x4 v1 = __builtin_amdgcn_raw_buffer_load_b128(rk, (h * 16 + (r32 & 15)) * 16 + 1536, kt * 2048, 16);
          if (r32 >= 16) { v0 = ones; v1 = ones; }
          f32x16 s = mfma32(__builtin_bit_cast(bf16x8, kf), q, z);
          f32x8 p0, p1;
#pragma unroll
          for (int i = 0; i < 8; ++i) { p0[i] = __builtin_amdgcn_exp2f(s[i] - m); p1[i] = __builtin_amdgcn_exp2f(s[8 + i] - m); }
          o = __builtin_amdgcn_smfmac_f32_32x32x32_bf16(__builtin_bit_cast(bf16x8, v0), __builtin_bit_cast(bf16x16, p0), o, (int)0xdddddddd, 0, 0);
          o = __builtin_amdgcn_smfmac_f32_32x32x32_bf16(__builtin_bit_cast(bf16x8, v1), __builtin_bit_cast(bf16x16, p1), o, (int)0xdddddddd, 0, 0);
        }
        opub(o, t);
      }
    }
  }
  f32x16 acc = {0, 0, 0, 0, 0, 0, 0, 0, 0, 0, 0, 0, 0, 0, 0, 0};
  {
    bf16x8 wf[32], fo[2][4];
    const u32 ob_ = th * 1024 + l16;
    auto wread = [&](u32 i0, u32 i1) {
#pragma unroll
      for (u32 i = i0; i < i1; ++i) wf[i] = ldstr(tw_ + i * 2048, tw_ + i * 2048 + 1024);
      SCHED();
    };
    auto odma = [&](u32 c) { dma2c((c - 1) * 8192 + w * 2048, l16, sws, WS_O + g * 65536 + ((j + c) & 7) * 8192 + w * 2048); };
    auto oread = [&](u32 c) {
#pragma unroll
      for (u32 i = 0; i < 4; ++i) fo[c & 1][i] = ldsr(ob_ + (c ? (c - 1) * 8192 : 65536) + i * 2048);
      SCHED();
    };
    auto omul = [&](u32 c) {
#pragma unroll
      for (u32 i = 0; i < 4; ++i) acc = mfma32(fo[c & 1][i], wf[c * 4 + i], acc);
      SCHED();
    };
    waitvl_bar<6>();
    oread(0); wread(0, 12);
    WAITV(0);
    st8c(tag, fll + WS_OF + (g * 32 + j * 4 + w) * FS, sws);
    SA(7);
    wread(12, 24);
    BAR();
    wread(24, 32);
    if (w == 0) poll<0>(WS_OF + (g * 32 + r32) * FS, true, tag, sws);
    waitvl_bar<0>();
    SA(8);
    auto ostep = [&](u32 c) {
#pragma unroll
      for (u32 i = 0; i < 4; ++i) {
        SCHED();
        acc = mfma32(fo[(c - 1) & 1][i], wf[(c - 1) * 4 + i], acc);
        SCHED();
        fo[c & 1][i] = ldsr(ob_ + (c - 1) * 8192 + i * 2048);
      }
      SCHED();
    };
    odma(1); odma(2); odma(3); odma(4); odma(5);
    WAITV(8); BAR(); ostep(1); odma(6);
    WAITV(8); BAR(); ostep(2); odma(7);
    WAITV(8); BAR(); ostep(3);
    WAITV(6); BAR(); ostep(4);
    WAITV(4); BAR(); ostep(5);
    WAITV(2); BAR(); ostep(6);
    WAITV(0); BAR(); oread(7); omul(6); omul(7);
  }
  SA(9);
  float* op = a.out + (size_t)(g * 64 + th * 32 + h * 4) * 1024 + j * 64 + hp * 32 + r32;
  {
    const __amdgpu_buffer_rsrc_t ro = __builtin_amdgcn_make_buffer_rsrc(a.out, 0, 8388608, 0x20000);
    const u32 vo = (u32)((char*)op - (char*)a.out);
#pragma unroll
    for (int i = 0; i < 16; ++i) { const float x = acc[i]; __builtin_amdgcn_raw_buffer_store_b32(__builtin_bit_cast(u32, x), ro, vo, ((i & 3) + 8 * (i >> 2)) * 4096, AUXA); }
  }
  SA(10);
  STFLUSH;
}

DEVI void cmul(float& r, float& i, float ar, float ai, float br, float bi) { r = ar * br - ai * bi; i = ar * bi + ai * br; }
DEVI void cstep(float& r, float& i, float ar, float ai, float ur, float ui) {
  float nr = __builtin_fmaf(r, ar, __builtin_fmaf(-i, ai, ur)), ni = __builtin_fmaf(i, ar, __builtin_fmaf(r, ai, ui));
  r = nr; i = ni;
}

DEVI f32x2 fma2(f32x2 a, f32x2 b, f32x2 c) { return __builtin_elementwise_fma(a, b, c); }
DEVI void cstep2(f32x2& r, f32x2& i, float ar, float ai, f32x2 ur, f32x2 ui) {
  const f32x2 a = {ar, ar}, b = {ai, ai};
  const f32x2 nr = fma2(r, a, fma2(-i, b, ur)), ni = fma2(i, a, fma2(r, b, ui));
  r = nr; i = ni;
}

DEVI void role_b(u32 b, u32 w, u32 lane, u64 tf) {
  u64x8 k0 = args_request<0>(), k1 = args_request<64>();
  const u32 xcd = b & 7, idx = b >> 3, g = xcd * 4 + (idx >> 3), j = idx & 7;
  const u32 c16 = lane & 15, g4 = lane >> 4, r32 = lane & 31, h = lane >> 5, th = w & 1, pt = w >> 1;
  __builtin_amdgcn_s_setprio(3);
  STDECL;
  u32 vr0 = (lane >> 3) * 1024 + (((lane & 7) ^ (lane >> 4)) << 4);
  u32 vr1 = (lane >> 3) * 1024 + (((lane & 7) ^ (4 + (lane >> 4))) << 4) + 7168;
  const u32 xrow = (32 * h + 8 * ((lane >> 3) & 1) + ((lane >> 4) & 1)) * 1024;
  u32 vx0 = xrow + (((lane & 7) ^ (lane >> 4)) << 4);
  u32 vx1 = xrow + (((lane & 7) ^ (4 + (lane >> 4))) << 4) + 1024;
  u32 ra[4], rb[4];
#pragma unroll
  for (u32 s = 0; s < 4; ++s) { ra[s] = r32 * 128 + (((2 * s + h) ^ ((r32 >> 1) & 7)) << 4); rb[s] = ra[s] + 8192 + w * 4096; }
  u32 vo = (j * 64 + w * 16 + c16) * 2, vd = (j * 64 + pt * 32 + r32) * 2, l16 = lane * 16, fll = lane == 0 ? 0 : OOB;
  asm volatile("" : "+v"(vr0), "+v"(vr1), "+v"(vx0), "+v"(vx1), "+v"(ra[0]), "+v"(ra[1]), "+v"(ra[2]), "+v"(ra[3]),
               "+v"(rb[0]), "+v"(rb[1]), "+v"(rb[2]), "+v"(rb[3]), "+v"(vo), "+v"(vd), "+v"(l16), "+v"(fll));
  u32 xs_[4], xk_[4], red = 65536 + l16;
#pragma unroll
  for (u32 u = 0; u < 4; ++u) {
    xs_[u] = (4 * u + g4) * 1024 + ((c16 ^ (4 * u + g4)) << 4) - u * 1024;
    xk_[u] = w * 4096 + c16 * 256 + (((4 * u + g4) ^ c16) << 4);
  }
  u32 kvrd = 65536 + w * 4096 + ((lane >> 4) * 8 + ((lane & 15) >> 2)) * 32 + (lane & 3) * 8;
  u32 kvoff;
  {
    const u32 tq = j >> 1;
    const u32 ov = 1024 + (tq & 1) * 512 + ((g4 >> 1) * 16 + c16) * 16 + (g4 & 1) * 8;
    const u32 ok = ((g4 & 1) * 32 + (tq & 1) * 16 + c16) * 16 + (g4 >> 1) * 8;
    const u32 mv = 0u - (j & 1);
    kvoff = (WS_KV + (g * 2 + (tq >> 1)) * 2048 + ((ov & mv) | (ok & ~mv)));
  }
  u32 kvfo = lane == 0 ? WS_KVF + (g * 8 + j) * FS : WS_KVP + (g * 8 + j) * 8;
  kvfo = lane < 2 ? kvfo : OOB;
  asm volatile("" : "+v"(kvfo), "+v"(xs_[0]), "+v"(xs_[1]), "+v"(xs_[2]), "+v"(xs_[3]), "+v"(xk_[0]), "+v"(xk_[1]), "+v"(xk_[2]), "+v"(xk_[3]), "+v"(red), "+v"(kvrd), "+v"(kvoff));
  asm volatile("s_waitcnt lgkmcnt(0)" : "+s"(k0), "+s"(k1));
  Args a;
  a.out = (float*)k0[5]; a.ws = (char*)k0[6]; a.xb = (const u16*)k0[7];
  a.lre = (const u16*)k1[0]; a.lim = (const u16*)k1[1]; a.bre = (const u16*)k1[2]; a.bim = (const u16*)k1[3];
  a.cre = (const u16*)k1[4]; a.cim = (const u16*)k1[5]; a.ldt = (const u16*)k1[6]; a.dsk = (const u16*)k1[7];
  char* const ws = a.ws;
  const u32x4 sws = mk_srd(ws, WS_END), sxb = mk_srd(a.xb, 2048 * 1024);
  ST(0);
  const u64 tb = __builtin_amdgcn_s_memtime();
  dma4v(w * 4096, xs_[0], xs_[1], xs_[2], xs_[3], mk_srd((const void*)k0[0], 2048 * 1024), (g * 64 + (j >> 1) * 16) * 1024 + w * 256);
  dma4(65536 + w * 4096, l16, mk_srd((const void*)((j & 1) ? k0[3] : k0[2]), 512 * 32), w * 4096);
  u32x2 ep;
  u32 plr, pli, pdt, pdk;
  {
    const u32x4 s0 = mk_srd(a.lre, 1024), s1 = mk_srd(a.lim, 1024), s2 = mk_srd(a.ldt, 1024), s3 = mk_srd(a.dsk, 1024);
    asm volatile("buffer_load_dwordx2 %0, off, %5, 0 sc1\n\tbuffer_load_ushort %1, %10, %6, 0 offen\n\t"
                 "buffer_load_ushort %2, %10, %7, 0 offen\n\tbuffer_load_ushort %3, %10, %8, 0 offen\n\tbuffer_load_ushort %4, %11, %9, 0 offen"
                 : "=&v"(ep), "=&v"(plr), "=&v"(pli), "=&v"(pdt), "=&v"(pdk) : "s"(sws), "s"(s0), "s"(s1), "s"(s2), "s"(s3), "v"(vo), "v"(vd) : "memory");
  }
  f32x16 ur = {0, 0, 0, 0, 0, 0, 0, 0, 0, 0, 0, 0, 0, 0, 0, 0}, ui = ur;
  float ar, ai, gr, gi, a8r, a8i, a16r, a16i, a64r, a64i, akr, aki;
  u32x4 dg0, dg1;
  const u32 stg = 32768 + w * 12288;
  u32 rc0[4], rc1[4], tb0, tb1, rxw, rxr, vout, xw, xr_, eoff, efo, efp, hfo, hfp, rfa, tfa, vq, ho0, ho1, evo[8];
  u32x2 tag, tagv;
  {
    const u32x4 sbr = mk_srd(a.bre, 512 * 1024), sbi = mk_srd(a.bim, 512 * 1024);
    const u32 rot = BROT == 0 ? 0 : (j + (g & 3) * 2) & 7;
    auto bdx = [&](u32 c) { dma2((c % 3) * 24576 + w * 2048, vx0, vx1, sxb, (g * 64 + pt * 16 + th * 4) * 1024 + ((c + rot) & 7) * 128); };
    auto bdr = [&](u32 c) { dma2((c % 3) * 24576 + 8192 + w * 4096, vr0, vr1, sbr, (j * 64 + w * 16) * 1024 + ((c + rot) & 7) * 128); };
    auto bdi = [&](u32 c) { dma2((c % 3) * 24576 + 10240 + w * 4096, vr0, vr1, sbi, (j * 64 + w * 16) * 1024 + ((c + rot) & 7) * 128); };
    WAITV(5);
    SK(1);
    {
      bf16x8 wf[4], xf[4];
#pragma unroll
      for (int i = 0; i < 4; ++i) { xf[i] = ldsr(xk_[i]); wf[i] = ldstr(kvrd + i * 1024, kvrd + i * 1024 + 128); }
      f32x4 acc = {0, 0, 0, 0};
      if (j & 1) {
#pragma unroll
        for (int i = 0; i < 4; ++i) acc = mfma16(xf[i], wf[i], acc);
      } else {
#pragma unroll
        for (int i = 0; i < 4; ++i) acc = mfma16(wf[i], xf[i], acc);
      }
      *(LAS f32x4*)(size_t)(red + w * 4096) = acc;
      waitvl_bar<63>();
      SK(2);
      if (w == 0) {
        f32x4 s0 = *(LAS f32x4*)(size_t)(red), s1 = *(LAS f32x4*)(size_t)(red + 4096), s2 = *(LAS f32x4*)(size_t)(red + 8192), s3 = *(LAS f32x4*)(size_t)(red + 12288);
        s0 = (s0 + s1) + (s2 + s3);
        st8c(u32x2{pk2(s0[0], s0[1]), pk2(s0[2], s0[3])}, kvoff, sws);
      }
    }
    SK(3);
    asm volatile("s_waitcnt vmcnt(0)" : "+v"(ep), "+v"(plr), "+v"(pli), "+v"(pdt), "+v"(pdk)::"memory");
    {
      u64 e = (((u64)__builtin_amdgcn_readfirstlane(ep.y) << 32) | (u32)__builtin_amdgcn_readfirstlane(ep.x)) + 1;
      tag.x = (u32)e; tag.y = (u32)(e >> 32);
    }
    tagv = tag;
    if (w == 0) st8c(tag, kvfo, sws);
    __builtin_amdgcn_s_setprio(2);
    SB(1);
    {
      const float lr = bf2f(plr), li = bf2f(pli);
      const float dt = bf2f(pk2(expf(bf2f(pdt)), 0.f) & 0xffffu);
      const float e = expf(dt * lr);
      float sn, cs;
      sincosf(dt * li, &sn, &cs);
      ar = e * cs; ai = e * sn;
      const float nr = ar - 1.f, ni = ai;
      const bool sw = __builtin_fabsf(lr) >= __builtin_fabsf(li);
      const float big = sw ? lr : li, sm = sw ? li : lr, rt = sm / big, den = big + sm * rt;
      const float x = sw ? nr : ni, y = sw ? ni : nr;
      gr = 2.f * (x + y * rt) / den;
      gi = 2.f * (y - x * rt) / den;
      gi = sw ? gi : -gi;
      ai = -ai;
      a8r = ar; a8i = ai;
#pragma unroll
      for (int i = 0; i < 3; ++i) cmul(a8r, a8i, a8r, a8i, a8r, a8i);
      cmul(a16r, a16i, a8r, a8i, a8r, a8i);
      cmul(a64r, a64i, a16r, a16i, a16r, a16i);
      cmul(a64r, a64i, a64r, a64i, a64r, a64i);
      akr = a64r; aki = a64i;
#pragma unroll
      for (int i = 0; i < 3; ++i) cmul(akr, aki, akr, aki, akr, aki);
      const u32 dv = pdk << ((r32 & 1) * 16), di = (r32 >> 1) - 4 * h;
#pragma unroll
      for (u32 i = 0; i < 4; ++i) { dg0[i] = di == i ? dv : 0u; dg1[i] = di == 8 + i ? dv : 0u; }
      asm volatile("" : "+v"(ar), "+v"(ai), "+v"(gr), "+v"(gi), "+v"(a8r), "+v"(a8i), "+v"(a16r), "+v"(a16i), "+v"(a64r), "+v"(a64i),
                   "+v"(akr), "+v"(aki), "+v"(dg0), "+v"(dg1));
      SCHED();
    }
    {
      const u32 q4 = c16 >> 2, p4 = lane & 3, m = (g4 & 1) * 2 + (p4 >> 1);
      tb0 = stg + m * 256 + (((8 * h + q4) ^ (m << 2)) << 4) + (p4 & 1) * 8;
      tb1 = tb0 ^ 64;
#pragma unroll
      for (u32 s = 0; s < 4; ++s) { rc0[s] = ra[s] + pt * 12288 + 16384; rc1[s] = rc0[s] ^ 4096; }
      rxw = 49152 + w * 4096 + l16; rxr = rxw ^ 8192;
      vout = (g * 64 + th * 32 + h * 4) * 4096 + 2048 + (j * 64 + pt * 32 + r32) * 4;
      xw = stg + lane * 8; xr_ = stg + c16 * 8;
      eoff = g4 == 0 ? WS_E + ((j * 4 + w) * 32 + g) * 128 + c16 * 8 : OOB;
      efo = fll + WS_EF + ((j * 4 + w) * 32 + g) * FS;
      efp = WS_EF + ((j * 4 + w) * 32 + (r32 < g ? r32 : g)) * FS;
      hfo = fll + WS_HF + (g * 32 + j * 4 + w) * FS;
      hfp = WS_HF + (g * 32 + r32) * FS;
      rfa = WS_RF + (g * 8 + j) * FS; tfa = WS_TF + (g * 8 + j) * FS;
      vq = (g * 64 + th * 32 + r32) * 1024 + j * 128 + pt * 64 + h * 16;
      const u32 off = WS_H + g * 131072 + h * 65536 + (j * 4 + w) * 1024, m0_ = (g4 & 1) * 2;
      ho0 = off + m0_ * 256 + ((c16 ^ (m0_ << 2)) << 4);
      ho1 = off + (m0_ + 1) * 256 + ((c16 ^ ((m0_ + 1) << 2)) << 4);
      const int gb = (int)g - 8 - 8 * (int)g4;
#pragma unroll
      for (int i = 0; i < 8; ++i) evo[i] = gb + i >= 0 ? (u32)(gb + i) * 128 + c16 * 8 : OOB;
      asm volatile("" : "+v"(rc0[0]), "+v"(rc0[1]), "+v"(rc0[2]), "+v"(rc0[3]), "+v"(rc1[0]), "+v"(rc1[1]), "+v"(rc1[2]), "+v"(rc1[3]),
                   "+v"(tb0), "+v"(tb1), "+v"(rxw), "+v"(rxr), "+v"(vout), "+v"(xw), "+v"(xr_), "+v"(eoff), "+v"(efo), "+v"(efp), "+v"(hfo), "+v"(hfp));
      asm volatile("" : "+v"(rfa), "+v"(tfa), "+v"(vq), "+v"(ho0), "+v"(ho1), "+v"(evo[0]), "+v"(evo[1]), "+v"(evo[2]), "+v"(evo[3]),
                   "+v"(evo[4]), "+v"(evo[5]), "+v"(evo[6]), "+v"(evo[7]), "+v"(tagv));
    }
    SB(2);
    while (__builtin_amdgcn_s_memtime() - tb < BSTART) __builtin_amdgcn_s_sleep(2);
    __builtin_amdgcn_s_setprio(BPRIO);
    BAR();
    bdx(0); bdr(0); bdi(0); bdx(1); bdr(1); bdi(1);
    bf16x8 fx[2][8], fb[2][4];
    auto step = [&](u32 c, bool rd, bool mul) {
      const u32 sl = (c % 3) * 24576, n = c + 2, p = (c - 1) & 1;
#pragma unroll
      for (u32 s = 0; s < 4; ++s) {
        SCHED();
        if (mul) ur = mfma32(fx[p][2 * s], fb[p][s], ur);
        SCHED();
        if (rd) { fb[c & 1][s] = ldsr(rb[s] + sl); fx[c & 1][2 * s] = ldsr(ra[s] + sl); }
        SCHED();
        if (mul) ui = mfma32(fx[p][2 * s + 1], fb[p][s], ui);
        SCHED();
        if (rd) fx[c & 1][2 * s + 1] = ldsr(ra[s] + sl + 4096);
        SCHED();
        if (rd && c > 1 && n < 8 && s == 0) bdx(n);
        if (rd && c > 1 && n < 8 && s == 1) bdr(n);
        if (rd && c > 1 && n < 8 && s == 2) bdi(n);
      }
      SCHED();
    };
    SB(3);
    waitvl_bar<6>(); SB(4); step(0, true, false);
    waitvl_bar<0>(); SB(5); step(1, true, true);
    step(2, false, true);
    while (__builtin_amdgcn_s_memtime() - tb < BSTART2) __builtin_amdgcn_s_sleep(2);
    if (TOUCH) { SCHED(); __builtin_amdgcn_s_setprio(3); SCHED(); }
    if (TOUCH & 1) {
      const u32 t = w == 0 ? WS_OF + (g * 32 + r32) * FS + h * (WS_HF - WS_OF) : w == 1 ? (h ? tfa : WS_E + ((j * 4 + (lane & 3)) * 32 + g) * 128) : WS_EF + ((j * 4 + (lane & 3)) * 32 + g) * FS;
      asm volatile("buffer_load_dword v255, %0, %1, 0 offen" ::"v"(t), "s"(sws) : "memory", "v255");
    }
    if (TOUCH & 2) {
      const u32 t = WS_H + g * 131072 + (lane >> 4) * 32768 + (j * 4 + w) * 1024 + (lane & 7) * 128;
      const u32 u = WS_O + g * 65536 + j * 8192 + w * 2048 + (h * 8 + (lane & 7)) * 128;
      asm volatile("buffer_load_dword v255, %0, %1, 0 offen" ::"v"((lane & 8) ? u : t), "s"(sws) : "memory", "v255");
    }
    if (TOUCH & 4) {
      const u32 t = (g * 64 + w * 16 + c16) * 4096 + (g4 >> 1) * 2048 + j * 256 + (g4 & 1) * 128;
      asm volatile("buffer_load_dword v255, %0, %1, 0 offen" ::"v"(t), "s"(mk_srd(a.out, 8388608)) : "memory", "v255");
    }
    if (TOUCH) { SCHED(); __builtin_amdgcn_s_setprio(BPRIO); SCHED(); }
    bdx(2); PACE; bdr(2); PACE; bdi(2); PACE; bdx(3); PACE; bdr(3); PACE; bdi(3);
    waitvl_bar<6>(); SB(6); step(2, true, false);
    waitvl_bar<6>(); SB(7); step(3, true, true);
    waitvl_bar<6>(); SB(8); step(4, true, true);
    waitvl_bar<6>(); SB(9); step(5, true, true);
    waitvl_bar<6>(); SB(10); step(6, true, true);
    waitvl_bar<0>(); step(7, true, true);
    waitvl_bar<0>(); step(8, false, true);
  }
  __builtin_amdgcn_s_setprio(2);
  ST(1);
  f32x2 er = {0.f, 0.f}, ei = {0.f, 0.f};
  {
#pragma unroll
    for (int i = 0; i < 16; ++i) {
      const float p = ur[i], q = ui[i];
      const auto r = __builtin_amdgcn_permlane16_swap(__builtin_bit_cast(u32, p), __builtin_bit_cast(u32, q), false, false);
      const u32 r0 = r[0], r1 = r[1];
      ur[i] = __builtin_bit_cast(float, r0); ui[i] = __builtin_bit_cast(float, r1);
    }
    const f32x2 g_r = {gr, gr}, g_i = {gi, gi};
#pragma unroll
    for (int m = 0; m < 8; ++m) {
      const f32x2 xr = {ur[2 * m], ur[2 * m + 1]}, xi = {ui[2 * m], ui[2 * m + 1]};
      const f32x2 yr = fma2(g_r, xr, -(g_i * xi)), yi = fma2(-g_r, xi, -(g_i * xr));
      ur[2 * m] = yr.x; ur[2 * m + 1] = yr.y; ui[2 * m] = yi.x; ui[2 * m + 1] = yi.y;
      if (m == 0) { er = yr; ei = yi; } else cstep2(er, ei, ar, ai, yr, yi);
    }
  }
  f32x2 es[4];
  {
    float tr = er.x, ti = ei.x;
    cstep(tr, ti, a8r, a8i, er.y, ei.y);
    *(volatile LAS f32x2*)(size_t)xw = f32x2{tr, ti};
#pragma unroll
    for (int s = 0; s < 4; ++s) es[s] = *(volatile LAS f32x2*)(size_t)(xr_ + s * 128);
    tr = es[0].x; ti = es[0].y;
#pragma unroll
    for (int s = 1; s < 4; ++s) cstep(tr, ti, a16r, a16i, es[s].x, es[s].y);
    u32x2 ev = {__builtin_bit_cast(u32, tr), __builtin_bit_cast(u32, ti)};
    st8c(ev, eoff, sws);
  }
  const u32x4 scr = mk_srd(a.cre, 512 * 1024), sci = mk_srd(a.cim, 512 * 1024);
  auto cdma = [&](u32 c) {
    const u32 sl = (c % 5) * 16384 + w * 2048, so = (j * 64 + w * 16) * 1024 + c * 128;
    dma2(sl, vr0, vr1, scr, so);
    dma2(sl + 8192, vr0, vr1, sci, so);
  };
  u32x4 xq0, xq1;
  u32x2 rf;
  asm volatile("buffer_load_dwordx2 %0, %1, %2, 0 offen sc1" : "=v"(rf) : "v"(rfa), "s"(sws) : "memory");
  cdma(0); cdma(1);
  {
    asm volatile("buffer_load_dwordx4 %0, %2, %3, 0 offen\n\tbuffer_load_dwordx4 %1, %2, %3, 0 offen offset:32" : "=&v"(xq0), "=&v"(xq1) : "v"(vq), "s"(sxb) : "memory");
  }
  asm volatile("s_waitcnt vmcnt(10)" : "+v"(rf)::"memory");
  if (__builtin_amdgcn_ballot_w64(rf.x != tag.x || rf.y != tag.y) != 0) poll<4>(rfa, true, tag, sws);
  st8c(tagv, efo, sws);
  ST(2);
  asm volatile("s_waitcnt vmcnt(1)" : "+v"(xq0), "+v"(xq1)::"memory");
  if (EDLY) __builtin_amdgcn_s_sleep(EDLY);
  poll<PSL>(efp, true, tag, sws);
  ST(3);
  {
    const __amdgpu_buffer_rsrc_t re = __builtin_amdgcn_make_buffer_rsrc(ws + WS_E + (j * 4 + w) * 4096, 0, 4096, 0x20000);
    u32x2 ev[8];
#pragma unroll
    for (int m = 0; m < 8; ++m) ev[m] = __builtin_amdgcn_raw_buffer_load_b64(re, evo[m], 0, 16);
    float cr, ci;
    { const f32x2 e2 = __builtin_bit_cast(f32x2, ev[0]); cr = e2.x; ci = e2.y; }
#pragma unroll
    for (int m = 1; m < 8; ++m) { const f32x2 e2 = __builtin_bit_cast(f32x2, ev[m]); cstep(cr, ci, a64r, a64i, e2.x, e2.y); }
    *(volatile LAS f32x2*)(size_t)xw = f32x2{cr, ci};
    f32x2 ps[4];
#pragma unroll
    for (int s = 0; s < 4; ++s) ps[s] = *(volatile LAS f32x2*)(size_t)(xr_ + s * 128);
    cr = ps[3].x; ci = ps[3].y;
#pragma unroll
    for (int k = 2; k >= 0; --k) {
      if (8u * (k + 1) < g) cstep(cr, ci, akr, aki, ps[k].x, ps[k].y); else { cr = ps[k].x; ci = ps[k].y; }
    }
#pragma unroll
    for (u32 s = 0; s < 3; ++s) {
      float tr = cr, ti = ci;
      cstep(tr, ti, a16r, a16i, es[s].x, es[s].y);
      cr = s < g4 ? tr : cr; ci = s < g4 ? ti : ci;
    }
    float br = cr, bi = ci;
    cstep(br, bi, a8r, a8i, er.x, ei.x);
    f32x2 hr = {cr, br}, hi = {ci, bi};
#pragma unroll
    for (int m = 0; m < 8; ++m) {
      cstep2(hr, hi, ar, ai, f32x2{ur[2 * m], ur[2 * m + 1]}, f32x2{ui[2 * m], ui[2 * m + 1]});
      ur[2 * m] = hr.x; ur[2 * m + 1] = hr.y; ui[2 * m] = hi.x; ui[2 * m + 1] = hi.y;
    }
#pragma unroll
    for (u32 i = 0; i < 2; ++i) {
      u32x4 pr, pi;
#pragma unroll
      for (int t = 0; t < 4; ++t) { pr[t] = pk2(ur[4 * t + i], ur[4 * t + 2 + i]); pi[t] = pk2(ui[4 * t + i], ui[4 * t + 2 + i]); }
      st16s(pr, i ? ho1 : ho0, sws, 0);
      st16s(pi, i ? ho1 : ho0, sws, 32768);
    }
    WAITV(0);
    st8c(tagv, hfo, sws);
  }
  ST(4);
  f32x16 acc[2];
  {
    const f32x16 z = {0, 0, 0, 0, 0, 0, 0, 0, 0, 0, 0, 0, 0, 0, 0, 0};
    acc[0] = mfma32(__builtin_bit_cast(bf16x8, xq0), __builtin_bit_cast(bf16x8, dg0), z);
    acc[0] = mfma32(__builtin_bit_cast(bf16x8, xq1), __builtin_bit_cast(bf16x8, dg1), acc[0]);
    asm volatile("" : "+v"(acc[0]));
  }
  if (HDLY) __builtin_amdgcn_s_sleep(HDLY);
  if (w == 0) poll<PSL>(hfp, true, tag, sws);
  BAR();
  ST(5);
  u32 rz0[4], rz1[4];
#pragma unroll
  for (u32 s = 0; s < 4; ++s) { rz0[s] = rc0[s] - 16384; rz1[s] = rc1[s] - 16384; }
  asm volatile("" : "+v"(rz0[0]), "+v"(rz0[1]), "+v"(rz0[2]), "+v"(rz0[3]), "+v"(rz1[0]), "+v"(rz1[1]), "+v"(rz1[2]), "+v"(rz1[3]));
  __builtin_amdgcn_s_setprio(BPRIO);
  bf16x8 hf[32];
  {
    auto hdma = [&](u32 u) { dma4c(stg + (u % 3) * 4096, l16, sws, WS_H + g * 131072 + th * 65536 + pt * 32768 + u * 4096); };
    auto hread = [&](u32 u) {
#pragma unroll
      for (u32 s = 0; s < 4; ++s) hf[u * 4 + s] = ldstr(tb0 + (u % 3) * 4096 + s * 1024, tb1 + (u % 3) * 4096 + s * 1024);
    };
    hdma(0); hdma(1); hdma(2);
    WAITV(8); hread(0); WAITVL(8); hdma(3);
    WAITV(8); hread(1); WAITVL(8); hdma(4);
    WAITV(8); hread(2); WAITVL(8); hdma(5);
    WAITV(8); hread(3); WAITVL(8); hdma(6);
    WAITV(8); hread(4); WAITVL(8); hdma(7);
    WAITV(8); hread(5);
    WAITV(4); hread(6);
    WAITV(0); hread(7);
  }
  waitvl_bar<0>();
  ST(6);
  {
    bf16x8 fc[8];
    auto cread = [&](u32 i) {
      const u32 o = ((i >> 3) % 5) * 16384, s = (i >> 1) & 3;
      fc[i & 7] = o ? ldsr(((i & 1) ? rc0[s] : rc1[s]) + (o - 16384)) : ldsr((i & 1) ? rz0[s] : rz1[s]);
    };
    auto crun = [&](u32 i0, u32 i1) {
      const f32x16 z = {0, 0, 0, 0, 0, 0, 0, 0, 0, 0, 0, 0, 0, 0, 0, 0};
#pragma unroll
      for (u32 i = i0; i < i1; ++i) {
        SCHED();
        if (i & 1) acc[0] = mfma32(hf[i >> 1], fc[i & 7], acc[0]); else acc[1] = mfma32(hf[i >> 1], fc[i & 7], i ? acc[1] : z);
        SCHED();
        if (i + 6 < 64) cread(i + 6);
      }
      SCHED();
    };
    cdma(2); cdma(3); cdma(4);
    waitvl_bar<0>();
#pragma unroll
    for (u32 i = 0; i < 6; ++i) cread(i);
    crun(0, 18);
    waitvl_bar<63>();
    cdma(5); cdma(6); cdma(7);
    if (w == 0) { __builtin_amdgcn_s_setprio(2); poll<TSL>(tfa, true, tag, sws); }
    if (g == 31 && j == 0 && w == 0) st8c(tag, fll + WS_EP, sws);
    __builtin_amdgcn_s_setprio(BPRIO);
    waitvl_bar<0>();
    ST(7);
    crun(18, 64);
  }
  __builtin_amdgcn_s_setprio(2);
  ST(8);
  waitvl_bar<63>();
#pragma unroll
  for (int i = 0; i < 4; ++i) *(LAS f32x4*)(size_t)(rxw + i * 1024) = f32x4{acc[1][4 * i], acc[1][4 * i + 1], acc[1][4 * i + 2], acc[1][4 * i + 3]};
  waitvl_bar<63>();
  {
    const __amdgpu_buffer_rsrc_t ro = __builtin_amdgcn_make_buffer_rsrc(a.out, 0, 8388608, 0x20000);
#pragma unroll
    for (int i = 0; i < 4; ++i) {
      const f32x4 p = *(LAS f32x4*)(size_t)(rxr + i * 1024);
#pragma unroll
      for (int e = 0; e < 4; ++e) { const float x = acc[0][4 * i + e] + p[e]; __builtin_amdgcn_raw_buffer_store_b32(__builtin_bit_cast(u32, x), ro, vout, (e + 8 * i) * 4096, AUXB); }
    }
  }
  ST(9);
  STFLUSH;
}

__global__ void __launch_bounds__(256, 2) fused_kernel(Args) {
  __shared__ __attribute__((aligned(16))) char lds[81920];
  u64 tf = 0;
  asm volatile("" ::"v"((u32)(size_t)(LAS char*)lds));
  const u32 lane = threadIdx.x & 63, w = __builtin_amdgcn_readfirstlane(threadIdx.x >> 6), b = blockIdx.x;
  if (__builtin_expect(b < 256, 1)) role_a(b, w, lane, tf); else {
    role_b(b - 256, w, lane, tf);
  }
}

extern "C" void kernel_launch(void* const* d_in, const int* in_sizes, int n_in, void* d_out, int out_size, void* d_ws,
                              size_t ws_size, hipStream_t stream) {
  Args a;
  a.xa = (const u16*)d_in[0]; a.wq = (const u16*)d_in[1]; a.wk = (const u16*)d_in[2]; a.wv = (const u16*)d_in[3];
  a.wo = (const u16*)d_in[4]; a.xb = (const u16*)d_in[5]; a.lre = (const u16*)d_in[6]; a.lim = (const u16*)d_in[7];
  a.bre = (const u16*)d_in[8]; a.bim = (const u16*)d_in[9]; a.cre = (const u16*)d_in[10]; a.cim = (const u16*)d_in[11];
  a.ldt = (const u16*)d_in[12]; a.dsk = (const u16*)d_in[13];
  a.out = (float*)d_out; a.ws = (char*)d_ws;
  fused_kernel<<<512, 256, 0, stream>>>(a);
}
```

```cpp
#include <hip/hip_runtime.h>
#define QROT 0
#define BROT 0
#define BEARLY 0
#define BDELAY0 0
#define PSL 4
#define KFS 32
#define TBODY 7
#define TITER 7
#define PCNT 1
#define ADELAY 6
#define B1DS 8
#define B1DM 8
#define BSTART2 7500
#define PACEN 2
#define PACE __builtin_amdgcn_s_sleep(PACEN)
#define TOUCH 1
#define NSH 18
#define HPACE 2
#define FL 16
#define EDLY 16
#define HDLY 16
#define TSL 4
#define BSTART 4000
#define FPOS 5
#define BDELAY 0
#define TDELAY 0
#define AUXA 17
#define AUXB 17
typedef unsigned short u16;
typedef unsigned u32;
typedef unsigned long long u64;
typedef float f32x2 __attribute__((ext_vector_type(2)));
typedef float f32x4 __attribute__((ext_vector_type(4)));
typedef float f32x8 __attribute__((ext_vector_type(8)));
typedef float f32x16 __attribute__((ext_vector_type(16)));
typedef __bf16 bf16x2 __attribute__((ext_vector_type(2)));
typedef __bf16 bf16x8 __attribute__((ext_vector_type(8)));
typedef __bf16 bf16x16 __attribute__((ext_vector_type(16)));
typedef short s16x4 __attribute__((ext_vector_type(4)));
typedef u32 u32x2 __attribute__((ext_vector_type(2)));
typedef u32 u32x4 __attribute__((ext_vector_type(4)));
#define LAS __attribute__((address_space(3)))
#define DEVI __device__ __forceinline__

constexpr u32 FS = 128;
constexpr u32 WS_EP = 0;
constexpr u32 WS_KVP = 4096;
constexpr u32 WS_KVF = 32768;
constexpr u32 WS_RF = 65536;
constexpr u32 WS_TF = 98304;
constexpr u32 WS_EF = 131072;
constexpr u32 WS_HF = 262144;
constexpr u32 WS_KV = 393216;
constexpr u32 WS_E = 524288;
constexpr u32 WS_OF = 655360;
constexpr u32 WS_O = 1048576;
constexpr u32 WS_H = WS_O + 2097152;
constexpr u32 WS_END = WS_H + 4194304;
constexpr u32 WS_DBG = WS_END;
constexpr u32 OOB = 0x80000000u;

struct Args {
  const u16 *xa, *wq, *wk, *wv, *wo;
  float* out;
  char* ws;
  const u16 *xb, *lre, *lim, *bre, *bim, *cre, *cim, *ldt, *dsk;
};
typedef u64 u64x8 __attribute__((ext_vector_type(8)));

DEVI u32x4 mk_srd(const void* p, u32 bytes) {
  u64 a = (u64)p;
  u32x4 r = {(u32)a, (u32)(a >> 32) & 0xffffu, bytes, 0x20000u};
  return r;
}
DEVI void dma2(u32 m0v, u32 v0, u32 v1, u32x4 srd, u32 soff) {
  asm volatile("s_mov_b32 m0, %0\n\ts_nop 0\n\tbuffer_load_dwordx4 %1, %3, %4 offen lds\n\tbuffer_load_dwordx4 %2, %3, %4 offen offset:1024 lds"
               ::"s"(m0v), "v"(v0), "v"(v1), "s"(srd), "s"(soff) : "memory");
}
DEVI void dma2c(u32 m0v, u32 v0, u32x4 srd, u32 soff) {
  asm volatile("s_mov_b32 m0, %0\n\ts_nop 0\n\tbuffer_load_dwordx4 %1, %2, %3 offen sc1 lds\n\tbuffer_load_dwordx4 %1, %2, %3 offen offset:1024 sc1 lds"
               ::"s"(m0v), "v"(v0), "s"(srd), "s"(soff) : "memory");
}
DEVI void dma4(u32 m0v, u32 v0, u32x4 srd, u32 soff) {
  asm volatile("s_mov_b32 m0, %0\n\ts_nop 0\n\tbuffer_load_dwordx4 %1, %2, %3 offen lds\n\tbuffer_load_dwordx4 %1, %2, %3 offen offset:1024 lds\n\t"
               "buffer_load_dwordx4 %1, %2, %3 offen offset:2048 lds\n\tbuffer_load_dwordx4 %1, %2, %3 offen offset:3072 lds"
               ::"s"(m0v), "v"(v0), "s"(srd), "s"(soff) : "memory");
}
DEVI void dma4c(u32 m0v, u32 v0, u32x4 srd, u32 soff) {
  asm volatile("s_mov_b32 m0, %0\n\ts_nop 0\n\tbuffer_load_dwordx4 %1, %2, %3 offen sc1 lds\n\tbuffer_load_dwordx4 %1, %2, %3 offen offset:1024 sc1 lds\n\t"
               "buffer_load_dwordx4 %1, %2, %3 offen offset:2048 sc1 lds\n\tbuffer_load_dwordx4 %1, %2, %3 offen offset:3072 sc1 lds"
               ::"s"(m0v), "v"(v0), "s"(srd), "s"(soff) : "memory");
}
DEVI void dma1(u32 m0v, u32 v0, u32x4 srd, u32 soff) {
  asm volatile("s_mov_b32 m0, %0\n\ts_nop 0\n\tbuffer_load_dwordx4 %1, %2, %3 offen lds" ::"s"(m0v), "v"(v0), "s"(srd), "s"(soff) : "memory");
}
DEVI void dma4v(u32 m0v, u32 v0, u32 v1, u32 v2, u32 v3, u32x4 srd, u32 soff) {
  asm volatile("s_mov_b32 m0, %0\n\ts_nop 0\n\tbuffer_load_dwordx4 %1, %5, %6 offen lds\n\tbuffer_load_dwordx4 %2, %5, %6 offen offset:1024 lds\n\t"
               "buffer_load_dwordx4 %3, %5, %6 offen offset:2048 lds\n\tbuffer_load_dwordx4 %4, %5, %6 offen offset:3072 lds"
               ::"s"(m0v), "v"(v0), "v"(v1), "v"(v2), "v"(v3), "s"(srd), "s"(soff) : "memory");
}
template <int OFF> DEVI u64x8 args_request() {
  u64x8 k;
  asm volatile("s_load_dwordx16 %0, %1, %2" : "=&s"(k) : "s"(__builtin_amdgcn_kernarg_segment_ptr()), "i"(OFF));
  return k;
}
template <int OFF> DEVI void dma1c(u32 m0v, u32 v0, u32x4 srd, u32 soff) {
  asm volatile("s_mov_b32 m0, %0\n\ts_nop 0\n\tbuffer_load_dwordx4 %1, %2, %3 offen offset:%4 sc1 lds" ::"s"(m0v), "v"(v0), "s"(srd), "s"(soff), "i"(OFF) : "memory");
}
template <int OFF> DEVI void dma1o(u32 m0v, u32 v0, u32x4 srd, u32 soff) {
  asm volatile("s_mov_b32 m0, %0\n\ts_nop 0\n\tbuffer_load_dwordx4 %1, %2, %3 offen offset:%4 lds" ::"s"(m0v), "v"(v0), "s"(srd), "s"(soff), "i"(OFF) : "memory");
}
DEVI void dmaq(u32 m0v, u32 v0, u32 v1, u32x4 sx, u32 ox, u32 v2, u32 v3, u32x4 sw, u32 ow) {
  asm volatile("s_mov_b32 m0, %0\n\ts_nop 0\n\tbuffer_load_dwordx4 %5, %7, %8 offen offset:2048 lds\n\tbuffer_load_dwordx4 %6, %7, %8 offen offset:3072 lds\n\t"
               "buffer_load_dwordx4 %1, %3, %4 offen lds\n\tbuffer_load_dwordx4 %2, %3, %4 offen offset:1024 lds"
               ::"s"(m0v), "v"(v0), "v"(v1), "s"(sx), "s"(ox), "v"(v2), "v"(v3), "s"(sw), "s"(ow) : "memory");
}
DEVI u64 sload64(const char* p, u32 off) {
  u64 v;
  asm volatile("s_load_dwordx2 %0, %1, %2" : "=&s"(v) : "s"(p), "s"(off) : "memory");
  return v;
}
#define KWAIT()
DEVI u32x2 mk_tag(u64 ep) {
  const u64 t = ep + 1 + (ep == ~0ull);
  return u32x2{(u32)t, (u32)(t >> 32)};
}
DEVI void st8c(u32x2 v, u32 voff, u32x4 srd) {
  asm volatile("buffer_store_dwordx2 %0, %1, %2, 0 offen sc1" ::"v"(v), "v"(voff), "s"(srd) : "memory");
}
DEVI void st16c(u32x4 v, u32 voff, u32x4 srd) {
  asm volatile("buffer_store_dwordx4 %0, %1, %2, 0 offen sc1\n\ts_nop 1" ::"v"(v), "v"(voff), "s"(srd) : "memory");
}
DEVI void st16s(u32x4 v, u32 voff, u32x4 srd, u32 soff) {
  asm volatile("buffer_store_dwordx4 %0, %1, %2, %3 offen sc1\n\ts_nop 1" ::"v"(v), "v"(voff), "s"(srd), "s"(soff) : "memory");
}
#define WAITV(n) asm volatile("s_waitcnt vmcnt(" #n ")" ::: "memory")
#define WAITVL(n) asm volatile("s_waitcnt vmcnt(" #n ") lgkmcnt(0)" ::: "memory")
#define BAR() asm volatile("s_barrier" ::: "memory")
template <int N> DEVI void waitvl_bar() { asm volatile("s_waitcnt vmcnt(%0) lgkmcnt(0)\n\ts_barrier" ::"i"(N) : "memory"); }

template <int SLEEP> DEVI void poll(u32 voff, bool need, u32x2 tag, u32x4 srd) {
#pragma unroll 1
  for (u32 it = 0; it < (1u << 18); ++it) {
    u32x2 f;
    asm volatile("buffer_load_dwordx2 %0, %1, %2, 0 offen sc1\n\ts_waitcnt vmcnt(0)" : "=&v"(f) : "v"(voff), "s"(srd) : "memory");
    if (__builtin_amdgcn_ballot_w64(__builtin_bit_cast(u64, f) != __builtin_bit_cast(u64, tag)) == 0) break;
    if (SLEEP) __builtin_amdgcn_s_sleep(SLEEP);
  }
}
DEVI u32 pk2(float a, float b) {
  f32x2 x = {a, b};
  return __builtin_bit_cast(u32, __builtin_convertvector(x, bf16x2));
}
DEVI float smul(float a, float b) { float r; asm("v_mul_f32 %0, %1, %2" : "=v"(r) : "v"(a), "v"(b)); return r; }
DEVI float bf2f(u32 v) { return __builtin_bit_cast(float, v << 16); }
DEVI bf16x8 ldsr(u32 a) { return *(const LAS bf16x8*)(size_t)a; }
DEVI bf16x8 ldstr(u32 a0, u32 a1) {
  s16x4 x = __builtin_amdgcn_ds_read_tr16_b64_v4i16((LAS s16x4*)(size_t)a0);
  s16x4 y = __builtin_amdgcn_ds_read_tr16_b64_v4i16((LAS s16x4*)(size_t)a1);
  return __builtin_bit_cast(bf16x8, __builtin_shufflevector(x, y, 0, 1, 2, 3, 4, 5, 6, 7));
}
#define SCHED() __builtin_amdgcn_sched_barrier(0)
DEVI f32x16 mfma32(bf16x8 a, bf16x8 b, f32x16 c) { return __builtin_amdgcn_mfma_f32_32x32x16_bf16(a, b, c, 0, 0, 0); }
DEVI f32x4 mfma16(bf16x8 a, bf16x8 b, f32x4 c) { return __builtin_amdgcn_mfma_f32_16x16x32_bf16(a, b, c, 0, 0, 0); }

#define SW 0
#define STDECL
#define STC(v)
#define ST(i)
#define STFLUSH

#define SK(i)
#define SB(i)
#define SQ(i)
#define SA(i) ST(i)
#define STR2(x) #x
#define STR(x) STR2(x)
#define EX(b, i) "v_exp_f32 v[" #b "+" #i "], v[" #b "+" #i "]\n\t"
#define SM(o, vv, b, i) "v_smfmac_f32_32x32x32_bf16 " o ", " vv ", v[" #b "+" #i ":" #b "+" #i "+7], v156\n\t"
#define OA "v[96:111]"
#define OB "v[112:127]"
#define QA "v[128:131]"
#define QB "v[132:135]"
#define KF "v[136:139]"
#define QK(b, k, q) "v_mfma_f32_32x32x16_bf16 v[" #b ":" #b "+15], " k ", " q ", 0\n\t"
#define WL "s_waitcnt lgkmcnt(0)\n\t"
#define IT(A, B, AN, BN, X0, X1, Y0, Y1, KO, VO, E0, E1, E2, E3, E4, E5)                              \
  EX(BN, 13) EX(BN, 14) EX(BN, 15) EX(A, 0) EX(A, 1) SM(OB, Y1, BN, 8) WL E0                          \
  "ds_read_b128 " Y0 ", %[va] offset:" #VO "+1024\n\tds_read_b128 " Y1 ", %[va] offset:" #VO "+1536\n\t" \
  EX(A, 2) EX(A, 3) EX(A, 4) EX(A, 5) EX(A, 6) QK(AN, KF, QA) E1                                      \
  EX(A, 7) EX(A, 8) EX(A, 9) EX(A, 10) EX(A, 11) EX(A, 12) SM(OA, X0, A, 0) E2                        \
  EX(A, 13) EX(A, 14) EX(A, 15) EX(B, 0) EX(B, 1) SM(OA, X1, A, 8) E3                                 \
  EX(B, 2) EX(B, 3) EX(B, 4) EX(B, 5) EX(B, 6) QK(BN, KF, QB) E4                                      \
  "ds_read_b128 " KF ", %[ka] offset:" #KO "\n\t"                                                   \
  EX(B, 7) EX(B, 8) EX(B, 9) EX(B, 10) EX(B, 11) EX(B, 12) SM(OB, X0, B, 0) E5
#define VA0 "v[140:143]"
#define VA1 "v[144:147]"
#define VB0 "v[148:151]"
#define VB1 "v[152:155]"
#define ITE(KO, VO, E0, E1, E2, E3, E4, E5) IT(64, 80, 160, 176, VA0, VA1, VB0, VB1, KO, VO, E0, E1, E2, E3, E4, E5)
#define ITO(KO, VO, E0, E1, E2, E3, E4, E5) IT(160, 176, 64, 80, VB0, VB1, VA0, VA1, KO, VO, E0, E1, E2, E3, E4, E5)
#define STEP "s_add_u32 %[so], %[so], s44\n\t"
#define NEXT "s_add_u32 %[so], %[so], s50\n\t"
#define MASK "s_and_b32 %[so], %[so], s45\n\t"
#define M0A "s_add_u32 m0, m0, s46\n\t"
#define M0M "s_and_b32 m0, m0, 0xffff\n\t"
#define BUMP "v_add_u16 %[ka], s46, %[ka]\n\tv_add_u16 %[va], %[vinc], %[va]\n\t"
#define TRG(i) TRG_(i, TITER)
#define TRG_(i, t) TRG__(i, t)
#define TRG__(i, t) ".if " #i " == " #t "\n\tbuffer_store_dwordx2 %[tag], %[tfo], %[sws], 0 offen sc1\n\t.endif\n\t"
#define LT0 ""
#define LT1 ""
#define LTO
#define ADMA(i, o) "buffer_load_dwordx4 %[dv], s[40:43], %[so] offen offset:" #o " sc1 lds\n\t"
#define ABAR "s_barrier\n\t"
#define BPRIO 0
#define PRHI "s_setprio 3\n\t"
#define PRLO "s_setprio 1\n\t"

DEVI void role_a(u32 b, u32 w, u32 lane, u64 tf) {
  u64x8 k0 = args_request<0>();
  const u32 xcd = b & 7, idx = b >> 3, g = xcd * 4 + (idx >> 3), j = idx & 7;
  const u32 r32 = lane & 31, h = lane >> 5, th = w & 1, hp = w >> 1;
  __builtin_amdgcn_s_setprio(2);
  STDECL;
  u32 l16 = lane * 16, xa_[4];
#pragma unroll
  for (u32 u = 0; u < 4; ++u) xa_[u] = (th * 2 + (r32 >> 4)) * 4096 + (r32 & 15) * 128 + (((2 * u + h) ^ ((r32 >> 1) & 7)) << 4);
  u32 vr0 = (lane >> 3) * 1024 + (((lane & 7) ^ (lane >> 4)) << 4);
  u32 vr1 = (lane >> 3) * 1024 + (((lane & 7) ^ (4 + (lane >> 4))) << 4) + 7168;
  u32 vt0 = (lane >> 3) * 1024 + (((lane & 7) ^ (((lane >> 4) & 1) << 2)) << 4) + j * 128;
  u32 vt1 = vt0 + 7168;
  u32 trb;
  {
    const u32 q4 = (lane & 15) >> 2, p4 = lane & 3, blk = (lane >> 4) & 1;
    trb = q4 * 128 + (((hp * 4 + blk * 2 + (p4 >> 1)) ^ ((q4 >> 1) << 2)) << 4) + (p4 & 1) * 8;
  }
  u32 ta_ = 2048 + trb + h * 1024, tw_ = trb + h * 512;
  f32x16 oa = {0, 0, 0, 0, 0, 0, 0, 0, 0, 0, 0, 0, 0, 0, 0, 0}, ob = oa;
  u32 vl = l16 + w * 4096, ka = l16, va, vinc = (~r32 & 16) << 10;
  {
    const u32 z = 192 - (((r32 - 20) >> 3) == 0 ? 64 : 0), o = (r32 & 4) << 4;
    const u32 c = 64512 + ((r32 & 27) == 16 ? o : z), v = (h * 16 + r32) * 16;
    va = r32 < 16 ? v : c;
  }
  u32 dv = vl;
  f32x8 sinit = {0.f, 0.f, 0.f, 0.f, 0.f, -__builtin_inff(), -__builtin_inff(), -__builtin_inff()};
  u32x4 vinit = {0, 0, 0, 0};
  u32 sidx = 0xddddddddu;
  u32 fll = lane < FL ? lane * 8 : OOB, fl0 = fll | (w == 0 ? 0 : OOB);
  u32 pre = lane < 16 ? WS_OF + (g * 32 + j * 4 + w) * FS : lane < 32 ? WS_TF + (g * 8 + j) * FS : WS_RF + (g * 8 + j) * FS;
  pre = (pre + (lane & 15) * 8) | ((lane < 16 || (w == 0 && lane < 48)) ? 0 : OOB) | ((TOUCH || lane >= 32) ? 0 : OOB);
  asm volatile("" : "+v"(pre));
  u32 f0a = WS_KVF + ((xcd * 32 + 32 + lane) & 255) * FS, fwa = WS_KVP + (w * 64 + lane) * 8;
  u32 onea = 65536 + (w * 2 + h) * 2048 + ((lane >> 4) & 1) * 512 + (lane & 15) * 16, onev = (lane & 8) ? 0u : 0x3f803f80u;
  asm volatile("" : "+{v[96:111]}"(oa), "+{v[112:127]}"(ob), "+{v[184:191]}"(sinit), "+{v[152:155]}"(vinit), "+{v156}"(sidx), "+v"(ka), "+v"(va),
               "+v"(vinc), "+v"(dv), "+v"(fl0), "+v"(fll), "+v"(f0a), "+v"(fwa), "+v"(onea), "+v"(onev), "+v"(vl));
  u32 xb_[4], tb_ = ta_ + 65536;
#pragma unroll
  for (u32 u = 0; u < 4; ++u) xb_[u] = xa_[u] + 65536;
  asm volatile("" : "+v"(l16), "+v"(xa_[0]), "+v"(xa_[1]), "+v"(xa_[2]), "+v"(xa_[3]), "+v"(xb_[0]), "+v"(xb_[1]), "+v"(xb_[2]), "+v"(xb_[3]),
               "+v"(vr0), "+v"(vr1), "+v"(vt0), "+v"(vt1), "+v"(ta_), "+v"(tb_), "+v"(tw_));
  u32 qox = (g * 64 + w * 16) * 1024;
  asm volatile("s_waitcnt lgkmcnt(0)" : "+s"(k0), "+s"(qox));
  KWAIT();
  Args a;
  a.xa = (const u16*)k0[0]; a.wq = (const u16*)k0[1]; a.wk = (const u16*)k0[2]; a.wv = (const u16*)k0[3]; a.wo = (const u16*)k0[4];
  a.out = (float*)k0[5]; a.ws = (char*)k0[6];
  char* const ws = a.ws;
  const u32x4 sws = mk_srd(ws, WS_END), sxa = mk_srd(a.xa, 2048 * 1024), swq = mk_srd(a.wq - 1024, 512 * 1024 + 2048);
  ST(0);
  u64 ep = sload64(ws, WS_EP);
  if (ADELAY) __builtin_amdgcn_s_sleep(ADELAY);
  const u32 rot = QROT == 0 ? 0 : QROT == 2 ? (g & 1) * 4 : (g & 3) * 2;
  constexpr u32 QSL[8] = {16384, 32768, 49152, 0, 65536, 16384, 32768, 49152};
  auto qd = [&](u32 c) {
    const u32 kc = (c + rot) & 7;
    dmaq(QSL[c] + w * 4096, vr0, vr1, sxa, qox + kc * 128, vt0, vt1, swq, (kc * 64 + w * 16) * 1024);
  };
  qd(0); qd(1); qd(2); qd(3);
  asm volatile("s_waitcnt vmcnt(12) lgkmcnt(0)\n\ts_barrier" : "+s"(ep)::"memory");
  ST(1);
  const u32x2 tag = mk_tag(ep);
  const u32x4 skv = mk_srd(ws + WS_KV, 131072), swo = mk_srd(a.wo, 512 * 1024);
  u32 so = ((xcd + 1) & 7) * 16384;
  u32x4 qa, qb;
  {
    f32x16 qacc = {0, 0, 0, 0, 0, 0, 0, 0, 0, 0, 0, 0, 0, 0, 0, 0};
    bf16x8 fx[2][4], fw[2][4];
    auto step = [&](u32 c) {
      const u32 n = c + 4, kc = (n + rot) & 7;
#pragma unroll
      for (u32 s = 0; s < 4; ++s) {
        SCHED();
        if (c > 0) qacc = mfma32(fw[(c - 1) & 1][s], fx[(c - 1) & 1][s], qacc);
        SCHED();
        if (c < 8) {
          const u32 ta = c == 4 ? tb_ + s * 4096 : ta_ + QSL[c & 7] + s * 4096;
          fw[c & 1][s] = ldstr(ta, ta + 512);
          fx[c & 1][s] = c == 4 ? ldsr(xb_[s]) : ldsr(xa_[s] + QSL[c & 7]);
        }
        SCHED();
        if (n < 8) {
          const u32 m = QSL[n & 7] + w * 4096, ox = qox + kc * 128, ow = (kc * 64 + w * 16) * 1024;
          if (s == 0) dma1o<2048>(m, vt0, swq, ow);
          if (s == 1) dma1o<3072>(m, vt1, swq, ow);
          if (s == 2) dma1o<0>(m, vr0, sxa, ox);
          if (s == 3) dma1o<1024>(m, vr1, sxa, ox);
        }

      }
      SCHED();
    };
    step(0);
    waitvl_bar<12>(); SQ(2); step(1);
    waitvl_bar<12>(); SQ(3); step(2);
    waitvl_bar<12>(); SQ(4); step(3);
    __builtin_amdgcn_s_setprio(3);
    st8c(u32x2{h ? tag.x : 0u, h ? tag.y : 0u}, pre, sws);
    WAITV(13); BAR(); SQ(5); SA(2);
    u32x2 f0 = tag;
    auto fload = [&]() { if (w == 0) asm volatile("buffer_load_dwordx2 %0, %1, %2, 0 offen sc1" : "+v"(f0) : "v"(f0a), "s"(sws) : "memory"); };
    step(4);
    waitvl_bar<9>(); SQ(6);
    *(LAS u32x4*)(size_t)onea = u32x4{onev, onev, onev, onev};
    step(5);
    waitvl_bar<5>(); SQ(7);
    step(6);
    WAITV(1); BAR(); SQ(8); SA(3);
    fload();
    step(7);
    step(8);
    asm volatile("s_waitcnt vmcnt(0)" : "+v"(f0)::"memory");
    SQ(9);
    if (__builtin_amdgcn_ballot_w64(f0.x != tag.x || f0.y != tag.y) != 0) poll<0>(f0a, true, tag, sws);
    BAR(); SQ(10); SA(4);
#pragma unroll
    for (u32 u = 0; u < 4; ++u) dma1c<0>(u * 4096 + w * 1024, l16, skv, so + u * 4096 + w * 1024);
    so = (so + 16384) & 0x1ffff;
    const float qs = 0.25f * 1.44269504088896340736f;
#pragma unroll
    for (int i = 0; i < 4; ++i) { qa[i] = pk2(qacc[2 * i] * qs, qacc[2 * i + 1] * qs); qb[i] = pk2(qacc[8 + 2 * i] * qs, qacc[9 + 2 * i] * qs); }
    asm volatile("" : "+{v[128:131]}"(qa), "+{v[132:135]}"(qb));
  }
  const u32 wv0 = vt0;
  const u32 wso = (j * 65536 + w * 32768) & 0x7ffff;
  SA(5); SQ(11);
  {
    const u32 m0v = w * 4096 + 16384;
    const u32 tfo = fl0 + WS_TF + (g * 8 + j) * FS;
    const u64 tag64 = ((u64)tag.y << 32) | tag.x;
    asm volatile(
        "s_mov_b32 s40, %[k0]\n\ts_mov_b32 s41, %[k1]\n\ts_mov_b32 s42, 0x80000\n\ts_mov_b32 s43, 0x20000\n\t"
        "s_mov_b32 s44, 0\n\ts_mov_b32 s45, 0x1ffff\n\ts_mov_b32 s46, 0x4000\n\ts_mov_b32 s47, 1\n\ts_mov_b32 s50, 0x4000\n\t"
        "s_mov_b32 m0, %[m0v]\n\t"
        "s_branch 7f\n\t"
        "4:\n\t"
        "s_mov_b32 s59, 0x40000\n\t"
        "6:\n\t"
        "buffer_load_dwordx2 v[158:159], %[fwa], %[sws], 0 offen sc1\n\ts_sub_u32 s59, s59, 1\n\ts_cbranch_scc1 5f\n\t"
        "s_waitcnt vmcnt(0)\n\tv_cmp_ne_u64 vcc, %[tag64], v[158:159]\n\ts_nop 4\n\ts_cbranch_vccnz 6b\n\ts_branch 5f\n\t"
        "7:\n\t"
        "s_waitcnt vmcnt(3) lgkmcnt(0)\n\ts_barrier\n\t"
        "ds_read_b128 " VB0 ", %[ka]\n\tds_read_b128 " KF ", %[ka] offset:2048\n\t"
        "ds_read_b128 " VA0 ", %[va] offset:1024\n\tds_read_b128 " VA1 ", %[va] offset:1536\n\t"
        "s_waitcnt lgkmcnt(3)\n\t" QK(64, VB0, QA) QK(80, VB0, QB) "s_nop 7\n\t"
        ITE(4096, 2048, LT0, "", "", "s_waitcnt vmcnt(0)\n\t" ABAR, "", ADMA(0, 0))
        ITO(6144, 4096, "", LT1, ADMA(1, 1024), ADMA(2, 2048), "", ADMA(3, 3072))
        ITE(8192, 6144, NEXT, MASK, "buffer_load_dwordx2 v[158:159], %[fwa], %[sws], 0 offen sc1\n\t", M0A PRLO, "", "")
        ITO(10240, 8192, "", "", "", "", "", "")
        ITE(12288, 10240, "", "", "", "", "", "")
        ITO(14336, 12288, "", "", "", "", "", "")
        ITE(0, 14336, "", "", "s_waitcnt vmcnt(0)\n\tv_cmp_ne_u64 vcc, %[tag64], v[158:159]\n\t", "s_cbranch_vccnz 4b\n\t5:\n\t" WL ABAR BUMP, "", PRHI ADMA(0, 0))
        ITO(2048, 0, "", "", ADMA(1, 1024), ADMA(2, 2048), "", ADMA(3, 3072))
        PRLO NEXT MASK M0A
        "1:\n\t"
        ITE(4096, 2048, LT0, "", PRHI ADMA(0, 0), PRLO STEP, "", "")
        ITO(6144, 4096, "", LT1, PRHI ADMA(1, 1024), PRLO STEP, "", "")
        ITE(8192, 6144, "", "", PRHI ADMA(2, 2048), PRLO STEP, "", "")
        ITO(10240, 8192, "", "", PRHI ADMA(3, 3072), PRLO NEXT, MASK, M0A M0M)
        ITE(12288, 10240, "s_cmp_eq_u32 s47, 5\n\t", "s_cselect_b32 s40, %[w0], s40\n\t", "s_cselect_b32 s41, %[w1], s41\n\ts_cselect_b32 %[so], %[wso], %[so]\n\t",
            "s_cselect_b32 s44, 7168, s44\n\ts_cselect_b32 s50, 109568, s50\n\t", "s_cselect_b32 s45, 0x7ffff, s45\n\t", "s_cselect_b64 vcc, -1, 0\n\t")
        ITO(14336, 12288, "", "", "v_cndmask_b32 %[dv], %[dv], %[wv0], vcc\n\t", "", "", "")
        ITE(0, 14336, "", "", "", "s_waitcnt vmcnt(4) lgkmcnt(0)\n\t" ABAR BUMP, "", "")
        ITO(2048, 0, "", "", "s_add_u32 s47, s47, 1\n\ts_cmp_lg_u32 s47, 7\n\t", "", "", "")
        "s_cbranch_scc1 1b\n\t"
        ITE(4096, 2048, LT0, TRG(0), PRHI ADMA(0, 0), PRLO STEP, "", PRHI ADMA(1, 1024))
        ITO(6144, 4096, PRLO STEP, LT1 TRG(1), PRHI ADMA(2, 2048), PRLO STEP, "", PRHI ADMA(3, 3072))
        ITE(8192, 6144, PRLO NEXT, MASK TRG(2), M0A, "", "", PRHI ADMA(0, 0))
        ITO(10240, 8192, PRLO STEP, TRG(3), PRHI ADMA(1, 1024), PRLO STEP, "", PRHI ADMA(2, 2048))
        ITE(12288, 10240, PRLO STEP, TRG(4), PRHI ADMA(3, 3072), PRLO NEXT, MASK, M0A)
        ITO(14336, 12288, "", TRG(5), "", "", "", "")
        ITE(0, 14336, "", TRG(6), "", WL ABAR, "", PRHI ADMA(0, 0))
        ITO(2048, 0, STEP, TRG(7), ADMA(1, 1024), STEP ADMA(2, 2048), STEP, ADMA(3, 3072))
        EX(176, 13) EX(176, 14) EX(176, 15)
        "s_setprio 3\n\ts_waitcnt lgkmcnt(0)\n\t"
        SM(OB, VB1, 176, 8)
        : "+{v[96:111]}"(oa), "+{v[112:127]}"(ob), "+{v[184:191]}"(sinit), "+{v[152:155]}"(vinit), [ka] "+v"(ka), [va] "+v"(va), [so] "+s"(so),
          [dv] "+v"(dv) LTO
        : "{v[128:131]}"(qa), "{v[132:135]}"(qb), "{v156}"(sidx), [vinc] "v"(vinc), [wv0] "v"(wv0), [wso] "s"(wso),
          [m0v] "s"(m0v), [tag] "v"(tag), [tag64] "s"(tag64), [tfo] "v"(tfo), [fwa] "v"(fwa), [sws] "s"(sws),
          [k0] "s"(skv.x), [k1] "s"(skv.y), [w0] "s"(swo.x), [w1] "s"(swo.y)
        : "memory", "scc", "vcc", "s40", "s41", "s42", "s43", "s44", "s45", "s46", "s47", "s48", "s49", "s50", "s59",
          "v64", "v65", "v66", "v67", "v68", "v69", "v70", "v71", "v72", "v73", "v74", "v75", "v76", "v77", "v78", "v79", "v80", "v81", "v82", "v83", "v84", "v85", "v86", "v87", "v88", "v89", "v90", "v91", "v92", "v93", "v94", "v95", "v136", "v137", "v138", "v139", "v140", "v141", "v142", "v143", "v144", "v145", "v146", "v147", "v148", "v149", "v150", "v151", "v158", "v159", "v160", "v161", "v162", "v163", "v164", "v165", "v166", "v167", "v168", "v169", "v170", "v171", "v172", "v173", "v174", "v175", "v176", "v177", "v178", "v179", "v180", "v181", "v182", "v183");
  }
  const u32 ooff = WS_O + g * 65536 + ((j * 4 + hp * 2) * 2 + th) * 1024 + l16;
  const u32 olds = 65536 + (hp * 4 + th) * 1024 + l16;
  auto opub = [&](const f32x16& o, u32 t) {
    float inv;
    asm("v_rcp_f32 %0, %1\n\ts_nop 0" : "=v"(inv) : "v"(o[8]));
    u32x4 p;
#pragma unroll
    for (int i = 0; i < 4; ++i) p[i] = pk2(smul(o[2 * i], inv), smul(o[2 * i + 1], inv));
    st16c(p, ooff + t * 2048, sws);
    *(LAS u32x4*)(size_t)(olds + t * 2048) = p;
  };
  opub(oa, 0);
  asm volatile("s_nop 7" : "+{v[112:127]}"(ob));
  opub(ob, 1);
  SA(6);
  {
    float ma = 0.f;
#pragma unroll
    for (int i = 0; i < 8; ++i) { ma = __builtin_fmaf(oa[i], 0.f, ma); ma = __builtin_fmaf(ob[i], 0.f, ma); }
    const bool ok = oa[8] >= 0x1p-64f && oa[8] <= 0x1p100f && ob[8] >= 0x1p-64f && ob[8] <= 0x1p100f && ma == 0.f;
    if (__builtin_expect(__builtin_amdgcn_ballot_w64(!ok) != 0, 0)) {
      WAITV(0);
      const __amdgpu_buffer_rsrc_t rk = __builtin_amdgcn_make_buffer_rsrc(ws + WS_KV, 0, 131072, 0x20000);
      const u32x4 ones = {0x3f803f80u, 0x3f803f80u, 0x3f803f80u, 0x3f803f80u};
#pragma unroll 1
      for (int t = 0; t < 2; ++t) {
        const bf16x8 q = __builtin_bit_cast(bf16x8, t ? qb : qa);
        f32x16 z = {0, 0, 0, 0, 0, 0, 0, 0, 0, 0, 0, 0, 0, 0, 0, 0};
        float m = -__builtin_inff();
#pragma unroll 1
        for (u32 kt = 0; kt < 64; ++kt) {
          u32x4 kf = __builtin_amdgcn_raw_buffer_load_b128(rk, lane * 16, kt * 2048, 16);
          f32x16 s = mfma32(__builtin_bit_cast(bf16x8, kf), q, z);
#pragma unroll
          for (int i = 0; i < 16; ++i) m = __builtin_fmaxf(m, s[i]);
        }
        m = __builtin_fmaxf(m, __shfl_xor(m, 32));
        f32x16 o = z;
#pragma unroll 1
        for (u32 kt = 0; kt < 64; ++kt) {
          u32x4 kf = __builtin_amdgcn_raw_buffer_load_b128(rk, lane * 16, kt * 2048, 16);
          u32x4 v0 = __builtin_amdgcn_raw_buffer_load_b128(rk, (h * 16 + (r32 & 15)) * 16 + 1024, kt * 2048, 16);
          u32x4 v1 = __builtin_amdgcn_raw_buffer_load_b128(rk, (h * 16 + (r32 & 15)) * 16 + 1536, kt * 2048, 16);
          if (r32 >= 16) { v0 = ones; v1 = ones; }
          f32x16 s = mfma32(__builtin_bit_cast(bf16x8, kf), q, z);
          f32x8 p0, p1;
#pragma unroll
          for (int i = 0; i < 8; ++i) { p0[i] = __builtin_amdgcn_exp2f(s[i] - m); p1[i] = __builtin_amdgcn_exp2f(s[8 + i] - m); }
          o = __builtin_amdgcn_smfmac_f32_32x32x32_bf16(__builtin_bit_cast(bf16x8, v0), __builtin_bit_cast(bf16x16, p0), o, (int)0xdddddddd, 0, 0);
          o = __builtin_amdgcn_smfmac_f32_32x32x32_bf16(__builtin_bit_cast(bf16x8, v1), __builtin_bit_cast(bf16x16, p1), o, (int)0xdddddddd, 0, 0);
        }
        opub(o, t);
      }
    }
  }
  f32x16 acc = {0, 0, 0, 0, 0, 0, 0, 0, 0, 0, 0, 0, 0, 0, 0, 0};
  {
    bf16x8 wf[32], fo[2][4];
    const u32 ob_ = th * 1024 + l16;
    auto wread = [&](u32 i0, u32 i1) {
#pragma unroll
      for (u32 i = i0; i < i1; ++i) wf[i] = ldstr(tw_ + i * 2048, tw_ + i * 2048 + 1024);
      SCHED();
    };
    auto odma = [&](u32 c) { dma2c((c - 1) * 8192 + w * 2048, l16, sws, WS_O + g * 65536 + ((j + c) & 7) * 8192 + w * 2048); };
    auto oread = [&](u32 c) {
#pragma unroll
      for (u32 i = 0; i < 4; ++i) fo[c & 1][i] = ldsr(ob_ + (c ? (c - 1) * 8192 : 65536) + i * 2048);
      SCHED();
    };
    auto omul = [&](u32 c) {
#pragma unroll
      for (u32 i = 0; i < 4; ++i) acc = mfma32(fo[c & 1][i], wf[c * 4 + i], acc);
      SCHED();
    };
    waitvl_bar<6>();
    oread(0); wread(0, 12);
    WAITV(0);
    st8c(tag, fll + WS_OF + (g * 32 + j * 4 + w) * FS, sws);
    SA(7);
    wread(12, 24);
    BAR();
    wread(24, 32);
    if (w == 0) poll<0>(WS_OF + (g * 32 + r32) * FS, true, tag, sws);
    waitvl_bar<0>();
    SA(8);
    auto ostep = [&](u32 c) {
#pragma unroll
      for (u32 i = 0; i < 4; ++i) {
        SCHED();
        acc = mfma32(fo[(c - 1) & 1][i], wf[(c - 1) * 4 + i], acc);
        SCHED();
        fo[c & 1][i] = ldsr(ob_ + (c - 1) * 8192 + i * 2048);
      }
      SCHED();
    };
    odma(1); odma(2); odma(3); odma(4); odma(5);
    WAITV(8); BAR(); ostep(1); odma(6);
    WAITV(8); BAR(); ostep(2); odma(7);
    WAITV(8); BAR(); ostep(3);
    WAITV(6); BAR(); ostep(4);
    WAITV(4); BAR(); ostep(5);
    WAITV(2); BAR(); ostep(6);
    WAITV(0); BAR(); oread(7); omul(6); omul(7);
  }
  SA(9);
  float* op = a.out + (size_t)(g * 64 + th * 32 + h * 4) * 1024 + j * 64 + hp * 32 + r32;
  {
    const __amdgpu_buffer_rsrc_t ro = __builtin_amdgcn_make_buffer_rsrc(a.out, 0, 8388608, 0x20000);
    const u32 vo = (u32)((char*)op - (char*)a.out);
#pragma unroll
    for (int i = 0; i < 16; ++i) { const float x = acc[i]; __builtin_amdgcn_raw_buffer_store_b32(__builtin_bit_cast(u32, x), ro, vo, ((i & 3) + 8 * (i >> 2)) * 4096, AUXA); }
  }
  SA(10);
  STFLUSH;
}

DEVI void cmul(float& r, float& i, float ar, float ai, float br, float bi) { r = ar * br - ai * bi; i = ar * bi + ai * br; }
DEVI void cstep(float& r, float& i, float ar, float ai, float ur, float ui) {
  float nr = __builtin_fmaf(r, ar, __builtin_fmaf(-i, ai, ur)), ni = __builtin_fmaf(i, ar, __builtin_fmaf(r, ai, ui));
  r = nr; i = ni;
}

DEVI f32x2 fma2(f32x2 a, f32x2 b, f32x2 c) { return __builtin_elementwise_fma(a, b, c); }
DEVI void cstep2(f32x2& r, f32x2& i, float ar, float ai, f32x2 ur, f32x2 ui) {
  const f32x2 a = {ar, ar}, b = {ai, ai};
  const f32x2 nr = fma2(r, a, fma2(-i, b, ur)), ni = fma2(i, a, fma2(r, b, ui));
  r = nr; i = ni;
}

DEVI void role_b(u32 b, u32 w, u32 lane, u64 tf) {
  u64x8 k0 = args_request<0>(), k1 = args_request<64>();
  const u32 xcd = b & 7, idx = b >> 3, g = xcd * 4 + (idx >> 3), j = idx & 7;
  const u32 c16 = lane & 15, g4 = lane >> 4, r32 = lane & 31, h = lane >> 5, th = w & 1, pt = w >> 1;
  __builtin_amdgcn_s_setprio(3);
  STDECL;
  u32 vr0 = (lane >> 3) * 1024 + (((lane & 7) ^ (lane >> 4)) << 4);
  u32 vr1 = (lane >> 3) * 1024 + (((lane & 7) ^ (4 + (lane >> 4))) << 4) + 7168;
  const u32 xrow = (32 * h + 8 * ((lane >> 3) & 1) + ((lane >> 4) & 1)) * 1024;
  u32 vx0 = xrow + (((lane & 7) ^ (lane >> 4)) << 4);
  u32 vx1 = xrow + (((lane & 7) ^ (4 + (lane >> 4))) << 4) + 1024;
  u32 ra[4], rb[4];
#pragma unroll
  for (u32 s = 0; s < 4; ++s) { ra[s] = r32 * 128 + (((2 * s + h) ^ ((r32 >> 1) & 7)) << 4); rb[s] = ra[s] + 8192 + w * 4096; }
  u32 vo = (j * 64 + w * 16 + c16) * 2, vd = (j * 64 + pt * 32 + r32) * 2, l16 = lane * 16, fll = lane < FL ? lane * 8 : OOB;
  asm volatile("" : "+v"(vr0), "+v"(vr1), "+v"(vx0), "+v"(vx1), "+v"(ra[0]), "+v"(ra[1]), "+v"(ra[2]), "+v"(ra[3]),
               "+v"(rb[0]), "+v"(rb[1]), "+v"(rb[2]), "+v"(rb[3]), "+v"(vo), "+v"(vd), "+v"(l16), "+v"(fll));
  u32 xs_[4], xk_[4], red = 65536 + l16;
#pragma unroll
  for (u32 u = 0; u < 4; ++u) {
    xs_[u] = (4 * u + g4) * 1024 + ((c16 ^ (4 * u + g4)) << 4) - u * 1024;
    xk_[u] = w * 4096 + c16 * 256 + (((4 * u + g4) ^ c16) << 4);
  }
  u32 kvrd = 65536 + w * 4096 + ((lane >> 4) * 8 + ((lane & 15) >> 2)) * 32 + (lane & 3) * 8;
  u32 kvoff;
  {
    const u32 tq = j >> 1;
    const u32 ov = 1024 + (tq & 1) * 512 + ((g4 >> 1) * 16 + c16) * 16 + (g4 & 1) * 8;
    const u32 ok = ((g4 & 1) * 32 + (tq & 1) * 16 + c16) * 16 + (g4 >> 1) * 8;
    const u32 mv = 0u - (j & 1);
    kvoff = (WS_KV + (g * 2 + (tq >> 1)) * 2048 + ((ov & mv) | (ok & ~mv)));
  }
  u32 kvfo = lane == 16 ? WS_KVP + (g * 8 + j) * 8 : fll + WS_KVF + (g * 8 + j) * FS;
  u32 pre = g4 == 0 ? WS_EF + ((j * 4 + w) * 32 + g) * FS : g4 == 1 ? WS_HF + (g * 32 + j * 4 + w) * FS : WS_KVF + (g * 8 + j) * FS;
  pre = (pre + c16 * 8) | ((g4 < 2 || (w == 0 && g4 == 2)) ? 0 : OOB) | (TOUCH ? 0 : OOB);
  u32x2 zero2 = {0, 0};
  asm volatile("" : "+v"(pre), "+v"(zero2));
  asm volatile("" : "+v"(kvfo), "+v"(xs_[0]), "+v"(xs_[1]), "+v"(xs_[2]), "+v"(xs_[3]), "+v"(xk_[0]), "+v"(xk_[1]), "+v"(xk_[2]), "+v"(xk_[3]), "+v"(red), "+v"(kvrd), "+v"(kvoff));
  const u32 stg = 65536 + w * 4096;
  u32 rc0[4], rc1[4], rz0[4], rz1[4], tb0, tb1, rxw, rxr, vout, xw, xr_, eoff, efo, efp, hfo, hfp, rfa, tfa, vq, hw0, hw1, evo[8];
  {
    const u32 q4 = c16 >> 2, p4 = lane & 3, m = (g4 & 1) * 2 + (p4 >> 1);
    tb0 = stg + m * 256 + (((8 * h + q4) ^ (m << 2)) << 4) + (p4 & 1) * 8;
    tb1 = tb0 ^ 64;
#pragma unroll
    for (u32 s = 0; s < 4; ++s) { rz0[s] = ra[s] + pt * 12288; rz1[s] = rz0[s] ^ 4096; rc0[s] = rz0[s] + 16384; rc1[s] = rz1[s] + 16384; }
    rxw = 49152 + w * 4096 + l16; rxr = rxw ^ 8192;
    vout = (g * 64 + th * 32 + h * 4) * 4096 + 2048 + (j * 64 + pt * 32 + r32) * 4;
    xw = stg + lane * 8; xr_ = stg + c16 * 8;
    eoff = g4 == 0 ? WS_E + ((j * 4 + w) * 32 + g) * 128 + c16 * 8 : OOB;
    efo = fll + WS_EF + ((j * 4 + w) * 32 + g) * FS;
    efp = WS_EF + ((j * 4 + w) * 32 + (r32 < g ? r32 : g)) * FS;
    hfo = fll + WS_HF + (g * 32 + j * 4 + w) * FS;
    hfp = WS_HF + (g * 32 + r32) * FS;
    rfa = WS_RF + (g * 8 + j) * FS; tfa = WS_TF + (g * 8 + j) * FS;
    vq = (g * 64 + th * 32 + r32) * 1024 + j * 128 + pt * 64 + h * 16;
    const u32 m0_ = (g4 & 1) * 2;
    hw0 = stg + h * 2048 + m0_ * 256 + ((c16 ^ (m0_ << 2)) << 4);
    hw1 = stg + h * 2048 + (m0_ + 1) * 256 + ((c16 ^ ((m0_ + 1) << 2)) << 4);
    const int gb = (int)g - 8 - 8 * (int)g4;
#pragma unroll
    for (int i = 0; i < 8; ++i) evo[i] = gb + i >= 0 ? (u32)(gb + i) * 128 + c16 * 8 : OOB;
    asm volatile("" : "+v"(rc0[0]), "+v"(rc0[1]), "+v"(rc0[2]), "+v"(rc0[3]), "+v"(rc1[0]), "+v"(rc1[1]), "+v"(rc1[2]), "+v"(rc1[3]),
                 "+v"(tb0), "+v"(tb1), "+v"(rxw), "+v"(rxr), "+v"(vout), "+v"(xw), "+v"(xr_), "+v"(eoff), "+v"(efo), "+v"(efp), "+v"(hfo), "+v"(hfp));
    asm volatile("" : "+v"(rfa), "+v"(tfa), "+v"(vq), "+v"(hw0), "+v"(hw1), "+v"(evo[0]), "+v"(evo[1]), "+v"(evo[2]), "+v"(evo[3]),
                 "+v"(evo[4]), "+v"(evo[5]), "+v"(evo[6]), "+v"(evo[7]),
                 "+v"(rz0[0]), "+v"(rz0[1]), "+v"(rz0[2]), "+v"(rz0[3]), "+v"(rz1[0]), "+v"(rz1[1]), "+v"(rz1[2]), "+v"(rz1[3]));
  }
  u32 kox = (g * 64 + (j >> 1) * 16) * 1024 + w * 256;
  asm volatile("s_waitcnt lgkmcnt(0)" : "+s"(k0), "+s"(k1), "+s"(kox));
  KWAIT();
  Args a;
  a.out = (float*)k0[5]; a.ws = (char*)k0[6]; a.xb = (const u16*)k0[7];
  a.lre = (const u16*)k1[0]; a.lim = (const u16*)k1[1]; a.bre = (const u16*)k1[2]; a.bim = (const u16*)k1[3];
  a.cre = (const u16*)k1[4]; a.cim = (const u16*)k1[5]; a.ldt = (const u16*)k1[6]; a.dsk = (const u16*)k1[7];
  char* const ws = a.ws;
  const u32x4 sws = mk_srd(ws, WS_END), sxb = mk_srd(a.xb, 2048 * 1024);
  ST(0);
  u64 tb, ep;
  asm volatile("s_memtime %0\n\ts_load_dwordx2 %1, %2, 0" : "=&s"(tb), "=&s"(ep) : "s"(ws) : "memory");
  dma4v(w * 4096, xs_[0], xs_[1], xs_[2], xs_[3], mk_srd((const void*)k0[0], 2048 * 1024), kox);
  dma4(65536 + w * 4096, l16, mk_srd((const void*)((j & 1) ? k0[3] : k0[2]), 512 * 32), w * 4096);
  st8c(zero2, pre, sws);
  u32 plr, pli, pdt, pdk;
  {
    const u32x4 s0 = mk_srd(a.lre, 1024), s1 = mk_srd(a.lim, 1024), s2 = mk_srd(a.ldt, 1024), s3 = mk_srd(a.dsk, 1024);
    asm volatile("buffer_load_ushort %0, %8, %4, 0 offen\n\tbuffer_load_ushort %1, %8, %5, 0 offen\n\t"
                 "buffer_load_ushort %2, %8, %6, 0 offen\n\tbuffer_load_ushort %3, %9, %7, 0 offen"
                 : "=&v"(plr), "=&v"(pli), "=&v"(pdt), "=&v"(pdk) : "s"(s0), "s"(s1), "s"(s2), "s"(s3), "v"(vo), "v"(vd) : "memory");
  }
  f32x16 ur = {0, 0, 0, 0, 0, 0, 0, 0, 0, 0, 0, 0, 0, 0, 0, 0}, ui = ur;
  float ar, ai, gr, gi, a8r, a8i, a16r, a16i, a64r, a64i, akr, aki;
  u32x4 dg0, dg1;
  u32x2 tag, tagv;
  {
    const u32x4 sbr = mk_srd(a.bre, 512 * 1024), sbi = mk_srd(a.bim, 512 * 1024);
    const u32 rot = BROT == 0 ? 0 : (j + (g & 3) * 2) & 7;
    auto bdx = [&](u32 c) { dma2((c % 3) * 24576 + w * 2048, vx0, vx1, sxb, (g * 64 + pt * 16 + th * 4) * 1024 + ((c + rot) & 7) * 128); };
    auto bdr = [&](u32 c) { dma2((c % 3) * 24576 + 8192 + w * 4096, vr0, vr1, sbr, (j * 64 + w * 16) * 1024 + ((c + rot) & 7) * 128); };
    auto bdi = [&](u32 c) { dma2((c % 3) * 24576 + 10240 + w * 4096, vr0, vr1, sbi, (j * 64 + w * 16) * 1024 + ((c + rot) & 7) * 128); };
    asm volatile("s_waitcnt vmcnt(5) lgkmcnt(0)" : "+s"(tb), "+s"(ep)::"memory");
    SK(1);
    {
      bf16x8 wf[4], xf[4];
#pragma unroll
      for (int i = 0; i < 4; ++i) { xf[i] = ldsr(xk_[i]); wf[i] = ldstr(kvrd + i * 1024, kvrd + i * 1024 + 128); }
      f32x4 acc = {0, 0, 0, 0};
      if (j & 1) {
#pragma unroll
        for (int i = 0; i < 4; ++i) acc = mfma16(xf[i], wf[i], acc);
      } else {
#pragma unroll
        for (int i = 0; i < 4; ++i) acc = mfma16(wf[i], xf[i], acc);
      }
      *(LAS f32x4*)(size_t)(red + w * 4096) = acc;
      waitvl_bar<63>();
      SK(2);
      if (w == 0) {
        f32x4 s0 = *(LAS f32x4*)(size_t)(red), s1 = *(LAS f32x4*)(size_t)(red + 4096), s2 = *(LAS f32x4*)(size_t)(red + 8192), s3 = *(LAS f32x4*)(size_t)(red + 12288);
        s0 = (s0 + s1) + (s2 + s3);
        st8c(u32x2{pk2(s0[0], s0[1]), pk2(s0[2], s0[3])}, kvoff, sws);
      }
    }
    SK(3);
    tag = mk_tag(ep);
    tagv = tag;
    asm volatile("s_waitcnt vmcnt(0)" : "+v"(tagv), "+v"(plr), "+v"(pli), "+v"(pdt), "+v"(pdk)::"memory");
    if (w == 0) st8c(tagv, kvfo, sws);
    __builtin_amdgcn_s_setprio(2);
    SB(1);
    while (__builtin_amdgcn_s_memtime() - tb < BSTART) __builtin_amdgcn_s_sleep(2);
    BAR();
    bdx(0); bdr(0); bdi(0); bdx(1); bdr(1); bdi(1);
    {
      const float lr = bf2f(plr), li = bf2f(pli);
      const float dt = bf2f(pk2(expf(bf2f(pdt)), 0.f) & 0xffffu);
      const float e = expf(dt * lr);
      float sn, cs;
      sincosf(dt * li, &sn, &cs);
      ar = e * cs; ai = e * sn;
      const float nr = ar - 1.f, ni = ai;
      const bool sw = __builtin_fabsf(lr) >= __builtin_fabsf(li);
      const float big = sw ? lr : li, sm = sw ? li : lr, rt = sm / big, den = big + sm * rt;
      const float x = sw ? nr : ni, y = sw ? ni : nr;
      gr = 2.f * (x + y * rt) / den;
      gi = 2.f * (y - x * rt) / den;
      gi = sw ? gi : -gi;
      ai = -ai;
      a8r = ar; a8i = ai;
#pragma unroll
      for (int i = 0; i < 3; ++i) cmul(a8r, a8i, a8r, a8i, a8r, a8i);
      cmul(a16r, a16i, a8r, a8i, a8r, a8i);
      cmul(a64r, a64i, a16r, a16i, a16r, a16i);
      cmul(a64r, a64i, a64r, a64i, a64r, a64i);
      akr = a64r; aki = a64i;
#pragma unroll
      for (int i = 0; i < 3; ++i) cmul(akr, aki, akr, aki, akr, aki);
      const u32 dv = pdk << ((r32 & 1) * 16), di = (r32 >> 1) - 4 * h;
#pragma unroll
      for (u32 i = 0; i < 4; ++i) { dg0[i] = di == i ? dv : 0u; dg1[i] = di == 8 + i ? dv : 0u; }
      asm volatile("" : "+v"(ar), "+v"(ai), "+v"(gr), "+v"(gi), "+v"(a8r), "+v"(a8i), "+v"(a16r), "+v"(a16i), "+v"(a64r), "+v"(a64i),
                   "+v"(akr), "+v"(aki), "+v"(dg0), "+v"(dg1));
      SCHED();
    }
    SB(2);
    __builtin_amdgcn_s_setprio(BPRIO);
    bf16x8 fx[2][8], fb[2][4];
    auto step = [&](u32 c, bool rd, bool mul) {
      const u32 sl = (c % 3) * 24576, n = c + 2, p = (c - 1) & 1;
#pragma unroll
      for (u32 s = 0; s < 4; ++s) {
        SCHED();
        if (mul) ur = mfma32(fx[p][2 * s], fb[p][s], ur);
        SCHED();
        if (rd) { fb[c & 1][s] = ldsr(rb[s] + sl); fx[c & 1][2 * s] = ldsr(ra[s] + sl); }
        SCHED();
        if (mul) ui = mfma32(fx[p][2 * s + 1], fb[p][s], ui);
        SCHED();
        if (rd) fx[c & 1][2 * s + 1] = ldsr(ra[s] + sl + 4096);
        SCHED();
        if (rd && c > 1 && n < 8 && s == 0) bdx(n);
        if (rd && c > 1 && n < 8 && s == 1) bdr(n);
        if (rd && c > 1 && n < 8 && s == 2) bdi(n);
      }
      SCHED();
    };
    SB(3);
    waitvl_bar<6>(); SB(4); step(0, true, false);
    waitvl_bar<0>(); SB(5); step(1, true, true);
    step(2, false, true);
    while (__builtin_amdgcn_s_memtime() - tb < BSTART2) __builtin_amdgcn_s_sleep(2);
    bdx(2); PACE; bdr(2); PACE; bdi(2); PACE; bdx(3); PACE; bdr(3); PACE; bdi(3);
    waitvl_bar<6>(); SB(6); step(2, true, false);
    waitvl_bar<6>(); SB(7); step(3, true, true);
    waitvl_bar<6>(); SB(8); step(4, true, true);
    waitvl_bar<6>(); SB(9); step(5, true, true);
    waitvl_bar<6>(); SB(10); step(6, true, true);
    waitvl_bar<0>(); step(7, true, true);
    waitvl_bar<0>(); step(8, false, true);
  }
  __builtin_amdgcn_s_setprio(2);
  ST(1);
  f32x2 er = {0.f, 0.f}, ei = {0.f, 0.f};
  {
#pragma unroll
    for (int i = 0; i < 16; ++i) {
      const float p = ur[i], q = ui[i];
      const auto r = __builtin_amdgcn_permlane16_swap(__builtin_bit_cast(u32, p), __builtin_bit_cast(u32, q), false, false);
      const u32 r0 = r[0], r1 = r[1];
      ur[i] = __builtin_bit_cast(float, r0); ui[i] = __builtin_bit_cast(float, r1);
    }
    const f32x2 g_r = {gr, gr}, g_i = {gi, gi};
#pragma unroll
    for (int m = 0; m < 8; ++m) {
      const f32x2 xr = {ur[2 * m], ur[2 * m + 1]}, xi = {ui[2 * m], ui[2 * m + 1]};
      const f32x2 yr = fma2(g_r, xr, -(g_i * xi)), yi = fma2(-g_r, xi, -(g_i * xr));
      ur[2 * m] = yr.x; ur[2 * m + 1] = yr.y; ui[2 * m] = yi.x; ui[2 * m + 1] = yi.y;
      if (m == 0) { er = yr; ei = yi; } else cstep2(er, ei, ar, ai, yr, yi);
    }
  }
  f32x2 es[4];
  {
    float tr = er.x, ti = ei.x;
    cstep(tr, ti, a8r, a8i, er.y, ei.y);
    *(volatile LAS f32x2*)(size_t)xw = f32x2{tr, ti};
#pragma unroll
    for (int s = 0; s < 4; ++s) es[s] = *(volatile LAS f32x2*)(size_t)(xr_ + s * 128);
    tr = es[0].x; ti = es[0].y;
#pragma unroll
    for (int s = 1; s < 4; ++s) cstep(tr, ti, a16r, a16i, es[s].x, es[s].y);
    u32x2 ev = {__builtin_bit_cast(u32, tr), __builtin_bit_cast(u32, ti)};
    st8c(ev, eoff, sws);
  }
  const u32x4 scr = mk_srd(a.cre, 512 * 1024), sci = mk_srd(a.cim, 512 * 1024);
  auto cdma = [&](u32 c) {
    const u32 sl = (c % 5) * 16384 + w * 2048, so = (j * 64 + w * 16) * 1024 + c * 128;
    dma2(sl, vr0, vr1, scr, so);
    dma2(sl + 8192, vr0, vr1, sci, so);
  };
  u32x4 xq0, xq1;
  u32x2 rf;
  asm volatile("buffer_load_dwordx2 %0, %1, %2, 0 offen sc1" : "=v"(rf) : "v"(rfa), "s"(sws) : "memory");
  cdma(0); cdma(1);
  {
    asm volatile("buffer_load_dwordx4 %0, %2, %3, 0 offen\n\tbuffer_load_dwordx4 %1, %2, %3, 0 offen offset:32" : "=&v"(xq0), "=&v"(xq1) : "v"(vq), "s"(sxb) : "memory");
  }
  asm volatile("s_waitcnt vmcnt(10)" : "+v"(rf)::"memory");
  if (__builtin_amdgcn_ballot_w64(rf.x != tag.x || rf.y != tag.y) != 0) poll<4>(rfa, true, tag, sws);
  st8c(tagv, efo, sws);
  ST(2);
  asm volatile("s_waitcnt vmcnt(1)" : "+v"(xq0), "+v"(xq1)::"memory");
  if (EDLY) __builtin_amdgcn_s_sleep(EDLY);
  poll<PSL>(efp, true, tag, sws);
  ST(3);
  {
    const __amdgpu_buffer_rsrc_t re = __builtin_amdgcn_make_buffer_rsrc(ws + WS_E + (j * 4 + w) * 4096, 0, 4096, 0x20000);
    u32x2 ev[8];
#pragma unroll
    for (int m = 0; m < 8; ++m) ev[m] = __builtin_amdgcn_raw_buffer_load_b64(re, evo[m], 0, 16);
    float cr, ci;
    { const f32x2 e2 = __builtin_bit_cast(f32x2, ev[0]); cr = e2.x; ci = e2.y; }
#pragma unroll
    for (int m = 1; m < 8; ++m) { const f32x2 e2 = __builtin_bit_cast(f32x2, ev[m]); cstep(cr, ci, a64r, a64i, e2.x, e2.y); }
    *(volatile LAS f32x2*)(size_t)xw = f32x2{cr, ci};
    f32x2 ps[4];
#pragma unroll
    for (int s = 0; s < 4; ++s) ps[s] = *(volatile LAS f32x2*)(size_t)(xr_ + s * 128);
    cr = ps[3].x; ci = ps[3].y;
#pragma unroll
    for (int k = 2; k >= 0; --k) {
      if (8u * (k + 1) < g) cstep(cr, ci, akr, aki, ps[k].x, ps[k].y); else { cr = ps[k].x; ci = ps[k].y; }
    }
#pragma unroll
    for (u32 s = 0; s < 3; ++s) {
      float tr = cr, ti = ci;
      cstep(tr, ti, a16r, a16i, es[s].x, es[s].y);
      cr = s < g4 ? tr : cr; ci = s < g4 ? ti : ci;
    }
    float br = cr, bi = ci;
    cstep(br, bi, a8r, a8i, er.x, ei.x);
    f32x2 hr = {cr, br}, hi = {ci, bi};
#pragma unroll
    for (int m = 0; m < 8; ++m) {
      cstep2(hr, hi, ar, ai, f32x2{ur[2 * m], ur[2 * m + 1]}, f32x2{ui[2 * m], ui[2 * m + 1]});
      ur[2 * m] = hr.x; ur[2 * m + 1] = hr.y; ui[2 * m] = hi.x; ui[2 * m + 1] = hi.y;
    }
#pragma unroll
    for (u32 i = 0; i < 2; ++i) {
      u32x4 pr, pi;
#pragma unroll
      for (int t = 0; t < 4; ++t) { pr[t] = pk2(ur[4 * t + i], ur[4 * t + 2 + i]); pi[t] = pk2(ui[4 * t + i], ui[4 * t + 2 + i]); }
      *(LAS u32x4*)(size_t)(i ? hw1 : hw0) = pr;
      *(LAS u32x4*)(size_t)((i ? hw1 : hw0) + 1024) = pi;
    }
    asm volatile("" ::: "memory");
#pragma unroll
    for (u32 f = 0; f < 4; ++f)
      st16s(__builtin_bit_cast(u32x4, ldstr(tb0 + f * 1024, tb1 + f * 1024)), l16, sws, WS_H + g * 131072 + f * 32768 + (j * 4 + w) * 1024);
    cdma(2); cdma(3);
    WAITV(8);
    st8c(tagv, hfo, sws);
  }
  ST(4);
  f32x16 acc[2];
  {
    const f32x16 z = {0, 0, 0, 0, 0, 0, 0, 0, 0, 0, 0, 0, 0, 0, 0, 0};
    acc[0] = mfma32(__builtin_bit_cast(bf16x8, xq0), __builtin_bit_cast(bf16x8, dg0), z);
    acc[0] = mfma32(__builtin_bit_cast(bf16x8, xq1), __builtin_bit_cast(bf16x8, dg1), acc[0]);
    asm volatile("" : "+v"(acc[0]));
  }
  if (HDLY) __builtin_amdgcn_s_sleep(HDLY);
  if (w == 0) poll<PSL>(hfp, true, tag, sws);
  waitvl_bar<0>();
  ST(5);
  __builtin_amdgcn_s_setprio(BPRIO);
  cdma(4);
  bf16x8 hf[32];
  {
    const __amdgpu_buffer_rsrc_t rh = __builtin_amdgcn_make_buffer_rsrc(ws + WS_H + g * 131072 + th * 65536 + pt * 32768, 0, 32768, 0x20000);
#pragma unroll
    for (u32 k = 0; k < 32; ++k) {
      hf[k] = __builtin_bit_cast(bf16x8, __builtin_amdgcn_raw_buffer_load_b128(rh, l16 + (k & 3) * 1024, (k >> 2) * 4096, 16));
      if (HPACE && (k & 1)) { SCHED(); __builtin_amdgcn_s_sleep(HPACE); SCHED(); }
    }
    SCHED();
  }
  ST(6);
  {
    bf16x8 fc[8];
    auto cread = [&](u32 i) {
      const u32 o = ((i >> 3) % 5) * 16384, s = (i >> 1) & 3;
      fc[i & 7] = o ? ldsr(((i & 1) ? rc0[s] : rc1[s]) + (o - 16384)) : ldsr((i & 1) ? rz0[s] : rz1[s]);
    };
    auto crun = [&](u32 i0, u32 i1) {
      const f32x16 z = {0, 0, 0, 0, 0, 0, 0, 0, 0, 0, 0, 0, 0, 0, 0, 0};
#pragma unroll
      for (u32 i = i0; i < i1; ++i) {
        SCHED();
        if (i & 1) acc[0] = mfma32(hf[i >> 1], fc[i & 7], acc[0]); else acc[1] = mfma32(hf[i >> 1], fc[i & 7], i ? acc[1] : z);
        SCHED();
        if (i + 6 < 64) cread(i + 6);
      }
      SCHED();
    };
#pragma unroll
    for (u32 i = 0; i < 6; ++i) cread(i);
    crun(0, 18);
    asm volatile("" : "+v"(hf[31]));
    waitvl_bar<0>();
    cdma(5); cdma(6); cdma(7);
    crun(18, NSH);
    if (w == 0) { __builtin_amdgcn_s_setprio(2); poll<TSL>(tfa, true, tag, sws); }
    if (g == 31 && j == 0 && w == 0) st8c(tag, fll + WS_EP, sws);
    __builtin_amdgcn_s_setprio(BPRIO);
    waitvl_bar<0>();
    ST(7);
    crun(NSH, 64);
  }
  __builtin_amdgcn_s_setprio(2);
  ST(8);
  waitvl_bar<63>();
#pragma unroll
  for (int i = 0; i < 4; ++i) *(LAS f32x4*)(size_t)(rxw + i * 1024) = f32x4{acc[1][4 * i], acc[1][4 * i + 1], acc[1][4 * i + 2], acc[1][4 * i + 3]};
  waitvl_bar<63>();
  {
    const __amdgpu_buffer_rsrc_t ro = __builtin_amdgcn_make_buffer_rsrc(a.out, 0, 8388608, 0x20000);
#pragma unroll
    for (int i = 0; i < 4; ++i) {
      const f32x4 p = *(LAS f32x4*)(size_t)(rxr + i * 1024);
#pragma unroll
      for (int e = 0; e < 4; ++e) { const float x = acc[0][4 * i + e] + p[e]; __builtin_amdgcn_raw_buffer_store_b32(__builtin_bit_cast(u32, x), ro, vout, (e + 8 * i) * 4096, AUXB); }
    }
  }
  ST(9);
  STFLUSH;
}

__global__ void __launch_bounds__(256, 2) fused_kernel(Args) {
  __shared__ __attribute__((aligned(16))) char lds[81920];
  u64 tf = 0;
  asm volatile("" ::"v"((u32)(size_t)(LAS char*)lds));
  const u32 lane = threadIdx.x & 63, w = __builtin_amdgcn_readfirstlane(threadIdx.x >> 6), b = blockIdx.x;
  if (__builtin_expect(b < 256, 1)) role_a(b, w, lane, tf); else {
    role_b(b - 256, w, lane, tf);
  }
}

extern "C" void kernel_launch(void* const* d_in, const int* in_sizes, int n_in, void* d_out, int out_size, void* d_ws,
                              size_t ws_size, hipStream_t stream) {
  Args a;
  a.xa = (const u16*)d_in[0]; a.wq = (const u16*)d_in[1]; a.wk = (const u16*)d_in[2]; a.wv = (const u16*)d_in[3];
  a.wo = (const u16*)d_in[4]; a.xb = (const u16*)d_in[5]; a.lre = (const u16*)d_in[6]; a.lim = (const u16*)d_in[7];
  a.bre = (const u16*)d_in[8]; a.bim = (const u16*)d_in[9]; a.cre = (const u16*)d_in[10]; a.cim = (const u16*)d_in[11];
  a.ldt = (const u16*)d_in[12]; a.dsk = (const u16*)d_in[13];
  a.out = (float*)d_out; a.ws = (char*)d_ws;
  fused_kernel<<<512, 256, 0, stream>>>(a);
}
```

```cpp
#include <hip/hip_runtime.h>
typedef unsigned short u16;
typedef unsigned u32;
typedef unsigned long long u64;
typedef float f32x2 __attribute__((ext_vector_type(2)));
typedef float f32x4 __attribute__((ext_vector_type(4)));
typedef float f32x8 __attribute__((ext_vector_type(8)));
typedef float f32x16 __attribute__((ext_vector_type(16)));
typedef __bf16 bf16x2 __attribute__((ext_vector_type(2)));
typedef __bf16 bf16x8 __attribute__((ext_vector_type(8)));
typedef __bf16 bf16x16 __attribute__((ext_vector_type(16)));
typedef short s16x4 __attribute__((ext_vector_type(4)));
typedef u32 u32x2 __attribute__((ext_vector_type(2)));
typedef u32 u32x4 __attribute__((ext_vector_type(4)));
#define LAS __attribute__((address_space(3)))
#define DEVI __device__ __forceinline__

constexpr u32 FS = 128;
constexpr u32 WS_EP = 0;
constexpr u32 WS_KVP = 4096;
constexpr u32 WS_KVF = 32768;
constexpr u32 WS_RF = 65536;
constexpr u32 WS_TF = 98304;
constexpr u32 WS_EF = 131072;
constexpr u32 WS_HF = 262144;
constexpr u32 WS_KV = 393216;
constexpr u32 WS_E = 524288;
constexpr u32 WS_OF = 655360;
constexpr u32 WS_O = 1048576;
constexpr u32 WS_H = WS_O + 2097152;
constexpr u32 WS_END = WS_H + 4194304;
constexpr u32 OOB = 0x80000000u;
constexpr u32 SENT = 0xffffffffu;
constexpr int WT = 17;

struct Args {
  const u16 *xa, *wq, *wk, *wv, *wo;
  float* out;
  char* ws;
  const u16 *xb, *lre, *lim, *bre, *bim, *cre, *cim, *ldt, *dsk;
};
typedef u64 u64x8 __attribute__((ext_vector_type(8)));

DEVI u32x4 mk_srd(const void* p, u32 bytes) {
  u64 a = (u64)p;
  u32x4 r = {(u32)a, (u32)(a >> 32) & 0xffffu, bytes, 0x20000u};
  return r;
}
DEVI void dma2(u32 m0v, u32 v0, u32 v1, u32x4 srd, u32 soff) {
  asm volatile("s_mov_b32 m0, %0\n\ts_nop 0\n\tbuffer_load_dwordx4 %1, %3, %4 offen lds\n\tbuffer_load_dwordx4 %2, %3, %4 offen offset:1024 lds"
               ::"s"(m0v), "v"(v0), "v"(v1), "s"(srd), "s"(soff) : "memory");
}
DEVI void dma2c(u32 m0v, u32 v0, u32x4 srd, u32 soff) {
  asm volatile("s_mov_b32 m0, %0\n\ts_nop 0\n\tbuffer_load_dwordx4 %1, %2, %3 offen sc1 lds\n\tbuffer_load_dwordx4 %1, %2, %3 offen offset:1024 sc1 lds"
               ::"s"(m0v), "v"(v0), "s"(srd), "s"(soff) : "memory");
}
DEVI void dma4(u32 m0v, u32 v0, u32x4 srd, u32 soff) {
  asm volatile("s_mov_b32 m0, %0\n\ts_nop 0\n\tbuffer_load_dwordx4 %1, %2, %3 offen lds\n\tbuffer_load_dwordx4 %1, %2, %3 offen offset:1024 lds\n\t"
               "buffer_load_dwordx4 %1, %2, %3 offen offset:2048 lds\n\tbuffer_load_dwordx4 %1, %2, %3 offen offset:3072 lds"
               ::"s"(m0v), "v"(v0), "s"(srd), "s"(soff) : "memory");
}
DEVI void dma4c(u32 m0v, u32 v0, u32x4 srd, u32 soff) {
  asm volatile("s_mov_b32 m0, %0\n\ts_nop 0\n\tbuffer_load_dwordx4 %1, %2, %3 offen sc1 lds\n\tbuffer_load_dwordx4 %1, %2, %3 offen offset:1024 sc1 lds\n\t"
               "buffer_load_dwordx4 %1, %2, %3 offen offset:2048 sc1 lds\n\tbuffer_load_dwordx4 %1, %2, %3 offen offset:3072 sc1 lds"
               ::"s"(m0v), "v"(v0), "s"(srd), "s"(soff) : "memory");
}
DEVI void dma4v(u32 m0v, u32 v0, u32 v1, u32 v2, u32 v3, u32x4 srd, u32 soff) {
  asm volatile("s_mov_b32 m0, %0\n\ts_nop 0\n\tbuffer_load_dwordx4 %1, %5, %6 offen lds\n\tbuffer_load_dwordx4 %2, %5, %6 offen offset:1024 lds\n\t"
               "buffer_load_dwordx4 %3, %5, %6 offen offset:2048 lds\n\tbuffer_load_dwordx4 %4, %5, %6 offen offset:3072 lds"
               ::"s"(m0v), "v"(v0), "v"(v1), "v"(v2), "v"(v3), "s"(srd), "s"(soff) : "memory");
}
template <int OFF> DEVI u64x8 args_request() {
  u64x8 k;
  asm volatile("s_load_dwordx16 %0, %1, %2" : "=&s"(k) : "s"(__builtin_amdgcn_kernarg_segment_ptr()), "i"(OFF));
  return k;
}
template <int OFF> DEVI void dma1o(u32 m0v, u32 v0, u32x4 srd, u32 soff) {
  asm volatile("s_mov_b32 m0, %0\n\ts_nop 0\n\tbuffer_load_dwordx4 %1, %2, %3 offen offset:%4 lds" ::"s"(m0v), "v"(v0), "s"(srd), "s"(soff), "i"(OFF) : "memory");
}
DEVI void dmaq(u32 m0v, u32 v0, u32 v1, u32x4 sx, u32 ox, u32 v2, u32 v3, u32x4 sw, u32 ow) {
  asm volatile("s_mov_b32 m0, %0\n\ts_nop 0\n\tbuffer_load_dwordx4 %5, %7, %8 offen offset:2048 lds\n\tbuffer_load_dwordx4 %6, %7, %8 offen offset:3072 lds\n\t"
               "buffer_load_dwordx4 %1, %3, %4 offen lds\n\tbuffer_load_dwordx4 %2, %3, %4 offen offset:1024 lds"
               ::"s"(m0v), "v"(v0), "v"(v1), "s"(sx), "s"(ox), "v"(v2), "v"(v3), "s"(sw), "s"(ow) : "memory");
}
DEVI u32x2 mk_tag(u64 ep) {
  u32 lo = (u32)ep, hi = (u32)(ep >> 32), t;
  asm("s_add_u32 %0, %0, 0x7f4a7c15\n\ts_addc_u32 %1, %1, 0x9e3779b9\n\ts_or_b32 %2, %0, %1\n\ts_cselect_b32 %0, %0, 1" : "+s"(lo), "+s"(hi), "=&s"(t)::"scc");
  return u32x2{lo, hi};
}
DEVI void st8c(u32x2 v, u32 voff, u32x4 srd) {
  asm volatile("buffer_store_dwordx2 %0, %1, %2, 0 offen sc1" ::"v"(v), "v"(voff), "s"(srd) : "memory");
}
DEVI void st16s(u32x4 v, u32 voff, u32x4 srd, u32 soff) {
  asm volatile("buffer_store_dwordx4 %0, %1, %2, %3 offen sc1\n\ts_nop 1" ::"v"(v), "v"(voff), "s"(srd), "s"(soff) : "memory");
}
#define WAITV(n) asm volatile("s_waitcnt vmcnt(" #n ")" ::: "memory")
#define BAR() asm volatile("s_barrier" ::: "memory")
template <int N> DEVI void waitvl_bar() { asm volatile("s_waitcnt vmcnt(%0) lgkmcnt(0)\n\ts_barrier" ::"i"(N) : "memory"); }

template <int NAP> DEVI void poll(u32 voff, u32x2 tag, u32x4 srd) {
#pragma unroll 1
  for (u32 it = 0; it < (1u << 20); ++it) {
    u32x2 f;
    asm volatile("buffer_load_dwordx2 %0, %1, %2, 0 offen sc1\n\ts_waitcnt vmcnt(0)" : "=&v"(f) : "v"(voff), "s"(srd) : "memory");
    if (__builtin_amdgcn_ballot_w64(__builtin_bit_cast(u64, f) != __builtin_bit_cast(u64, tag)) == 0) break;
    if (NAP) __builtin_amdgcn_s_sleep(NAP);
  }
}
DEVI u32 pk2(float a, float b) {
  f32x2 x = {a, b};
  return __builtin_bit_cast(u32, __builtin_convertvector(x, bf16x2));
}
DEVI float sadd(float a, float b) { float r; asm("v_add_f32 %0, %1, %2" : "=v"(r) : "v"(a), "v"(b)); return r; }
DEVI u32 umax3(u32 a, u32 b, u32 c) { const u32 m = a > b ? a : b; return m > c ? m : c; }
DEVI float bf2f(u32 v) { return __builtin_bit_cast(float, v << 16); }
DEVI bf16x8 ldsr(u32 a) { return *(const LAS bf16x8*)(size_t)a; }
DEVI bf16x8 ldstr(u32 a0, u32 a1) {
  s16x4 x = __builtin_amdgcn_ds_read_tr16_b64_v4i16((LAS s16x4*)(size_t)a0);
  s16x4 y = __builtin_amdgcn_ds_read_tr16_b64_v4i16((LAS s16x4*)(size_t)a1);
  return __builtin_bit_cast(bf16x8, __builtin_shufflevector(x, y, 0, 1, 2, 3, 4, 5, 6, 7));
}
#define SLEEP(n) __builtin_amdgcn_s_sleep(n)
#define PRIO(n) __builtin_amdgcn_s_setprio(n)
#define SCHED() __builtin_amdgcn_sched_barrier(0)
DEVI f32x16 mfma32(bf16x8 a, bf16x8 b, f32x16 c) { return __builtin_amdgcn_mfma_f32_32x32x16_bf16(a, b, c, 0, 0, 0); }
DEVI f32x4 mfma16(bf16x8 a, bf16x8 b, f32x4 c) { return __builtin_amdgcn_mfma_f32_16x16x32_bf16(a, b, c, 0, 0, 0); }

#define EX(b, i) "v_exp_f32 v[" #b "+" #i "], v[" #b "+" #i "]\n\t"
#define SM(o, vv, b, i) "v_smfmac_f32_32x32x32_bf16 " o ", " vv ", v[" #b "+" #i ":" #b "+" #i "+7], v156\n\t"
#define OA "v[96:111]"
#define OB "v[112:127]"
#define QA "v[128:131]"
#define QB "v[132:135]"
#define QK(b, k, q) "v_mfma_f32_32x32x16_bf16 v[" #b ":" #b "+15], " k ", " q ", 0\n\t"
#define WL "s_waitcnt lgkmcnt(0)\n\t"
#define RV0(Y0, VO) "ds_read_b128 " Y0 ", %[va] offset:" #VO "+1024\n\t"
#define RV1(Y1, VO) "ds_read_b128 " Y1 ", %[va] offset:" #VO "+1536\n\t"
#define ITH(A, BN, Y1, E0, R0) EX(BN, 13) EX(BN, 14) EX(BN, 15) EX(A, 0) EX(A, 1) SM(OB, Y1, BN, 8) WL E0 R0
#define ITT(A, B, AN, BN, X0, X1, KU, KL, KO, E1, E2, E3, E4, E5, R2)                                 \
  EX(A, 2) EX(A, 3) EX(A, 4) EX(A, 5) EX(A, 6) QK(AN, KU, QA) E1                                      \
  EX(A, 7) EX(A, 8) EX(A, 9) EX(A, 10) EX(A, 11) EX(A, 12) SM(OA, X0, A, 0) E2 R2                     \
  EX(A, 13) EX(A, 14) EX(A, 15) EX(B, 0) EX(B, 1) SM(OA, X1, A, 8) E3                                 \
  "ds_read_b128 " KL ", %[ka] offset:" #KO "\n\t"                                                     \
  EX(B, 2) EX(B, 3) EX(B, 4) EX(B, 5) EX(B, 6) QK(BN, KU, QB) E4                                      \
  EX(B, 7) EX(B, 8) EX(B, 9) EX(B, 10) EX(B, 11) EX(B, 12) SM(OB, X0, B, 0) E5
#define VA0 "v[140:143]"
#define VA1 "v[144:147]"
#define VB0 "v[148:151]"
#define VB1 "v[152:155]"
#define K0 "v[136:139]"
#define K1 "v[192:195]"
#define IT0(KO, E1, E2, E3, E4, E5) ITT(64, 80, 160, 176, VA0, VA1, K1, K0, KO, E1, E2, E3, E4, E5, "")
#define ITES(KO, VO, E0, E1, E2, E3, E4, E5) ITH(64, 176, VB1, E0, RV0(VB0, VO)) ITT(64, 80, 160, 176, VA0, VA1, K1, K0, KO, E1, E2, E3, E4, E5, RV1(VB1, VO))
#define ITEB(KO, VO, E0, E1, E2, E3, E4, E5) ITH(64, 176, VB1, E0, RV0(VB0, VO) RV1(VB1, VO)) ITT(64, 80, 160, 176, VA0, VA1, K1, K0, KO, E1, E2, E3, E4, E5, "")
#define ITOS(KO, VO, E0, E1, E2, E3, E4, E5) ITH(160, 80, VA1, E0, RV0(VA0, VO)) ITT(160, 176, 64, 80, VB0, VB1, K0, K1, KO, E1, E2, E3, E4, E5, RV1(VA1, VO))
#define BUMPK "v_add_u16 %[ka], 0x4000, %[ka]\n\t"
#define BUMPV "v_add_u16 %[va], %[vinc], %[va]\n\t"
#define FLAG(off) "buffer_store_dwordx2 %[tag], " off ", %[sws], 0 offen sc1\n\t"
#define SRC(base, off) "s_add_u32 s44, " base ", " #off "\n\t"
#define DST(off) "s_add_u32 m0, %[w4k], " #off "\n\t"
#define WRAP(mask) "s_and_b32 s44, s44, " #mask "\n\t"
#define STEP SRC("s44", 7168)
#define SYNC(n) "s_waitcnt vmcnt(" #n ") lgkmcnt(0)\n\ts_barrier\n\t"
#define KDMA(o) "buffer_load_dwordx4 %[dv], %[skv], s44 offen offset:" #o " sc1 lds\n\t"
#define WDMA(o) "buffer_load_dwordx4 %[wv0], %[swo], s44 offen offset:" #o " lds\n\t"
#define PRHI "s_setprio 3\n\t"
#define PRLO "s_setprio 1\n\t"

DEVI void role_a(u32 b, u32 w, u32 lane) {
  u64x8 k0 = args_request<0>();
  const u32 xcd = b & 7, idx = b >> 3, g = xcd * 4 + (idx >> 3), j = idx & 7;
  const u32 r32 = lane & 31, h = lane >> 5, th = w & 1, hp = w >> 1;
  PRIO(2);
  u32 vr0 = (lane >> 3) * 1024 + (((lane & 7) ^ (lane >> 4)) << 4);
  u32 vr1 = (lane >> 3) * 1024 + (((lane & 7) ^ (4 + (lane >> 4))) << 4) + 7168;
  u32 vt0 = (lane >> 3) * 1024 + (((lane & 7) ^ (((lane >> 4) & 1) << 2)) << 4) + j * 128;
  u32 vt1 = vt0 + 7168;
  u32 xa_[4], trb;
#pragma unroll
  for (u32 u = 0; u < 4; ++u) xa_[u] = (th * 2 + (r32 >> 4)) * 4096 + (r32 & 15) * 128 + (((2 * u + h) ^ ((r32 >> 1) & 7)) << 4);
  {
    const u32 q4 = (lane & 15) >> 2, p4 = lane & 3, blk = (lane >> 4) & 1;
    trb = q4 * 128 + (((hp * 4 + blk * 2 + (p4 >> 1)) ^ ((q4 >> 1) << 2)) << 4) + (p4 & 1) * 8;
  }
  u32 ta_ = 2048 + trb + h * 1024, tw_ = trb + h * 512;
  asm volatile("" : "+v"(xa_[0]), "+v"(xa_[1]), "+v"(xa_[2]), "+v"(xa_[3]), "+v"(vr0), "+v"(vr1), "+v"(vt0), "+v"(vt1), "+v"(ta_), "+v"(tw_));
  u32 qox = (g * 64 + w * 16) * 1024;
  asm volatile("s_waitcnt lgkmcnt(0)" : "+s"(k0), "+s"(qox));
  Args a;
  a.xa = (const u16*)k0[0]; a.wq = (const u16*)k0[1]; a.wk = (const u16*)k0[2]; a.wv = (const u16*)k0[3]; a.wo = (const u16*)k0[4];
  a.out = (float*)k0[5]; a.ws = (char*)k0[6];
  char* const ws = a.ws;
  const u32x4 sws = mk_srd(ws, WS_END), sxa = mk_srd(a.xa, 2048 * 1024), swq = mk_srd(a.wq - 1024, 512 * 1024 + 2048);
  u64 ep;
  SLEEP(4);
  const u32 rot = xcd;
  constexpr u32 QSL[8] = {16384, 32768, 49152, 0, 65536, 16384, 32768, 49152};
  auto qd = [&](u32 c) {
    const u32 kc = (c + rot) & 7;
    dmaq(QSL[c] + w * 4096, vr0, vr1, sxa, qox + kc * 128, vt0, vt1, swq, (kc * 64 + w * 16) * 1024);
  };
  qd(0);
  asm volatile("buffer_load_dwordx2 v[254:255], off, %0, 0 sc1" ::"s"(sws) : "memory", "v254", "v255");
  qd(1);
  SCHED();
  u32 l16 = lane * 16, vl = l16 + w * 4096, ka = l16, va, vinc = (~r32 & 16) << 10;
  {
    const u32 z = 192 - (((r32 - 20) >> 3) == 0 ? 64 : 0), o = (r32 & 4) << 4;
    const u32 c = 64512 + ((r32 & 27) == 16 ? o : z), v = (h * 16 + r32) * 16;
    va = r32 < 16 ? v : c;
  }
  u32 dv = vl;
  const u32 fll = lane < 16 ? lane * 8 : OOB;
  u32 tfo = (fll | (w == 0 ? 0 : OOB)) + WS_TF + (g * 8 + j) * FS, ofo = fll + WS_OF + (g * 32 + j * 4 + w) * FS, ofp = WS_OF + (g * 32 + r32) * FS;
  u32 pre = h ? WS_RF + (g * 8 + j) * FS : WS_TF + (g * 8 + j) * FS;
  pre = lane < 16 ? ofo : ((pre + (lane & 15) * 8) | (w == 0 ? 0 : OOB) | (lane < 48 ? 0 : OOB));
  u32 f0a = WS_KVF + ((xcd * 32 + 32 + lane) & 255) * FS, fwa = WS_KVP + (w * 64 + lane) * 8;
  u32 onea = 65536 + (w * 2 + h) * 2048 + ((lane >> 4) & 1) * 512 + (lane & 15) * 16, onev = (lane & 8) ? 0u : 0x3f803f80u;
  u32 xb_[4], tb_ = ta_ + 65536;
#pragma unroll
  for (u32 u = 0; u < 4; ++u) xb_[u] = xa_[u] + 65536;
  u32 ooff = WS_O + g * 65536 + ((j * 4 + hp * 2) * 2 + th) * 1024 + l16;
  u32 olds = 65536 + (hp * 4 + th) * 1024 + l16, ob_ = th * 1024 + l16, oc_ = ob_ + 65536;
  u32 vout = (g * 64 + th * 32 + h * 4) * 4096 + (j * 64 + hp * 32 + r32) * 4;
  asm volatile("" : "+v"(ka), "+v"(va), "+v"(vinc), "+v"(dv), "+v"(tfo), "+v"(ofo), "+v"(ofp), "+v"(f0a), "+v"(fwa), "+v"(onea), "+v"(onev), "+v"(pre),
               "+v"(l16), "+v"(xb_[0]), "+v"(xb_[1]), "+v"(xb_[2]), "+v"(xb_[3]), "+v"(tb_), "+v"(ooff), "+v"(olds), "+v"(ob_), "+v"(oc_), "+v"(vout));
  SCHED();
  qd(2);
  SCHED();
  f32x16 oa = {0, 0, 0, 0, 0, 0, 0, 0, 0, 0, 0, 0, 0, 0, 0, 0}, ob = oa;
  u32 sidx = 0xddddddddu;
  u32x4 sent = {SENT, SENT, SENT, SENT};
  asm volatile("" : "+{v[96:111]}"(oa), "+{v[112:127]}"(ob), "+{v156}"(sidx), "+v"(sent));
  SCHED();
  qd(3);
  PRIO(3);
  { u32 lo, hi;
    asm volatile("s_waitcnt vmcnt(12)\n\tv_readfirstlane_b32 %0, v254\n\tv_readfirstlane_b32 %1, v255\n\ts_barrier" : "=s"(lo), "=s"(hi)::"memory");
    ep = ((u64)hi << 32) | lo; }
  const u32x2 tag = mk_tag(ep);
  const u64 tag64 = ((u64)tag.y << 32) | tag.x;
  u32x2 tagv = tag;
  asm volatile("" : "+v"(tagv));
  const u32x4 skv = mk_srd(ws + WS_KV, 131072), swo = mk_srd(a.wo, 512 * 1024);
  u32 so = ((xcd + 1) & 7) * 16384;
  u32x4 qa, qb;
  {
    f32x16 qacc = {0, 0, 0, 0, 0, 0, 0, 0, 0, 0, 0, 0, 0, 0, 0, 0};
    bf16x8 fx[2][4], fw[2][4];
    auto step = [&](u32 c) {
      const u32 n = c + 4, kc = (n + rot) & 7;
#pragma unroll
      for (u32 s = 0; s < 4; ++s) {
        SCHED();
        if (c > 0) qacc = mfma32(fw[(c - 1) & 1][s], fx[(c - 1) & 1][s], qacc);
        SCHED();
        if (c < 8) {
          const u32 ta = c == 4 ? tb_ + s * 4096 : ta_ + QSL[c & 7] + s * 4096;
          fw[c & 1][s] = ldstr(ta, ta + 512);
          fx[c & 1][s] = c == 4 ? ldsr(xb_[s]) : ldsr(xa_[s] + QSL[c & 7]);
        }
        SCHED();
        if (n < 8) {
          const u32 m = QSL[n & 7] + w * 4096, ox = qox + kc * 128, ow = (kc * 64 + w * 16) * 1024;
          if (s == 0) dma1o<2048>(m, vt0, swq, ow);
          if (s == 1) dma1o<3072>(m, vt1, swq, ow);
          if (s == 2) dma1o<0>(m, vr0, sxa, ox);
          if (s == 3) dma1o<1024>(m, vr1, sxa, ox);
        }
      }
      SCHED();
    };
    step(0);
    waitvl_bar<12>(); step(1);
    waitvl_bar<12>(); step(2);
    waitvl_bar<12>(); step(3);
    st8c(u32x2{0u, 0u}, pre, sws);
    WAITV(13); BAR();
    bool got = w != 0;
    auto fload = [&]() { if (!got) asm volatile("buffer_load_dwordx2 v[254:255], %0, %1, 0 offen sc1" ::"v"(f0a), "s"(sws) : "memory", "v254", "v255"); };
    auto ftry = [&](bool last) {
      if (!got) {
        u64 bad;
        asm volatile("s_waitcnt vmcnt(0)\n\tv_cmp_ne_u64 %0, %1, v[254:255]\n\ts_nop 1" : "=s"(bad) : "s"(tag64) : "memory");
        got = bad == 0;
        if (last && !got) { poll<0>(f0a, tag, sws); got = true; }
        if (got) {
          dma4c(0, l16, skv, so); dma4c(4096, l16, skv, so + 4096);
          st8c(tagv, tfo + (WS_RF - WS_TF), sws);
          dma4c(8192, l16, skv, so + 8192); dma4c(12288, l16, skv, so + 12288);
        } else fload();
      }
    };
    step(4);
    WAITV(9); BAR();
    step(5);
    WAITV(5); BAR();
    fload();
    *(LAS u32x4*)(size_t)onea = u32x4{onev, onev, onev, onev};
    step(6);
    WAITV(1); BAR();
    step(7);
    ftry(false);
    step(8);
    asm volatile("s_nop 7\n\ts_nop 3\n\t"
                 "v_mul_f32 v64, %3, v64\n\tv_mul_f32 v65, %3, v65\n\tv_mul_f32 v66, %3, v66\n\tv_mul_f32 v67, %3, v67\n\t"
                 "v_mul_f32 v68, %3, v68\n\tv_mul_f32 v69, %3, v69\n\tv_mul_f32 v70, %3, v70\n\tv_mul_f32 v71, %3, v71\n\t"
                 "v_mul_f32 v72, %3, v72\n\tv_mul_f32 v73, %3, v73\n\tv_mul_f32 v74, %3, v74\n\tv_mul_f32 v75, %3, v75\n\t"
                 "v_mul_f32 v76, %3, v76\n\tv_mul_f32 v77, %3, v77\n\tv_mul_f32 v78, %3, v78\n\tv_mul_f32 v79, %3, v79\n\t"
                 "v_cvt_pk_bf16_f32 v128, v64, v65\n\tv_cvt_pk_bf16_f32 v129, v66, v67\n\tv_cvt_pk_bf16_f32 v130, v68, v69\n\tv_cvt_pk_bf16_f32 v131, v70, v71\n\t"
                 "v_cvt_pk_bf16_f32 v132, v72, v73\n\tv_cvt_pk_bf16_f32 v133, v74, v75\n\tv_cvt_pk_bf16_f32 v134, v76, v77\n\tv_cvt_pk_bf16_f32 v135, v78, v79\n\t"
                 : "={v[128:131]}"(qa), "={v[132:135]}"(qb), "+{v[64:79]}"(qacc) : "s"(0.25f * 1.44269504088896340736f));
    ftry(true);
  }
  const u32 wso = (j * 65536 + w * 32768) & 0x7ffff;
  {
    const u32 w4k = w * 4096;
    asm volatile(
        "s_add_u32 m0, %[w4k], 16384\n\ts_add_u32 s44, %[so], 16384\n\ts_and_b32 s44, s44, 0x1ffff\n\t"
        "s_branch 7f\n\t"
        "4:\n\t"
        "s_mov_b32 s59, 0x100000\n\t"
        "6:\n\t"
        "buffer_load_dwordx2 v[158:159], %[fwa], %[sws], 0 offen sc1\n\ts_sub_u32 s59, s59, 1\n\ts_cbranch_scc1 5f\n\t"
        "s_waitcnt vmcnt(0)\n\tv_cmp_ne_u64 vcc, %[tag64], v[158:159]\n\ts_nop 4\n\ts_cbranch_vccnz 6b\n\ts_branch 5f\n\t"
        "7:\n\t"
        "s_waitcnt vmcnt(9) lgkmcnt(0)\n\ts_barrier\n\t"
        "ds_read_b128 " K0 ", %[ka]\n\tds_read_b128 " K1 ", %[ka] offset:2048\n\t" RV0(VA0, 0) RV1(VA1, 0)
        KDMA(0) "s_waitcnt lgkmcnt(3)\n\t" QK(64, K0, QA) KDMA(1024) QK(80, K0, QB) RV0(VB0, 2048) RV1(VB1, 2048)
        "s_waitcnt lgkmcnt(2)\n\ts_nop 6\n\t" EX(64, 0) EX(64, 1)
        IT0(4096, KDMA(2048), "", "", "", KDMA(3072))
        ITOS(6144, 4096, "", "buffer_load_dwordx2 v[158:159], %[fwa], %[sws], 0 offen sc1\n\t", PRLO, "", "", "")
        ITES(8192, 6144, "", "", "s_waitcnt vmcnt(5)\n\ts_barrier\n\t", "", "", "")
        ITOS(10240, 8192, "", "", "", "", "", "")
        ITES(12288, 10240, "", "", "", "", "", "")
        ITOS(14336, 12288, "", "", "", "", "", "")
        ITEB(0, 14336, "", BUMPK, SRC("%[so]", 32768) "s_waitcnt vmcnt(0)\n\tv_cmp_ne_u64 vcc, %[tag64], v[158:159]\n\t", DST(32768) "s_cbranch_vccnz 4b\n\t5:\n\t" SYNC(0), WRAP(0x1ffff) BUMPV, PRHI KDMA(0))
        ITOS(2048, 0, PRLO, PRHI KDMA(1024), PRLO, "", "", PRHI KDMA(2048))
        ITES(4096, 2048, PRLO, PRHI KDMA(3072), PRLO SRC("%[so]", 49152), DST(49152), WRAP(0x1ffff), PRHI KDMA(0))
        ITOS(6144, 4096, PRLO, PRHI KDMA(1024), PRLO, "", "", PRHI KDMA(2048))
        ITES(8192, 6144, PRLO, PRHI KDMA(3072), PRLO SRC("%[so]", 65536), DST(0), WRAP(0x1ffff), PRHI KDMA(0))
        ITOS(10240, 8192, PRLO, PRHI KDMA(1024), PRLO, "", "", PRHI KDMA(2048))
        ITES(12288, 10240, PRLO, PRHI KDMA(3072), PRLO, "", "", "")
        ITOS(14336, 12288, "", "", "", "", "", "")
        ITEB(0, 14336, "", BUMPK, SRC("%[so]", 81920), DST(16384) SYNC(8), WRAP(0x1ffff) BUMPV, PRHI KDMA(0))
        ITOS(2048, 0, PRLO, "", "", "", "", PRHI KDMA(1024))
        ITES(4096, 2048, PRLO, "", "", "", "", PRHI KDMA(2048))
        ITOS(6144, 4096, PRLO, "", "", "", "", PRHI KDMA(3072))
        ITES(8192, 6144, PRLO, "", "", "", "", "")
        ITOS(10240, 8192, "", "", "", "", "", "")
        ITES(12288, 10240, "", "", "", "", "", "")
        ITOS(14336, 12288, "", "", "", "", "", "")
        ITEB(0, 14336, "", BUMPK, SRC("%[so]", 98304), DST(32768) SYNC(4), WRAP(0x1ffff) BUMPV, PRHI KDMA(0))
        ITOS(2048, 0, PRLO, "", "", "", "", PRHI KDMA(1024))
        ITES(4096, 2048, PRLO, "", "", "", "", PRHI KDMA(2048))
        ITOS(6144, 4096, PRLO, "", "", "", "", PRHI KDMA(3072))
        ITES(8192, 6144, PRLO, "", "", "", "", "")
        ITOS(10240, 8192, "", "", "", "", "", "")
        ITES(12288, 10240, "", "", "", "", "", PRHI "buffer_store_dwordx4 %[sent], %[ooff], %[sws], 0 offen sc1\n\t")
        ITOS(14336, 12288, PRLO, "", "", "", "", PRHI "buffer_store_dwordx4 %[sent], %[ooff], %[sws], 0 offen offset:2048 sc1\n\t")
        ITES(0, 14336, PRLO, BUMPK, "", "", BUMPV, "")
        ITOS(2048, 0, "", "", "", "", "", "")
        ITES(4096, 2048, "", "", "", "", "", "")
        ITOS(6144, 4096, "", "", "", "", "", PRHI "buffer_load_dwordx2 v[158:159], %[ofq], %[sws], 0 offen sc1\n\t")
        ITES(8192, 6144, PRLO, "", "", "", "", "")
        ITOS(10240, 8192, "", "", "", "", "", "")
        ITES(12288, 10240, "", "", "", "", "", "")
        ITOS(14336, 12288, "", "", "", "", "", "")
        ITEB(0, 14336, "", BUMPK, SRC("%[so]", 114688), DST(49152) SYNC(0), WRAP(0x1ffff) BUMPV, PRHI KDMA(0))
        ITOS(2048, 0, PRLO, "", "", "", "", PRHI KDMA(1024))
        ITES(4096, 2048, PRLO, "", "", "", "", PRHI KDMA(2048))
        ITOS(6144, 4096, PRLO, "", "", "", "", PRHI KDMA(3072))
        ITES(8192, 6144, PRLO, "", "", "", "", "")
        ITOS(10240, 8192, "", "", "", "", "", "")
        ITES(12288, 10240, "", "", SRC("%[wso]", 0), DST(0), WRAP(0x7ffff), PRHI WDMA(0))
        ITOS(14336, 12288, PRLO, STEP, "", "", "", "")
        ITES(0, 14336, "", BUMPK, "", "", BUMPV, PRHI WDMA(1024))
        ITOS(2048, 0, PRLO, STEP, "", "", "", "")
        ITES(4096, 2048, "", "", "", "", "", PRHI WDMA(2048))
        ITOS(6144, 4096, PRLO, STEP, "", "", "", "")
        ITES(8192, 6144, "", "", "", "", "", PRHI WDMA(3072))
        ITOS(10240, 8192, PRLO, "", "", "", "", "")
        ITES(12288, 10240, "", "", "", "", "", "")
        ITOS(14336, 12288, "", "", "", "", "", "")
        ITEB(0, 14336, "", BUMPK, SRC("%[wso]", 131072), DST(16384) SYNC(4), WRAP(0x7ffff) BUMPV, PRHI WDMA(0))
        ITOS(2048, 0, PRLO, STEP, "", "", "", PRHI WDMA(1024))
        ITES(4096, 2048, PRLO, STEP, "", "", "", PRHI WDMA(2048))
        ITOS(6144, 4096, PRLO, STEP, "", "", "", PRHI WDMA(3072))
        ITES(8192, 6144, PRLO, "", SRC("%[wso]", 262144), DST(32768), WRAP(0x7ffff), PRHI WDMA(0))
        ITOS(10240, 8192, PRLO, STEP, "", "", "", PRHI WDMA(1024))
        ITES(12288, 10240, PRLO, STEP, "", "", "", PRHI WDMA(2048))
        ITOS(14336, 12288, PRLO, STEP FLAG("%[tfo]"), "", "", "", PRHI WDMA(3072))
        ITEB(0, 14336, PRLO, "", SRC("%[wso]", 393216), DST(49152) SYNC(63), WRAP(0x7ffff), PRHI WDMA(0))
        ITH(160, 80, VA1, STEP, WDMA(1024))
        EX(160, 2) EX(160, 3) EX(160, 4) EX(160, 5) EX(160, 6) EX(160, 7) EX(160, 8) EX(160, 9) SM(OA, VB0, 160, 0) STEP WDMA(2048)
        EX(160, 10) EX(160, 11) EX(160, 12) EX(160, 13) EX(160, 14) EX(160, 15) EX(176, 0) EX(176, 1) SM(OA, VB1, 160, 8) STEP WDMA(3072)
        EX(176, 2) EX(176, 3) EX(176, 4) EX(176, 5) EX(176, 6) EX(176, 7) EX(176, 8) EX(176, 9) EX(176, 10) EX(176, 11) EX(176, 12) SM(OB, VB0, 176, 0) FLAG("%[ofq]")
        EX(176, 13) EX(176, 14) EX(176, 15)
        "s_nop 2\n\t"
        SM(OB, VB1, 176, 8)
        : "+{v[96:111]}"(oa), "+{v[112:127]}"(ob), [ka] "+v"(ka), [va] "+v"(va)
        : "{v[128:131]}"(qa), "{v[132:135]}"(qb), "{v156}"(sidx), [vinc] "v"(vinc), [wv0] "v"(vt0), [dv] "v"(dv), [so] "s"(so), [wso] "s"(wso),
          [w4k] "s"(w4k), [tag] "v"(tagv), [tag64] "s"(tag64), [tfo] "v"(tfo), [fwa] "v"(fwa), [sws] "s"(sws),
          [skv] "s"(skv), [swo] "s"(swo), [sent] "v"(sent), [ofq] "v"(ofo), [ooff] "v"(ooff)
        : "memory", "scc", "vcc", "s44", "s59",
          "v64", "v65", "v66", "v67", "v68", "v69", "v70", "v71", "v72", "v73", "v74", "v75", "v76", "v77", "v78", "v79",
          "v80", "v81", "v82", "v83", "v84", "v85", "v86", "v87", "v88", "v89", "v90", "v91", "v92", "v93", "v94", "v95",
          "v136", "v137", "v138", "v139", "v140", "v141", "v142", "v143", "v144", "v145", "v146", "v147", "v148", "v149", "v150", "v151",
          "v152", "v153", "v154", "v155", "v158", "v159", "v160", "v161", "v162", "v163", "v164", "v165", "v166", "v167", "v168", "v169",
          "v170", "v171", "v172", "v173", "v174", "v175", "v176", "v177", "v178", "v179", "v180", "v181", "v182", "v183", "v184", "v185",
          "v186", "v187", "v188", "v189", "v190", "v191", "v192", "v193", "v194", "v195");
  }
  u64 unsafe;
  asm volatile("v_rcp_f32 v72, v104\n\tv_maximum3_f32 v73, |v96|, |v97|, v104\n\tv_mul_f32 v64, v96, v72\n\tv_mul_f32 v65, v97, v72\n\t"
               "v_mul_f32 v66, v98, v72\n\tv_mul_f32 v67, v99, v72\n\tv_mul_f32 v68, v100, v72\n\tv_mul_f32 v69, v101, v72\n\t"
               "v_mul_f32 v70, v102, v72\n\tv_mul_f32 v71, v103, v72\n\tv_maximum3_f32 v73, |v98|, |v99|, v73\n\tv_maximum3_f32 v73, |v100|, |v101|, v73\n\t"
               "v_maximum3_f32 v73, |v102|, |v103|, v73\n\tv_cvt_pk_bf16_f32 v76, v64, v65\n\tv_cvt_pk_bf16_f32 v77, v66, v67\n\tv_cvt_pk_bf16_f32 v78, v68, v69\n\t"
               "v_cvt_pk_bf16_f32 v79, v70, v71\n\tv_min_u32 v76, -2, v76\n\tv_min_u32 v77, -2, v77\n\tv_min_u32 v78, -2, v78\n\t"
               "v_min_u32 v79, -2, v79\n\t"
               "v_rcp_f32 v72, v120\n\tv_maximum3_f32 v73, |v112|, |v113|, v73\n\tv_mul_f32 v64, v112, v72\n\tv_mul_f32 v65, v113, v72\n\t"
               "v_mul_f32 v66, v114, v72\n\tv_mul_f32 v67, v115, v72\n\tv_mul_f32 v68, v116, v72\n\tv_mul_f32 v69, v117, v72\n\t"
               "v_mul_f32 v70, v118, v72\n\tv_mul_f32 v71, v119, v72\n\tv_maximum3_f32 v73, |v114|, |v115|, v73\n\tv_maximum3_f32 v73, |v116|, |v117|, v73\n\t"
               "v_maximum3_f32 v73, |v118|, |v119|, v73\n\tv_maximum3_f32 v73, v73, v73, v120\n\tv_min_f32 v74, v104, v120\n\tv_cmp_nge_f32 vcc, %3, v73\n\t"
               "v_cmp_nle_f32 %0, %4, v74\n\tv_cvt_pk_bf16_f32 v80, v64, v65\n\tv_cvt_pk_bf16_f32 v81, v66, v67\n\tv_cvt_pk_bf16_f32 v82, v68, v69\n\t"
               "v_cvt_pk_bf16_f32 v83, v70, v71\n\tv_min_u32 v80, -2, v80\n\tv_min_u32 v81, -2, v81\n\tv_min_u32 v82, -2, v82\n\t"
               "v_min_u32 v83, -2, v83\n\t"
               "s_or_b64 %0, %0, vcc\n\ts_cbranch_scc1 1f\n\t"
               "buffer_store_dwordx4 v[80:83], %5, %7, 0 offen offset:2048 sc1\n\tbuffer_store_dwordx4 v[76:79], %5, %7, 0 offen sc1\n\t"
               "ds_write_b128 %6, v[76:79]\n\tds_write_b128 %6, v[80:83] offset:2048\n\t"
               "1:"
               : "=&s"(unsafe) : "{v[96:111]}"(oa), "{v[112:127]}"(ob), "s"(0x1p100f), "s"(0x1p-64f), "v"(ooff), "v"(olds), "s"(sws)
               : "memory", "scc", "vcc", "v64", "v65", "v66", "v67", "v68", "v69", "v70", "v71", "v72", "v73", "v74",
                 "v76", "v77", "v78", "v79", "v80", "v81", "v82", "v83");
  if (__builtin_expect(unsafe != 0, 0)) {
    const __amdgpu_buffer_rsrc_t rk = __builtin_amdgcn_make_buffer_rsrc(ws + WS_KV, 0, 131072, 0x20000);
    const u32x4 ones = {0x3f803f80u, 0x3f803f80u, 0x3f803f80u, 0x3f803f80u};
#pragma unroll 1
    for (int t = 0; t < 2; ++t) {
      const bf16x8 q = __builtin_bit_cast(bf16x8, t ? qb : qa);
      f32x16 z = {0, 0, 0, 0, 0, 0, 0, 0, 0, 0, 0, 0, 0, 0, 0, 0};
      float m = -__builtin_inff();
#pragma unroll 1
      for (u32 kt = 0; kt < 64; ++kt) {
        u32x4 kf = __builtin_amdgcn_raw_buffer_load_b128(rk, lane * 16, kt * 2048, 16);
        f32x16 s = mfma32(__builtin_bit_cast(bf16x8, kf), q, z);
#pragma unroll
        for (int i = 0; i < 16; ++i) m = __builtin_fmaxf(m, s[i]);
      }
      m = __builtin_fmaxf(m, __shfl_xor(m, 32));
      f32x16 o = z;
#pragma unroll 1
      for (u32 kt = 0; kt < 64; ++kt) {
        u32x4 kf = __builtin_amdgcn_raw_buffer_load_b128(rk, lane * 16, kt * 2048, 16);
        u32x4 v0 = __builtin_amdgcn_raw_buffer_load_b128(rk, (h * 16 + (r32 & 15)) * 16 + 1024, kt * 2048, 16);
        u32x4 v1 = __builtin_amdgcn_raw_buffer_load_b128(rk, (h * 16 + (r32 & 15)) * 16 + 1536, kt * 2048, 16);
        if (r32 >= 16) { v0 = ones; v1 = ones; }
        f32x16 s = mfma32(__builtin_bit_cast(bf16x8, kf), q, z);
        f32x8 p0, p1;
#pragma unroll
        for (int i = 0; i < 8; ++i) { p0[i] = __builtin_amdgcn_exp2f(s[i] - m); p1[i] = __builtin_amdgcn_exp2f(s[8 + i] - m); }
        o = __builtin_amdgcn_smfmac_f32_32x32x32_bf16(__builtin_bit_cast(bf16x8, v0), __builtin_bit_cast(bf16x16, p0), o, (int)0xdddddddd, 0, 0);
        o = __builtin_amdgcn_smfmac_f32_32x32x32_bf16(__builtin_bit_cast(bf16x8, v1), __builtin_bit_cast(bf16x16, p1), o, (int)0xdddddddd, 0, 0);
      }
      const float inv = __builtin_amdgcn_rcpf(o[8]);
      u32x4 p;
#pragma unroll
      for (int i = 0; i < 4; ++i) { const u32 x = pk2(o[2 * i] * inv, o[2 * i + 1] * inv); p[i] = x < SENT ? x : SENT - 1; }
      st16s(p, ooff, sws, t * 2048);
      *(LAS u32x4*)(size_t)(olds + t * 2048) = p;
    }
  }
  f32x16 acc;
  {
    bf16x8 wf[32], fo[32];
    auto wread = [&](u32 i) { wf[i] = ldstr(tw_ + i * 2048, tw_ + i * 2048 + 1024); };
    auto odma = [&](u32 c) { dma2c((c < 7 ? (c - 1) * 8192 : 73728) + w * 2048, l16, sws, WS_O + g * 65536 + ((j + c) & 7) * 8192 + w * 2048); };
    auto ord = [&](u32 i) { fo[i] = ldsr(i < 4 ? oc_ + i * 2048 : i < 28 ? ob_ + (i - 4) * 2048 : oc_ + (i - 24) * 2048); };
    const f32x16 z16 = {0, 0, 0, 0, 0, 0, 0, 0, 0, 0, 0, 0, 0, 0, 0, 0};
    auto omul = [&](u32 i) { acc = mfma32(fo[i], wf[i], i ? acc : z16); };
    u32 mx = 0;
    waitvl_bar<7>();
#pragma unroll
    for (u32 i = 0; i < 4; ++i) ord(i);
#pragma unroll
    for (u32 i = 0; i < 24; ++i) {
      SCHED();
      wread(i);
      if (i >= 8 && i < 12) { SCHED(); omul(i - 8); }
    }
    SCHED();
    if (w == 0) poll<0>(ofp, tag, sws);
    waitvl_bar<63>();
    odma(1); odma(2); odma(3); odma(4); odma(5); odma(6);
    WAITV(10); BAR();
    ord(4); ord(5); ord(6); ord(7);
    odma(7);
    WAITV(10); BAR();
    ord(8); ord(9); ord(10); ord(11);
#pragma unroll
    for (u32 i = 4; i < 32; ++i) {
      SCHED();
      omul(i);
      SCHED();
      if (i == 6) { WAITV(8); BAR(); }
      if (i == 10) { WAITV(6); BAR(); }
      if (i == 14) { WAITV(4); BAR(); }
      if (i == 18) { WAITV(2); BAR(); }
      if (i == 22) { WAITV(0); BAR(); }
      if ((i & 2) && i < 24) { ord((i & ~3u) + 8 + (i & 1) * 2); ord((i & ~3u) + 9 + (i & 1) * 2); }
      if (!(i & 2) && i < 20) wread(24 + ((i - 4) >> 2) * 2 + (i & 1));
      const u32x4 f = __builtin_bit_cast(u32x4, fo[i]);
      asm("v_max3_u32 %0, %0, %1, %2\n\tv_max3_u32 %0, %0, %3, %4" : "+v"(mx) : "v"(f.x), "v"(f.y), "v"(f.z), "v"(f.w));
    }
    SCHED();
    if (__builtin_expect(__builtin_amdgcn_ballot_w64(mx == SENT) != 0, 0)) {
#pragma unroll
      for (int i = 0; i < 16; ++i) acc[i] = 0.f;
#pragma unroll
      for (u32 i = 0; i < 32; ++i) {
        u32x4 f;
#pragma unroll 1
        for (u32 it = 0; it < (1u << 18); ++it) {
          asm volatile("buffer_load_dwordx4 %0, %1, %2, %3 offen sc1\n\ts_waitcnt vmcnt(0)" : "=&v"(f) : "v"(ob_), "s"(sws), "s"(WS_O + g * 65536 + ((j * 4 + i) & 31) * 2048) : "memory");
          if (__builtin_amdgcn_ballot_w64(umax3(umax3(0, f.x, f.y), f.z, f.w) == SENT) == 0) break;
        }
        acc = mfma32(__builtin_bit_cast(bf16x8, f), wf[i], acc);
      }
    }
  }
  {
    const __amdgpu_buffer_rsrc_t ro = __builtin_amdgcn_make_buffer_rsrc(a.out, 0, 8388608, 0x20000);
#pragma unroll
    for (int i = 0; i < 16; ++i) { const float x = acc[i]; __builtin_amdgcn_raw_buffer_store_b32(__builtin_bit_cast(u32, x), ro, vout, ((i & 3) + 8 * (i >> 2)) * 4096, WT); SCHED(); }
  }
}

DEVI void cmul(float& r, float& i, float ar, float ai, float br, float bi) {
  asm("v_mul_f32 %0, %3, %5\n\tv_mul_f32 %1, %3, %4\n\tv_fma_f32 %0, %2, %4, -%0\n\tv_fma_f32 %1, %2, %5, %1"
      : "=&v"(r), "=&v"(i) : "v"(ar), "v"(ai), "v"(br), "v"(bi));
}
DEVI f32x2 fma2(f32x2 a, f32x2 b, f32x2 c) { return __builtin_elementwise_fma(a, b, c); }
DEVI void cstep2(f32x2& r, f32x2& i, float ar, float ai, f32x2 ur, f32x2 ui) {
  const f32x2 a = {ar, ar}, b = {ai, ai};
  const f32x2 nr = fma2(r, a, fma2(-i, b, ur)), ni = fma2(i, a, fma2(r, b, ui));
  r = nr; i = ni;
}

DEVI f32x2 cst(f32x2 c, float ar, f32x2 m, f32x2 u) { return fma2(c, f32x2{ar, ar}, fma2(__builtin_shufflevector(c, c, 1, 0), m, u)); }

DEVI void role_b(u32 b, u32 w, u32 lane) {
  u64x8 k0 = args_request<0>(), k1 = args_request<64>();
  const u32 xcd = b & 7, idx = b >> 3, g = xcd * 4 + (idx >> 3), j = idx & 7;
  const u32 c16 = lane & 15, g4 = lane >> 4, r32 = lane & 31, h = lane >> 5, th = w & 1, pt = w >> 1;
  PRIO(3);
  u32 l16 = lane * 16, xs_[4];
#pragma unroll
  for (u32 u = 0; u < 4; ++u) xs_[u] = (4 * u + g4) * 1024 + ((c16 ^ (4 * u + g4)) << 4) - u * 1024;
  u32 vo = (j * 64 + w * 16 + c16) * 2, vd = (j * 64 + pt * 32 + r32) * 2;
  u32 pre = h ? (WS_KVF + (g * 8 + j) * FS) | (w == 0 ? 0 : OOB) : WS_EF + ((j * 4 + w) * 32 + g) * FS;
  pre = (g4 == 1 ? WS_HF + (g * 32 + j * 4 + w) * FS : pre) + c16 * 8;
  pre = g4 == 3 ? OOB : pre;
  u32x2 zero2 = {0, 0};
  asm volatile("" : "+v"(l16), "+v"(xs_[0]), "+v"(xs_[1]), "+v"(xs_[2]), "+v"(xs_[3]), "+v"(vo), "+v"(vd), "+v"(pre), "+v"(zero2));
  u32 kox = (g * 64 + (j >> 1) * 16) * 1024 + w * 256;
  asm volatile("s_waitcnt lgkmcnt(0)" : "+s"(k0), "+s"(k1), "+s"(kox));
  Args a;
  a.out = (float*)k0[5]; a.ws = (char*)k0[6]; a.xb = (const u16*)k0[7];
  a.lre = (const u16*)k1[0]; a.lim = (const u16*)k1[1]; a.bre = (const u16*)k1[2]; a.bim = (const u16*)k1[3];
  a.cre = (const u16*)k1[4]; a.cim = (const u16*)k1[5]; a.ldt = (const u16*)k1[6]; a.dsk = (const u16*)k1[7];
  char* const ws = a.ws;
  const u32x4 sws = mk_srd(ws, WS_END), sxb = mk_srd(a.xb, 2048 * 1024);
  dma4v(w * 4096, xs_[0], xs_[1], xs_[2], xs_[3], mk_srd((const void*)k0[0], 2048 * 1024), kox);
  dma4(65536 + w * 4096, l16, mk_srd((const void*)((j & 1) ? k0[3] : k0[2]), 512 * 32), w * 4096);
  asm volatile("buffer_load_dwordx2 v[252:253], off, %0, 0 sc1" ::"s"(sws) : "memory", "v252", "v253");
  st8c(zero2, pre, sws);
  u32 plr, pli, pdt, pdk;
  {
    const u32x4 s0 = mk_srd(a.lre, 1024), s1 = mk_srd(a.lim, 1024), s2 = mk_srd(a.ldt, 1024), s3 = mk_srd(a.dsk, 1024);
    asm volatile("buffer_load_ushort %0, %8, %4, 0 offen\n\tbuffer_load_ushort %1, %8, %5, 0 offen\n\t"
                 "buffer_load_ushort %2, %8, %6, 0 offen\n\tbuffer_load_ushort %3, %9, %7, 0 offen"
                 : "=&v"(plr), "=&v"(pli), "=&v"(pdt), "=&v"(pdk) : "s"(s0), "s"(s1), "s"(s2), "s"(s3), "v"(vo), "v"(vd) : "memory");
  }
  SCHED();
  u32 fll = lane < 16 ? lane * 8 : OOB, xk_[4], red = 65536 + l16;
#pragma unroll
  for (u32 u = 0; u < 4; ++u) xk_[u] = w * 4096 + c16 * 256 + (((4 * u + g4) ^ c16) << 4);
  u32 kvrd = 65536 + w * 4096 + (g4 * 8 + (c16 >> 2)) * 32 + (lane & 3) * 8, kvoff;
  {
    const u32 tq = j >> 1;
    const u32 ov = 1024 + (tq & 1) * 512 + ((g4 >> 1) * 16 + c16) * 16 + (g4 & 1) * 8;
    const u32 ok = ((g4 & 1) * 32 + (tq & 1) * 16 + c16) * 16 + (g4 >> 1) * 8;
    const u32 mv = 0u - (j & 1);
    kvoff = (WS_KV + (g * 2 + (tq >> 1)) * 2048 + ((ov & mv) | (ok & ~mv)));
  }
  u32 kvfo = lane == 16 ? WS_KVP + (g * 8 + j) * 8 : fll + WS_KVF + (g * 8 + j) * FS;
  u32 vr0 = (lane >> 3) * 1024 + (((lane & 7) ^ (lane >> 4)) << 4);
  u32 vr1 = (lane >> 3) * 1024 + (((lane & 7) ^ (4 + (lane >> 4))) << 4) + 7168;
  const u32 xrow = (32 * h + 8 * ((lane >> 3) & 1) + ((lane >> 4) & 1)) * 1024;
  u32 vx0 = xrow + (((lane & 7) ^ (lane >> 4)) << 4);
  u32 vx1 = xrow + (((lane & 7) ^ (4 + (lane >> 4))) << 4) + 1024;
  u32 ra[4], rb[4];
#pragma unroll
  for (u32 s = 0; s < 4; ++s) { ra[s] = r32 * 128 + (((2 * s + h) ^ ((r32 >> 1) & 7)) << 4); rb[s] = ra[s] + 8192 + w * 4096; }
  asm volatile("" : "+v"(fll), "+v"(kvfo), "+v"(xk_[0]), "+v"(xk_[1]), "+v"(xk_[2]), "+v"(xk_[3]), "+v"(red), "+v"(kvrd), "+v"(kvoff),
               "+v"(vr0), "+v"(vr1), "+v"(vx0), "+v"(vx1), "+v"(ra[0]), "+v"(ra[1]), "+v"(ra[2]), "+v"(ra[3]), "+v"(rb[0]), "+v"(rb[1]), "+v"(rb[2]), "+v"(rb[3]));
  SCHED();
  const u32 stg = 65536 + w * 4096;
  u32 rc0[4], rc1[4], rz0[4], rz1[4], tb0, tb1, rxw, rxr, vout, xw, xr_, eoff, efo, efp, hfo, hfp, rfa, tfa, vq, hw0, hw1, evo[8], wrm;
  f32x16 ur = {0, 0, 0, 0, 0, 0, 0, 0, 0, 0, 0, 0, 0, 0, 0, 0}, ui = ur;
  float ar, ai, gr, gi, a8r, a16r, a64r, akr, apr;
  f32x2 m8, m16, m64, mk, mp;
  u32x4 dg0, dg1;
  u32x2 tag, tagv;
  {
    const u32x4 sbr = mk_srd(a.bre, 512 * 1024), sbi = mk_srd(a.bim, 512 * 1024);
    const u32 rot = (xcd + j) & 7;
    auto bdx = [&](u32 c) { dma2((c % 3) * 24576 + w * 2048, vx0, vx1, sxb, (g * 64 + pt * 16 + th * 4) * 1024 + ((c + rot) & 7) * 128); };
    auto bdr = [&](u32 c) { dma2((c % 3) * 24576 + 8192 + w * 4096, vr0, vr1, sbr, (j * 64 + w * 16) * 1024 + ((c + rot) & 7) * 128); };
    auto bdi = [&](u32 c) { dma2((c % 3) * 24576 + 10240 + w * 4096, vr0, vr1, sbi, (j * 64 + w * 16) * 1024 + ((c + rot) & 7) * 128); };
    auto bd1 = [&](u32 c, u32 t, u32 u) {
      const u32 m = (c % 3) * 24576 + (t == 0 ? w * 2048 : 6144 + t * 2048 + w * 4096);
      const u32 so = (t == 0 ? g * 64 + pt * 16 + th * 4 : j * 64 + w * 16) * 1024 + ((c + rot) & 7) * 128;
      if (u) dma1o<1024>(m, t ? vr1 : vx1, t == 0 ? sxb : t == 1 ? sbr : sbi, so); else dma1o<0>(m, t ? vr0 : vx0, t == 0 ? sxb : t == 1 ? sbr : sbi, so);
    };
    WAITV(6);
    {
      bf16x8 wf[4], xf[4];
#pragma unroll
      for (int i = 0; i < 4; ++i) { xf[i] = ldsr(xk_[i]); wf[i] = ldstr(kvrd + i * 1024, kvrd + i * 1024 + 128); }
      f32x4 acc = {0, 0, 0, 0};
      if (j & 1) {
#pragma unroll
        for (int i = 0; i < 4; ++i) acc = mfma16(xf[i], wf[i], acc);
      } else {
#pragma unroll
        for (int i = 0; i < 4; ++i) acc = mfma16(wf[i], xf[i], acc);
      }
      *(LAS f32x4*)(size_t)(red + w * 4096) = acc;
      waitvl_bar<63>();
      if (w == 0) {
        f32x4 s0 = *(LAS f32x4*)(size_t)(red), s1 = *(LAS f32x4*)(size_t)(red + 4096), s2 = *(LAS f32x4*)(size_t)(red + 8192), s3 = *(LAS f32x4*)(size_t)(red + 12288);
#pragma unroll
        for (int i = 0; i < 4; ++i) s0[i] = sadd(sadd(s0[i], s1[i]), sadd(s2[i], s3[i]));
        st8c(u32x2{pk2(s0[0], s0[1]), pk2(s0[2], s0[3])}, kvoff, sws);
      }
    }
    SCHED();
    {
      const u32 q4 = c16 >> 2, p4 = lane & 3, m = (g4 & 1) * 2 + (p4 >> 1);
      tb0 = stg + m * 256 + (((8 * h + q4) ^ (m << 2)) << 4) + (p4 & 1) * 8;
      tb1 = tb0 ^ 64;
#pragma unroll
      for (u32 s = 0; s < 4; ++s) { rz0[s] = ra[s] + pt * 12288; rz1[s] = rz0[s] ^ 4096; rc0[s] = rz0[s] + 16384; rc1[s] = rz1[s] + 16384; }
      rxw = 49152 + w * 4096 + l16; rxr = rxw ^ 8192;
      vout = (g * 64 + th * 32 + h * 4) * 4096 + 2048 + (j * 64 + pt * 32 + r32) * 4;
      xw = stg + lane * 8; xr_ = stg + c16 * 8;
      eoff = g4 == 3 ? WS_E + ((j * 4 + w) * 32 + g) * 128 + c16 * 8 : OOB;
      efo = fll + WS_EF + ((j * 4 + w) * 32 + g) * FS;
      efp = WS_EF + ((j * 4 + w) * 32 + (r32 < g ? r32 : g)) * FS;
      hfo = fll + WS_HF + (g * 32 + j * 4 + w) * FS;
      hfp = WS_HF + (g * 32 + r32) * FS;
      rfa = WS_RF + (g * 8 + j) * FS; tfa = WS_TF + (g * 8 + j) * FS;
      vq = (g * 64 + th * 32 + r32) * 1024 + j * 128 + pt * 64 + h * 16;
      wrm = lane < 16 ? l16 * 8 : OOB;
      const u32 m0_ = (g4 & 1) * 2;
      hw0 = stg + h * 2048 + m0_ * 256 + ((c16 ^ (m0_ << 2)) << 4);
      hw1 = stg + h * 2048 + (m0_ + 1) * 256 + ((c16 ^ ((m0_ + 1) << 2)) << 4);
      const int gb = (int)g - 8 - 8 * (int)g4;
#pragma unroll
      for (int i = 0; i < 8; ++i) evo[i] = gb + i >= 0 ? (u32)(gb + i) * 128 + c16 * 8 : OOB;
      asm volatile("" : "+v"(rc0[0]), "+v"(rc0[1]), "+v"(rc0[2]), "+v"(rc0[3]), "+v"(rc1[0]), "+v"(rc1[1]), "+v"(rc1[2]), "+v"(rc1[3]),
                   "+v"(tb0), "+v"(tb1), "+v"(rxw), "+v"(rxr), "+v"(vout), "+v"(xw), "+v"(xr_), "+v"(eoff), "+v"(efo), "+v"(efp), "+v"(hfo), "+v"(hfp));
      asm volatile("" : "+v"(rfa), "+v"(tfa), "+v"(vq), "+v"(hw0), "+v"(hw1), "+v"(wrm), "+v"(evo[0]), "+v"(evo[1]), "+v"(evo[2]), "+v"(evo[3]),
                   "+v"(evo[4]), "+v"(evo[5]), "+v"(evo[6]), "+v"(evo[7]),
                   "+v"(rz0[0]), "+v"(rz0[1]), "+v"(rz0[2]), "+v"(rz0[3]), "+v"(rz1[0]), "+v"(rz1[1]), "+v"(rz1[2]), "+v"(rz1[3]));
    }
    SCHED();
    { u32 lo, hi;
      asm volatile("s_waitcnt vmcnt(5)\n\tv_readfirstlane_b32 %0, v252\n\tv_readfirstlane_b32 %1, v253\n\ts_nop 1" : "=s"(lo), "=s"(hi)::"memory");
      tag = mk_tag(((u64)hi << 32) | lo); tagv = tag; }
    asm volatile("s_waitcnt vmcnt(0)" : "+v"(tagv), "+v"(plr), "+v"(pli), "+v"(pdt), "+v"(pdk)::"memory");
    if (w == 0) st8c(tagv, kvfo, sws);
    PRIO(2);
    {
      const float lr = bf2f(plr), li = bf2f(pli);
      const float dt = bf2f(pk2(expf(bf2f(pdt)), 0.f) & 0xffffu);
      const float e = expf(dt * lr);
      float sn, cs;
      sincosf(dt * li, &sn, &cs);
      ar = e * cs; ai = e * sn;
      const float nr = ar - 1.f, ni = ai;
      const bool sw = __builtin_fabsf(lr) >= __builtin_fabsf(li);
      const float big = sw ? lr : li, sm = sw ? li : lr, rt = sm / big, den = big + sm * rt;
      const float x = sw ? nr : ni, y = sw ? ni : nr;
      gr = 2.f * (x + y * rt) / den;
      gi = 2.f * (y - x * rt) / den;
      gi = sw ? gi : -gi;
      ai = -ai;
      float a8i = ai, a16i, a32r, a32i, a48r, a48i, a64i, aki, api;
      a8r = ar;
#pragma unroll
      for (int i = 0; i < 3; ++i) cmul(a8r, a8i, a8r, a8i, a8r, a8i);
      cmul(a16r, a16i, a8r, a8i, a8r, a8i);
      cmul(a32r, a32i, a16r, a16i, a16r, a16i);
      cmul(a48r, a48i, a32r, a32i, a16r, a16i);
      cmul(a64r, a64i, a32r, a32i, a32r, a32i);
      akr = a64r; aki = a64i;
#pragma unroll
      for (int i = 0; i < 3; ++i) cmul(akr, aki, akr, aki, akr, aki);
      if (g <= 8) { akr = 0.f; aki = 0.f; }
      apr = h ? ((g4 & 1) ? a48r : a32r) : ((g4 & 1) ? a16r : 1.f);
      api = h ? ((g4 & 1) ? a48i : a32i) : ((g4 & 1) ? a16i : 0.f);
      m8 = f32x2{-a8i, a8i}; m16 = f32x2{-a16i, a16i}; m64 = f32x2{-a64i, a64i}; mk = f32x2{-aki, aki}; mp = f32x2{-api, api};
      const u32 dv = pdk << ((r32 & 1) * 16), di = (r32 >> 1) - 4 * h;
#pragma unroll
      for (u32 i = 0; i < 4; ++i) { dg0[i] = di == i ? dv : 0u; dg1[i] = di == 8 + i ? dv : 0u; }
      asm volatile("" : "+v"(ar), "+v"(ai), "+v"(gr), "+v"(gi), "+v"(a8r), "+v"(a16r), "+v"(a64r), "+v"(akr), "+v"(apr),
                   "+v"(m8), "+v"(m16), "+v"(m64), "+v"(mk), "+v"(mp), "+v"(dg0), "+v"(dg1));
      SCHED();
    }
    if (w == 0) poll<2>(rfa, tag, sws);
    BAR();
    bdx(0); SLEEP(2); bdr(0); SLEEP(2); bdi(0); SLEEP(2); bdx(1); SLEEP(2); bdr(1); SLEEP(2); bdi(1); SLEEP(2);
    PRIO(0);
    bf16x8 fx[2][8], fb[2][4];
    auto step = [&](u32 c, bool rd, bool mul) {
      const u32 sl = (c % 3) * 24576, n = c + 2, p = (c - 1) & 1;
#pragma unroll
      for (u32 s = 0; s < 4; ++s) {
        SCHED();
        if (mul) ur = mfma32(fx[p][2 * s], fb[p][s], ur);
        SCHED();
        if (rd) { fb[c & 1][s] = ldsr(rb[s] + sl); fx[c & 1][2 * s] = ldsr(ra[s] + sl); }
        SCHED();
        if (rd && c > 0 && n < 8 && s < 3) bd1(n, s, 0);
        SCHED();
        if (mul) ui = mfma32(fx[p][2 * s + 1], fb[p][s], ui);
        SCHED();
        if (rd) fx[c & 1][2 * s + 1] = ldsr(ra[s] + sl + 4096);
        SCHED();
        if (rd && c > 0 && n < 8 && s < 3) bd1(n, s, 1);
      }
      SCHED();
    };
    {
      const void* p = w == 1 ? (const void*)a.cre : w == 2 ? (const void*)a.cim : (const void*)k0[4];
      if (w) asm volatile("buffer_load_dword v255, %0, %1, %2 offen" ::"v"(wrm), "s"(mk_srd(p, 524288)), "s"((g * 8 + j) * 2048) : "memory", "v255");
    }
    bdx(2); SLEEP(2); bdr(2); SLEEP(2); bdi(2);
    waitvl_bar<12>(); step(0, true, false);
    waitvl_bar<6>(); step(1, true, true);
    waitvl_bar<6>(); step(2, true, true);
    waitvl_bar<6>(); step(3, true, true);
    waitvl_bar<6>(); step(4, true, true);
    waitvl_bar<6>(); step(5, true, true);
    waitvl_bar<6>(); step(6, true, true);
    waitvl_bar<0>(); step(7, true, true);
    waitvl_bar<0>(); step(8, false, true);
  }
  PRIO(2);
  f32x2 er = {0.f, 0.f}, ei = {0.f, 0.f};
  {
#pragma unroll
    for (int i = 0; i < 16; ++i) {
      const float p = ur[i], q = ui[i];
      const auto r = __builtin_amdgcn_permlane16_swap(__builtin_bit_cast(u32, p), __builtin_bit_cast(u32, q), false, false);
      const u32 r0 = r[0], r1 = r[1];
      ur[i] = __builtin_bit_cast(float, r0); ui[i] = __builtin_bit_cast(float, r1);
    }
    const f32x2 g_r = {gr, gr}, g_i = {gi, gi};
#pragma unroll
    for (int m = 0; m < 8; ++m) {
      const f32x2 xr = {ur[2 * m], ur[2 * m + 1]}, xi = {ui[2 * m], ui[2 * m + 1]};
      const f32x2 yr = fma2(g_r, xr, -(g_i * xi)), yi = fma2(-g_r, xi, -(g_i * xr));
      ur[2 * m] = yr.x; ur[2 * m + 1] = yr.y; ui[2 * m] = yi.x; ui[2 * m + 1] = yi.y;
      if (m == 0) { er = yr; ei = yi; } else cstep2(er, ei, ar, ai, yr, yi);
    }
  }
  const f32x2 ea = {er.x, ei.x};
  f32x2 pre16;
  {
    const f32x2 e16 = cst(ea, a8r, m8, f32x2{er.y, ei.y});
    *(volatile LAS u32x2*)(size_t)xw = zero2;
    *(volatile LAS f32x2*)(size_t)(xw + 384) = e16;
    const f32x2 z0 = *(volatile LAS f32x2*)(size_t)xw, z1 = *(volatile LAS f32x2*)(size_t)(xw + 128), z2 = *(volatile LAS f32x2*)(size_t)(xw + 256);
    pre16 = cst(cst(z0, a16r, m16, z1), a16r, m16, z2);
    st8c(__builtin_bit_cast(u32x2, cst(pre16, a16r, m16, e16)), eoff, sws);
  }
  const u32x4 scr = mk_srd(a.cre, 512 * 1024), sci = mk_srd(a.cim, 512 * 1024);
  auto cdma = [&](u32 c) {
    const u32 sl = (c % 5) * 16384 + w * 2048, so = (j * 64 + w * 16) * 1024 + c * 128;
    dma2(sl, vr0, vr1, scr, so);
    dma2(sl + 8192, vr0, vr1, sci, so);
  };
  u32x4 xq0, xq1;
  cdma(0); cdma(1);
  {
    asm volatile("buffer_load_dwordx4 %0, %2, %3, 0 offen\n\tbuffer_load_dwordx4 %1, %2, %3, 0 offen offset:32" : "=&v"(xq0), "=&v"(xq1) : "v"(vq), "s"(sxb) : "memory");
  }
  WAITV(10);
  st8c(tagv, efo, sws);
  asm volatile("s_waitcnt vmcnt(1)" : "+v"(xq0), "+v"(xq1)::"memory");
  SLEEP(16);
  poll<4>(efp, tag, sws);
  {
    const __amdgpu_buffer_rsrc_t re = __builtin_amdgcn_make_buffer_rsrc(ws + WS_E + (j * 4 + w) * 4096, 0, 4096, 0x20000);
    u32x2 ev[8];
#pragma unroll
    for (int m = 0; m < 8; ++m) ev[m] = __builtin_amdgcn_raw_buffer_load_b64(re, evo[m], 0, 16);
    f32x2 c = __builtin_bit_cast(f32x2, ev[0]);
#pragma unroll
    for (int m = 1; m < 8; ++m) c = cst(c, a64r, m64, __builtin_bit_cast(f32x2, ev[m]));
    *(volatile LAS f32x2*)(size_t)xw = c;
    f32x2 ps[4];
#pragma unroll
    for (int s = 0; s < 4; ++s) ps[s] = *(volatile LAS f32x2*)(size_t)(xr_ + s * 128);
    c = cst(cst(cst(ps[3], akr, mk, ps[2]), akr, mk, ps[1]), akr, mk, ps[0]);
    const f32x2 ca = cst(c, apr, mp, pre16), cb = cst(ca, a8r, m8, ea);
    f32x2 hr = {ca.x, cb.x}, hi = {ca.y, cb.y};
#pragma unroll
    for (int m = 0; m < 8; ++m) {
      cstep2(hr, hi, ar, ai, f32x2{ur[2 * m], ur[2 * m + 1]}, f32x2{ui[2 * m], ui[2 * m + 1]});
      ur[2 * m] = hr.x; ur[2 * m + 1] = hr.y; ui[2 * m] = hi.x; ui[2 * m + 1] = hi.y;
    }
#pragma unroll
    for (u32 i = 0; i < 2; ++i) {
      u32x4 pr, pi;
#pragma unroll
      for (int t = 0; t < 4; ++t) { pr[t] = pk2(ur[4 * t + i], ur[4 * t + 2 + i]); pi[t] = pk2(ui[4 * t + i], ui[4 * t + 2 + i]); }
      *(LAS u32x4*)(size_t)(i ? hw1 : hw0) = pr;
      *(LAS u32x4*)(size_t)((i ? hw1 : hw0) + 1024) = pi;
    }
    asm volatile("" ::: "memory");
    bf16x8 ht[4];
#pragma unroll
    for (u32 f = 0; f < 4; ++f) ht[f] = ldstr(tb0 + f * 1024, tb1 + f * 1024);
#pragma unroll
    for (u32 f = 0; f < 4; ++f) st16s(__builtin_bit_cast(u32x4, ht[f]), l16, sws, WS_H + g * 131072 + f * 32768 + (j * 4 + w) * 1024);
    cdma(2); cdma(3);
    WAITV(8);
    st8c(tagv, hfo, sws);
  }
  f32x16 acc[2];
  {
    const f32x16 z = {0, 0, 0, 0, 0, 0, 0, 0, 0, 0, 0, 0, 0, 0, 0, 0};
    acc[0] = mfma32(__builtin_bit_cast(bf16x8, xq0), __builtin_bit_cast(bf16x8, dg0), z);
    acc[0] = mfma32(__builtin_bit_cast(bf16x8, xq1), __builtin_bit_cast(bf16x8, dg1), acc[0]);
    asm volatile("" : "+v"(acc[0]));
  }
  SLEEP(16);
  if (w == 0) poll<4>(hfp, tag, sws);
  waitvl_bar<0>();
  PRIO(0);
  cdma(4);
  bf16x8 hf[32];
  {
    const __amdgpu_buffer_rsrc_t rh = __builtin_amdgcn_make_buffer_rsrc(ws + WS_H + g * 131072 + th * 65536 + pt * 32768, 0, 32768, 0x20000);
#pragma unroll
    for (u32 k = 0; k < 32; ++k) {
      hf[k] = __builtin_bit_cast(bf16x8, __builtin_amdgcn_raw_buffer_load_b128(rh, l16 + (k & 3) * 1024, (k >> 2) * 4096, 16));
      if (k & 1) { SCHED(); SLEEP(2); SCHED(); }
    }
    SCHED();
  }
  {
    bf16x8 fc[8];
    auto ks = [](u32 i) { return i < 56 ? i >> 1 : 28 + (i & 3); };
    auto ct = [](u32 i) { return i < 56 ? (~i & 1) : (i < 60); };
    auto cread = [&](u32 i) {
      const u32 o = ((ks(i) >> 2) % 5) * 16384, s = ks(i) & 3;
      fc[i & 7] = o ? ldsr((ct(i) ? rc1[s] : rc0[s]) + (o - 16384)) : ldsr(ct(i) ? rz1[s] : rz0[s]);
    };
    auto crun = [&](u32 i0, u32 i1) {
      const f32x16 z = {0, 0, 0, 0, 0, 0, 0, 0, 0, 0, 0, 0, 0, 0, 0, 0};
#pragma unroll
      for (u32 i = i0; i < i1; ++i) {
        SCHED();
        if (ct(i)) acc[1] = mfma32(hf[ks(i)], fc[i & 7], i ? acc[1] : z); else acc[0] = mfma32(hf[ks(i)], fc[i & 7], acc[0]);
        SCHED();
        if (i + 6 < 64) cread(i + 6);
        if (i == 44) BAR();
        if (i >= 62) {
#pragma unroll
          for (u32 q = 2 * (i - 62); q < 2 * (i - 61); ++q) *(LAS f32x4*)(size_t)(rxw + q * 1024) = f32x4{acc[1][4 * q], acc[1][4 * q + 1], acc[1][4 * q + 2], acc[1][4 * q + 3]};
        }
      }
      SCHED();
    };
#pragma unroll
    for (u32 i = 0; i < 6; ++i) cread(i);
    crun(0, 18);
    asm volatile("" : "+v"(hf[31]));
    waitvl_bar<0>();
    cdma(5); cdma(6); cdma(7);
    if (w == 0) { PRIO(2); poll<4>(tfa, tag, sws); }
    if (g == 31 && j == 0 && w == 0) st8c(tag, fll + WS_EP, sws);
    PRIO(0);
    waitvl_bar<0>();
    crun(18, 64);
  }
  PRIO(2);
  waitvl_bar<63>();
  {
    const __amdgpu_buffer_rsrc_t ro = __builtin_amdgcn_make_buffer_rsrc(a.out, 0, 8388608, 0x20000);
#pragma unroll
    for (int i = 0; i < 4; ++i) {
      const f32x4 p = *(LAS f32x4*)(size_t)(rxr + i * 1024);
#pragma unroll
      for (int e = 0; e < 4; ++e) { const float x = acc[0][4 * i + e] + p[e]; __builtin_amdgcn_raw_buffer_store_b32(__builtin_bit_cast(u32, x), ro, vout, (e + 8 * i) * 4096, WT); }
    }
  }
}

__global__ void __launch_bounds__(256, 2) fused_kernel(Args) {
  __shared__ __attribute__((aligned(16))) char lds[81920];
  asm volatile("" ::"v"((u32)(size_t)(LAS char*)lds));
  const u32 lane = threadIdx.x & 63, w = __builtin_amdgcn_readfirstlane(threadIdx.x >> 6), b = blockIdx.x;
  if (b >= 256) role_a(b - 256, w, lane); else role_b(b, w, lane);
}

extern "C" void kernel_launch(void* const* d_in, const int* in_sizes, int n_in, void* d_out, int out_size, void* d_ws,
                              size_t ws_size, hipStream_t stream) {
  Args a;
  a.xa = (const u16*)d_in[0]; a.wq = (const u16*)d_in[1]; a.wk = (const u16*)d_in[2]; a.wv = (const u16*)d_in[3];
  a.wo = (const u16*)d_in[4]; a.xb = (const u16*)d_in[5]; a.lre = (const u16*)d_in[6]; a.lim = (const u16*)d_in[7];
  a.bre = (const u16*)d_in[8]; a.bim = (const u16*)d_in[9]; a.cre = (const u16*)d_in[10]; a.cim = (const u16*)d_in[11];
  a.ldt = (const u16*)d_in[12]; a.dsk = (const u16*)d_in[13];
  a.out = (float*)d_out; a.ws = (char*)d_ws;
  fused_kernel<<<512, 256, 0, stream>>>(a);
}
```
